```python
import torch

def _softmax(x, axis=-1):
    return torch.softmax(x, dim=axis)
def _gelu(x):
    return torch.nn.functional.gelu(x, approximate="tanh")
def _silu(x):
    return torch.nn.functional.silu(x)
def _sigmoid(x):
    return torch.sigmoid(x)
def _layernorm(x, axis=-1, eps=1e-5):
    m = x.mean(dim=axis, keepdim=True)
    v = x.var(dim=axis, keepdim=True, unbiased=False)
    return (x - m) / torch.sqrt(v + eps)
def _scatter_add(tgt, idx, src, axis=0):
    return tgt.clone().index_add_(axis, idx, src)
def _segment_sum(src, seg, n):
    out = torch.zeros((n,) + src.shape[1:], dtype=src.dtype)
    return out.index_add_(0, seg, src)
def _pad(x, pw):
    flat = tuple(v for pair in reversed(pw) for v in pair)
    return torch.nn.functional.pad(x, flat)

kernel_name = 'gated_mix__grouped_kv_mqa__glu_gated_geglu__prompt'

def setup_inputs():

    _w = (lambda a: (lambda t: t.to(torch.bfloat16) if t.is_floating_point() else t)(torch.as_tensor(a)))
    def _A_setup():
        B, S, D, H = 1, 2048, 576, 32
        Dh = D // H
        return {
            "x": _w((lambda *s: torch.randn(*s, dtype=torch.bfloat16) * 0.05)(B, S, D)),
            "wq": _w((lambda *s: torch.randn(*s, dtype=torch.bfloat16) * 0.8)(D, H * Dh)),
            "wk": _w((lambda *s: torch.randn(*s, dtype=torch.bfloat16) * 0.8)(D, Dh)),
            "wv": _w((lambda *s: torch.randn(*s, dtype=torch.bfloat16) * 0.05)(D, Dh)),
            "wo": _w((lambda *s: torch.randn(*s, dtype=torch.bfloat16) * 0.05)(H * Dh, D)),
        }
    _A_in = _A_setup()

    def _B_setup():
        B, S, Dm, Df = 1, 2048, 576, 4608
        return {
            "x": _w((lambda *s: torch.randn(*s, dtype=torch.bfloat16) * 0.05)(B, S, Dm)),
            "w_gate": _w((lambda *s: torch.randn(*s, dtype=torch.bfloat16) * 0.8)(Dm, Df) * (Dm ** -0.5)),
            "w_up": _w((lambda *s: torch.randn(*s, dtype=torch.bfloat16) * 0.8)(Dm, Df) * (Dm ** -0.5)),
            "w_down": _w((lambda *s: torch.randn(*s, dtype=torch.bfloat16) * 0.8)(Df, Dm) * (Df ** -0.5)),
        }
    _B_in = _B_setup()

    return {
        "A_x": _A_in["x"],
        "A_wq": _A_in["wq"],
        "A_wk": _A_in["wk"],
        "A_wv": _A_in["wv"],
        "A_wo": _A_in["wo"],
        "B_x": _B_in["x"],
        "B_w_gate": _B_in["w_gate"],
        "B_w_up": _B_in["w_up"],
        "B_w_down": _B_in["w_down"],
        "gate_w": _w((lambda *s: torch.randn(*s, dtype=torch.bfloat16) * 0.05)(576) * 16.0),
    }

def reference(A_x, A_wq, A_wk, A_wv, A_wo, B_x, B_w_gate, B_w_up, B_w_down, gate_w):
    def _A_ref(x, wq, wk, wv, wo):
        B, S, D = x.shape
        H = 32
        Dh = D // H
        q = (lambda x, *d: x.permute(*d))((x @ wq).reshape(B, S, H, Dh), 0, 2, 1, 3)
        k = x @ wk
        v = x @ wv
        scores = torch.einsum("bhqd,bkd->bhqk", q, k) * (Dh ** -0.5)
        p = _softmax(scores, axis=-1)
        o = torch.einsum("bhqk,bkd->bhqd", p, v)
        return (lambda x, *d: x.permute(*d))(o, 0, 2, 1, 3).reshape(B, S, H * Dh) @ wo

    def _B_ref(x, w_gate, w_up, w_down):
        g = torch.einsum("bsd,df->bsf", x, w_gate)
        u = torch.einsum("bsd,df->bsf", x, w_up)
        h = _gelu(g) * u
        return torch.einsum("bsf,fd->bsd", h, w_down)

    x = A_x
    g = _sigmoid((x * gate_w).sum(-1, keepdims=True))
    a = _A_ref(x, A_wq, A_wk, A_wv, A_wo)
    b = _B_ref(x, B_w_gate, B_w_up, B_w_down)
    return g * a + (1 - g) * b
```

```cpp
#include <hip/hip_runtime.h>
#include <hip/hip_bf16.h>
typedef unsigned short u16;
__device__ inline float b2f(u16 x){ return __uint_as_float(((unsigned)x)<<16); }
__device__ inline u16 f2b(float f){ __hip_bfloat16 b = __float2bfloat16(f); return *(u16*)&b; }
constexpr int S=2048, D=576, H=32, DH=18, F=4608;
__global__ void gemm(const u16* A, const u16* B, float* C, int M, int N, int K){
  __shared__ float a[16][17], b[16][17];
  int tx=threadIdx.x&15, ty=threadIdx.x>>4; int row=blockIdx.y*16+ty, col=blockIdx.x*16+tx; float acc=0;
  for(int k=0;k<K;k+=16){ a[ty][tx]=b2f(A[(size_t)row*K+k+tx]); b[ty][tx]= col<N? b2f(B[(size_t)(k+ty)*N+col]):0.f; __syncthreads();
    for(int j=0;j<16;j++) acc+=a[ty][j]*b[j][tx]; __syncthreads(); }
  if(col<N) C[(size_t)row*N+col]=acc;
}
__global__ void attn(const float* q, const float* k, const float* v, u16* o){
  int idx=blockIdx.x*blockDim.x+threadIdx.x; int t=idx/H, h=idx%H; float qq[DH], acc[DH];
  #pragma unroll
  for(int d=0;d<DH;d++){ qq[d]=q[t*D+h*DH+d]*0.23570226f; acc[d]=0; }
  float m=-1e30f,l=0;
  for(int j=0;j<S;j++){ float s=0;
    #pragma unroll
    for(int d=0;d<DH;d++) s+=qq[d]*k[j*DH+d];
    float mn=fmaxf(m,s), al=__expf(m-mn), p=__expf(s-mn); l=l*al+p;
    #pragma unroll
    for(int d=0;d<DH;d++) acc[d]=acc[d]*al+p*v[j*DH+d];
    m=mn; }
  #pragma unroll
  for(int d=0;d<DH;d++) o[t*D+h*DH+d]=f2b(acc[d]/l);
}
__global__ void glu(const float* g, const float* u, u16* h){ size_t i=(size_t)blockIdx.x*256+threadIdx.x; float x=g[i];
  float t=tanhf(0.7978845608f*(x+0.044715f*x*x*x)); h[i]=f2b(0.5f*x*(1.f+t)*u[i]); }
__global__ void fin(const u16* x, const u16* gw, const float* a, const float* b, u16* out){
  int t=blockIdx.x; __shared__ float red[64]; float s=0; for(int d=threadIdx.x;d<D;d+=64) s+=b2f(x[t*D+d])*b2f(gw[d]);
  red[threadIdx.x]=s; __syncthreads(); float tot=0; for(int i=0;i<64;i++) tot+=red[i]; float g=1.f/(1.f+__expf(-tot));
  for(int d=threadIdx.x;d<D;d+=64) out[t*D+d]=f2b(g*a[t*D+d]+(1.f-g)*b[t*D+d]);
}
extern "C" void kernel_launch(void* const* d_in, const int* in_sizes, int n_in, void* d_out, int out_size, void* d_ws, size_t ws_size, hipStream_t stream){
  const u16* x=(const u16*)d_in[0]; const u16* wq=(const u16*)d_in[1]; const u16* wk=(const u16*)d_in[2]; const u16* wv=(const u16*)d_in[3]; const u16* wo=(const u16*)d_in[4];
  const u16* wg=(const u16*)d_in[6]; const u16* wu=(const u16*)d_in[7]; const u16* wd=(const u16*)d_in[8]; const u16* gw=(const u16*)d_in[9];
  float* ws=(float*)d_ws; float* q=ws; float* k=q+S*D; float* v=k+S*32; float* a=v+S*32; float* b=a+S*D; float* g=b+S*D; float* u=g+(size_t)S*F; u16* o=(u16*)(u+(size_t)S*F); u16* h=o+S*D;
  gemm<<<dim3(D/16,S/16),256,0,stream>>>(x,wq,q,S,D,D);
  gemm<<<dim3(2,S/16),256,0,stream>>>(x,wk,k,S,DH,D);
  gemm<<<dim3(2,S/16),256,0,stream>>>(x,wv,v,S,DH,D);
  attn<<<S*H/64,64,0,stream>>>(q,k,v,o);
  gemm<<<dim3(D/16,S/16),256,0,stream>>>(o,wo,a,S,D,D);
  gemm<<<dim3(F/16,S/16),256,0,stream>>>(x,wg,g,S,F,D);
  gemm<<<dim3(F/16,S/16),256,0,stream>>>(x,wu,u,S,F,D);
  glu<<<S*F/256,256,0,stream>>>(g,u,h);
  gemm<<<dim3(D/16,S/16),256,0,stream>>>(h,wd,b,S,D,F);
  fin<<<S,64,0,stream>>>(x,gw,a,b,(u16*)d_out);
}
```

```cpp
#include <hip/hip_runtime.h>
typedef unsigned u32;
typedef unsigned long long u64;
typedef u32 u32x16 __attribute__((ext_vector_type(16)));
#define RFL(x) __builtin_amdgcn_readfirstlane((u32)(x))
#define SRD(v, o, p, n) do { u64 a_ = (u64)(p); v[o] = RFL((u32)a_); v[o + 1] = RFL((u32)(a_ >> 32) & 0xffffu); v[o + 2] = (n); v[o + 3] = 0x20000u; } while (0)

__global__ void __launch_bounds__(256, 1)
fused_mix(const char* __restrict__ x, const char* __restrict__ wq, const char* __restrict__ wk, const char* __restrict__ wv,
          const char* __restrict__ wo, const char* __restrict__ wg, const char* __restrict__ wu, const char* __restrict__ wd,
          const char* __restrict__ gw, char* out, char* ws) {
  __shared__ __attribute__((aligned(1024))) char smem[163840];
  const u32 w = RFL(threadIdx.x >> 6), bid = blockIdx.x;
  const u32 xc = bid & 7u, rr = bid >> 3, tile = 8u * (xc & 1u) + (rr & 7u), j = 4u * (xc >> 1) + (rr >> 3), odd = j & 1u;
  u32x16 s0, s1, s2;
  const u32 F0 = 288u * j + 32u * odd, FH = 288u * j + (odd ? 0u : 256u);
  SRD(s0, 0, (w < 2u ? wg : wu) + 2u * F0, 576u * 9216u - 2u * F0);
  SRD(s0, 4, x + tile * 147456u, 147456u);
  SRD(s0, 8, wd + (u64)j * 331776u - 2048, 331776u + 2048u);
  SRD(s0, 12, ws, 67108864u);
  SRD(s1, 0, wo, 663552u);
  SRD(s1, 4, out, 2359296u);
  SRD(s1, 8, gw, 1152u);
  s1[12] = w; s1[13] = tile; s1[14] = j; s1[15] = bid;
  {
    const u32 jm = j < 3u ? j : j - 3u;
    const char* q0 = wq + 72u * j;
    const char* kv = j < 6u ? (j < 3u ? wk : wv) + (jm == 2u ? 20u : 16u * jm) - 56 : q0;
    const u64 u0 = (u64)(w < 2u ? wg + 2u * FH : q0), u1 = (u64)(w < 2u ? wu + 2u * FH : q0);
    const u64 u3 = w < 2u ? u1 : (u64)(gw - 20), u7 = w < 2u ? u1 : (u64)kv;
    s2[0] = (u32)u0; s2[1] = (u32)(u0 >> 32); s2[2] = (u32)u1; s2[3] = (u32)(u1 >> 32);
    s2[4] = (u32)u3; s2[5] = (u32)(u3 >> 32); s2[6] = (u32)u7; s2[7] = (u32)(u7 >> 32);
    s2[8] = s2[9] = 0u;
  }
  s2[10] = (u32)(size_t)smem; s2[11] = s2[12] = s2[13] = s2[14] = s2[15] = 0u;
#pragma unroll
  for (int i = 0; i < 16; ++i) { s0[i] = RFL(s0[i]); s1[i] = RFL(s1[i]); s2[i] = RFL(s2[i]); }
  asm volatile(
".macro MA a,b,c\nv_mfma_f32_16x16x32_bf16 v[\\a:\\a+3], v[\\b:\\b+3], a[\\c:\\c+3], v[\\a:\\a+3]\n.endm\n.macro TR a,b,c\n"
"ds_read_b64_tr_b16 v[\\a:\\a+1], v[\\b] offset:\\c\n.endm\n.macro EX a\nv_exp_f32 v[\\a], v[\\a]\n.endm\n.macro MU a,b,c\n"
"v_mul_f32 v[\\a], v[\\b], v[\\c]\n.endm\nv_mbcnt_lo_u32_b32 v100, -1, 0\nv_mbcnt_hi_u32_b32 v100, -1, v100\ns_lshr_b32 s31, s67, 3\n"
"s_lshl2_add_u32 s31, s31, s64\ns_lshl_b32 s31, s31, 7\nv_mov_b32 v184, s31\ns_getpc_b64 s[22:23]\nL_pc0:\nv_add_u32 v72, 0, v184\n"
"v_min_u32 v72, L_end-L_pc0-4, v72\nglobal_load_dword v253, v72, s[22:23]\ns_lshl_b32 s4, s64, 12\ns_lshr_b32 s0, s67, 6\ns_lshr_b32 s31, s67, 3\n"
"s_and_b32 s31, s31, 7\ns_lshl1_add_u32 s0, s0, s31\ns_lshl_b32 s0, s0, 7\ns_cmp_ge_u32 s0, 0x480\ns_cselect_b32 s28, 0x480, 0\ns_sub_u32 s0, s0, s28\n"
"s_add_u32 s1, s0, 0x80\ns_cmp_eq_u32 s1, 0x480\ns_cselect_b32 s1, 0, s1\nv_lshrrev_b32 v101, 3, v100\nv_and_b32 v102, 7, v100\n"
"v_lshrrev_b32 v103, 4, v100\nv_mul_u32_u24 v104, 0x2400, v101\ns_and_b32 s31, s64, 1\ns_mul_i32 s31, s31, 0x48000\nv_add_u32 v104, s31, v104\n"
"v_and_b32 v95, 1, v103\nv_lshlrev_b32 v95, 1, v95\nv_xor_b32 v105, v95, v102\nv_lshlrev_b32 v105, 4, v105\nv_add_u32 v188, v105, v104\n"
"v_xor_b32 v95, 64, v105\nv_add_u32 v189, v95, v104\nv_add_u32 v189, 0x11c00, v189\nv_add_u32 v190, 0x23800, v188\nv_add_u32 v191, 0x23800, v189\n"
"s_add_u32 m0, s4, 0\ns_mul_i32 s86, s0, 0x1200\nbuffer_load_dwordx4 v188, s[36:39], s86 offen lds\n"
"buffer_load_dwordx4 v189, s[36:39], s86 offen offset:1024 lds\nbuffer_load_dwordx4 v190, s[36:39], s86 offen offset:2048 lds\n"
"buffer_load_dwordx4 v191, s[36:39], s86 offen offset:3072 lds\nv_mul_u32_u24 v104, 0x480, v101\ns_mul_i32 s31, s64, 0x9000\n"
"v_add_u32 v104, s31, v104\nv_xor_b32 v105, v103, v102\nv_lshlrev_b32 v105, 4, v105\nv_add_u32 v192, v105, v104\nv_xor_b32 v95, 64, v105\n"
"v_add_u32 v193, v95, v104\nv_add_u32 v193, 0x2000, v193\nv_add_u32 v194, 0x4000, v192\nv_add_u32 v195, 0x4000, v193\ns_add_u32 m0, s4, 0x16000\n"
"s_nop 0\nbuffer_load_dwordx4 v192, s[40:43], s0 offen lds\nbuffer_load_dwordx4 v193, s[40:43], s0 offen offset:1024 lds\n"
"buffer_load_dwordx4 v194, s[40:43], s0 offen offset:2048 lds\nbuffer_load_dwordx4 v195, s[40:43], s0 offen offset:3072 lds\ns_add_u32 m0, s4, 0x4000\n"
"s_mul_i32 s86, s1, 0x1200\nbuffer_load_dwordx4 v188, s[36:39], s86 offen lds\nbuffer_load_dwordx4 v189, s[36:39], s86 offen offset:1024 lds\n"
"buffer_load_dwordx4 v190, s[36:39], s86 offen offset:2048 lds\nbuffer_load_dwordx4 v191, s[36:39], s86 offen offset:3072 lds\n"
"s_add_u32 m0, s4, 0x1a000\ns_nop 0\nbuffer_load_dwordx4 v192, s[40:43], s1 offen lds\nbuffer_load_dwordx4 v193, s[40:43], s1 offen offset:1024 lds\n"
"buffer_load_dwordx4 v194, s[40:43], s1 offen offset:2048 lds\nbuffer_load_dwordx4 v195, s[40:43], s1 offen offset:3072 lds\ns_add_u32 s2, s1, 0x80\n"
"s_cmp_eq_u32 s2, 0x480\ns_cselect_b32 s2, 0, s2\ns_add_u32 s3, s2, 0x80\ns_cmp_eq_u32 s3, 0x480\ns_cselect_b32 s3, 0, s3\ns_add_u32 s78, s3, 0x80\n"
"s_cmp_eq_u32 s78, 0x480\ns_cselect_b32 s78, 0, s78\ns_add_u32 s79, s78, 0x80\ns_cmp_eq_u32 s79, 0x480\ns_cselect_b32 s79, 0, s79\n"
"s_add_u32 s80, s79, 0x80\ns_cmp_eq_u32 s80, 0x480\ns_cselect_b32 s80, 0, s80\ns_add_u32 s81, s80, 0x80\ns_cmp_eq_u32 s81, 0x480\n"
"s_cselect_b32 s81, 0, s81\ns_add_u32 s19, s81, 0x80\ns_cmp_eq_u32 s19, 0x480\ns_cselect_b32 s19, 0, s19\ns_lshr_b32 s90, s64, 1\n"
"s_and_b32 s91, s64, 1\ns_and_b32 s92, s66, 1\ns_lshr_b32 s93, s67, 3\ns_lshr_b32 s94, s66, 1\ns_lshl1_add_u32 s95, s66, s91\n"
"v_and_b32 v106, 15, v100\nv_bfe_u32 v107, v100, 2, 2\nv_and_b32 v108, 3, v100\nv_lshlrev_b32 v108, 3, v108\nv_and_b32 v109, 1, v103\n"
"v_lshrrev_b32 v95, 1, v107\nv_lshl_or_b32 v109, v109, 1, v95\nv_lshlrev_b32 v220, 4, v100\ns_lshl_b32 s6, s64, 10\ns_lshl_b32 s31, s91, 1\n"
"v_xor_b32 v110, s31, v109\nv_lshl_add_u32 v110, v110, 5, v108\nv_lshl_add_u32 v196, v103, 3, v107\nv_lshl_add_u32 v196, v196, 7, v110\n"
"v_add_u32 v198, 0x10000, v196\nv_lshrrev_b32 v95, 1, v106\nv_xor_b32 v204, v95, v103\nv_lshlrev_b32 v204, 4, v204\n"
"v_lshl_add_u32 v204, v106, 7, v204\ns_lshl_b32 s31, s90, 13\ns_add_u32 s31, s31, 0x16000\nv_add_u32 v204, s31, v204\ns_lshl1_add_u32 s31, s91, 1\n"
"v_xor_b32 v111, s31, v109\nv_lshl_add_u32 v111, v111, 5, v108\nv_lshl_add_u32 v197, v103, 3, v107\nv_lshl_add_u32 v197, v197, 7, v111\n"
"v_add_u32 v199, 0x10000, v197\nv_add_u32 v205, 4, v103\nv_lshrrev_b32 v95, 1, v106\nv_xor_b32 v205, v95, v205\nv_lshlrev_b32 v205, 4, v205\n"
"v_lshl_add_u32 v205, v106, 7, v205\ns_lshl_b32 s31, s90, 13\ns_add_u32 s31, s31, 0x16000\nv_add_u32 v205, s31, v205\nv_cmp_lt_u32 vcc, 0, v100\n"
"v_mov_b32 v248, 0x40000000\ns_nop 0\nv_cndmask_b32 v248, 0, v248, vcc\nbuffer_load_dwordx2 v[70:71], v248, s[48:51], 0 offen sc1\ns_waitcnt vmcnt(9)\n"
"s_barrier\nds_read_b128 a[0:3], v204\nds_read_b128 a[4:7], v204 offset:2048\nds_read_b128 a[8:11], v204 offset:4096\n"
"ds_read_b128 a[12:15], v204 offset:6144\nds_read_b64_tr_b16 v[160:161], v196\nTR 162,196,512\nTR 164,196,8192\nTR 166,196,8704\n"
"ds_read_b128 a[16:19], v205\nds_read_b128 a[20:23], v205 offset:2048\nds_read_b128 a[24:27], v205 offset:4096\n"
"ds_read_b128 a[28:31], v205 offset:6144\ns_waitcnt lgkmcnt(6)\nv_mfma_f32_16x16x32_bf16 v[0:3], v[160:163], a[0:3], 0\n"
"ds_read_b64_tr_b16 v[168:169], v197\nTR 170,197,512\nv_mfma_f32_16x16x32_bf16 v[4:7], v[160:163], a[4:7], 0\ns_add_u32 m0, s4, 0x8000\n"
"s_mul_i32 s86, s2, 0x1200\nbuffer_load_dwordx4 v188, s[36:39], s86 offen lds\nv_mfma_f32_16x16x32_bf16 v[8:11], v[160:163], a[8:11], 0\n"
"v_lshrrev_b32 v114, 5, v100\nv_and_b32 v115, 1, v103\nv_mfma_f32_16x16x32_bf16 v[12:15], v[160:163], a[12:15], 0\n"
"buffer_load_dwordx4 v189, s[36:39], s86 offen offset:1024 lds\nv_and_b32 v116, 31, v100\ns_waitcnt lgkmcnt(6)\n"
"v_mfma_f32_16x16x32_bf16 v[16:19], v[164:167], a[0:3], 0\nTR 172,197,8192\nTR 174,197,8704\nv_mfma_f32_16x16x32_bf16 v[20:23], v[164:167], a[4:7], 0\n"
"buffer_load_dwordx4 v190, s[36:39], s86 offen offset:2048 lds\ns_lshl_b32 s82, s90, 2\nv_mfma_f32_16x16x32_bf16 v[24:27], v[164:167], a[8:11], 0\n"
"s_lshl3_add_u32 s82, s65, s82\ns_lshl_b32 s82, s82, 4\nv_mfma_f32_16x16x32_bf16 v[28:31], v[164:167], a[12:15], 0\n"
"buffer_load_dwordx4 v191, s[36:39], s86 offen offset:3072 lds\nv_lshlrev_b32 v223, 8, v114\ns_waitcnt lgkmcnt(2)\n"
"v_mfma_f32_16x16x32_bf16 v[32:35], v[168:171], a[0:3], 0\nTR 160,196,4096\nTR 162,196,4608\nv_mfma_f32_16x16x32_bf16 v[36:39], v[168:171], a[4:7], 0\n"
"s_add_u32 m0, s4, 0x1e000\ns_nop 0\nbuffer_load_dwordx4 v192, s[40:43], s2 offen lds\nv_mfma_f32_16x16x32_bf16 v[40:43], v[168:171], a[8:11], 0\n"
"v_lshl_add_u32 v223, v115, 14, v223\nv_lshl_add_u32 v223, v106, 4, v223\nv_mfma_f32_16x16x32_bf16 v[44:47], v[168:171], a[12:15], 0\n"
"buffer_load_dwordx4 v193, s[40:43], s2 offen offset:1024 lds\ns_add_u32 s31, s82, s66\ns_waitcnt lgkmcnt(2)\n"
"v_mfma_f32_16x16x32_bf16 v[48:51], v[172:175], a[0:3], 0\nTR 164,196,12288\nTR 166,196,12800\n"
"v_mfma_f32_16x16x32_bf16 v[52:55], v[172:175], a[4:7], 0\nbuffer_load_dwordx4 v194, s[40:43], s2 offen offset:2048 lds\ns_lshl_b32 s31, s31, 10\n"
"v_mfma_f32_16x16x32_bf16 v[56:59], v[172:175], a[8:11], 0\ns_lshl_b32 s30, s91, 9\ns_add_u32 s31, s31, s30\n"
"v_mfma_f32_16x16x32_bf16 v[60:63], v[172:175], a[12:15], 0\nbuffer_load_dwordx4 v195, s[40:43], s2 offen offset:3072 lds\n"
"s_add_u32 s31, s31, 0x40000\ns_waitcnt lgkmcnt(2)\nMA 0,160,16\nTR 168,197,4096\nTR 170,197,4608\nTR 172,197,12288\nTR 174,197,12800\nMA 4,160,20\n"
"s_add_u32 m0, s4, 0xc000\ns_mul_i32 s86, s3, 0x1200\nbuffer_load_dwordx4 v188, s[36:39], s86 offen lds\nMA 8,160,24\nv_add_u32 v223, s31, v223\n"
"s_lshl_b32 s31, s65, 5\nMA 12,160,28\nbuffer_load_dwordx4 v189, s[36:39], s86 offen offset:1024 lds\ns_add_u32 s31, s31, s95\ns_waitcnt lgkmcnt(4)\n"
"MA 16,164,16\ns_lshl_b32 s31, s31, 9\ns_lshl_b32 s30, s90, 8\nMA 20,164,20\nbuffer_load_dwordx4 v190, s[36:39], s86 offen offset:2048 lds\n"
"s_add_u32 s31, s31, s30\nMA 24,164,24\ns_add_u32 s31, s31, 0x280000\nv_lshl_add_u32 v224, v100, 2, s31\nMA 28,164,28\n"
"buffer_load_dwordx4 v191, s[36:39], s86 offen offset:3072 lds\ns_lshl3_add_u32 s8, s67, 0x100\ns_waitcnt vmcnt(13) lgkmcnt(0)\ns_barrier\n"
"MA 32,168,16\nds_read_b128 a[32:35], v204 offset:16384\nds_read_b128 a[36:39], v204 offset:18432\nds_read_b128 a[40:43], v204 offset:20480\n"
"ds_read_b128 a[44:47], v204 offset:22528\nTR 160,196,16384\nTR 162,196,16896\nMA 36,168,20\ns_add_u32 m0, s4, 0x22000\ns_nop 0\n"
"buffer_load_dwordx4 v192, s[40:43], s3 offen lds\nMA 40,168,24\ns_cmp_eq_u32 s64, 0\ns_cselect_b32 s14, 1, 0\nMA 44,168,28\n"
"buffer_load_dwordx4 v193, s[40:43], s3 offen offset:1024 lds\nv_mov_b32 v252, 0xbdd2d3e8\nMA 48,172,16\nTR 164,196,24576\nTR 166,196,25088\n"
"ds_read_b128 a[48:51], v205 offset:16384\nds_read_b128 a[52:55], v205 offset:18432\nds_read_b128 a[56:59], v205 offset:20480\n"
"ds_read_b128 a[60:63], v205 offset:22528\nMA 52,172,20\nbuffer_load_dwordx4 v194, s[40:43], s3 offen offset:2048 lds\ns_mul_i32 s99, s90, 0x9000\n"
"MA 56,172,24\ns_add_u32 s99, s99, 0x16000\ns_lshl_b32 s31, s91, 12\nMA 60,172,28\nbuffer_load_dwordx4 v195, s[40:43], s3 offen offset:3072 lds\n"
"s_add_u32 s31, s99, s31\ns_waitcnt lgkmcnt(6)\nMA 0,160,32\nTR 168,197,16384\nTR 170,197,16896\nMA 4,160,36\ns_add_u32 m0, s4, 0x10000\n"
"s_mul_i32 s86, s78, 0x1200\nbuffer_load_dwordx4 v188, s[36:39], s86 offen lds\nMA 8,160,40\nv_add_u32 v206, s31, v220\ns_lshl3_add_u32 s31, s91, s99\n"
"MA 12,160,44\nbuffer_load_dwordx4 v189, s[36:39], s86 offen offset:1024 lds\ns_add_u32 s31, s31, 0x8000\ns_waitcnt lgkmcnt(6)\nMA 16,164,32\n"
"TR 172,197,24576\nTR 174,197,25088\nMA 20,164,36\nbuffer_load_dwordx4 v190, s[36:39], s86 offen offset:2048 lds\nv_add_u32 v207, s31, v220\n"
"MA 24,164,40\nv_add_u32 v208, s99, v220\nv_lshl_add_u32 v200, v103, 2, v107\nMA 28,164,44\n"
"buffer_load_dwordx4 v191, s[36:39], s86 offen offset:3072 lds\nv_lshl_add_u32 v200, v200, 7, v110\ns_waitcnt lgkmcnt(2)\nMA 32,168,32\n"
"TR 160,196,20480\nTR 162,196,20992\nMA 36,168,36\ns_add_u32 m0, s4, 0x16000\ns_nop 0\nbuffer_load_dwordx4 v192, s[40:43], s78 offen lds\n"
"MA 40,168,40\nv_add_u32 v202, 0x10000, v200\nv_lshl_add_u32 v201, v103, 2, v107\nMA 44,168,44\n"
"buffer_load_dwordx4 v193, s[40:43], s78 offen offset:1024 lds\nv_lshl_add_u32 v201, v201, 7, v111\ns_waitcnt lgkmcnt(2)\nMA 48,172,32\n"
"TR 164,196,28672\nTR 166,196,29184\nMA 52,172,36\nbuffer_load_dwordx4 v194, s[40:43], s78 offen offset:2048 lds\nv_add_u32 v203, 0x10000, v201\n"
"MA 56,172,40\ns_lshl4_add_u32 s31, s65, s66\ns_lshl_b32 s30, s90, 2\nMA 60,172,44\nbuffer_load_dwordx4 v195, s[40:43], s78 offen offset:3072 lds\n"
"s_lshl3_add_u32 s31, s31, s30\ns_waitcnt lgkmcnt(2)\nMA 0,160,48\nTR 168,197,20480\nTR 170,197,20992\nTR 172,197,28672\nTR 174,197,29184\n"
"MA 4,160,52\ns_add_u32 m0, s4, 0\ns_mul_i32 s86, s79, 0x1200\nbuffer_load_dwordx4 v188, s[36:39], s86 offen lds\nMA 8,160,56\ns_mul_i32 s31, s31, 18\n"
"s_add_u32 s31, s31, s91\nMA 12,160,60\nbuffer_load_dwordx4 v189, s[36:39], s86 offen offset:1024 lds\ns_lshl_b32 s31, s31, 10\ns_waitcnt lgkmcnt(4)\n"
"MA 16,164,48\ns_add_u32 s31, s31, 0x400000\nv_add_u32 v209, s31, v220\nMA 20,164,52\nbuffer_load_dwordx4 v190, s[36:39], s86 offen offset:2048 lds\n"
"v_and_b32 v95, 1, v103\nMA 24,164,56\ns_lshl_b32 s31, s91, 1\nv_or_b32 v95, s31, v95\nMA 28,164,60\n"
"buffer_load_dwordx4 v191, s[36:39], s86 offen offset:3072 lds\nv_lshlrev_b32 v95, 1, v95\ns_waitcnt vmcnt(20) lgkmcnt(0)\ns_barrier\nMA 32,168,48\n"
"ds_read_b128 a[64:67], v204 offset:32768\nds_read_b128 a[68:71], v204 offset:34816\nds_read_b128 a[72:75], v204 offset:36864\n"
"ds_read_b128 a[76:79], v204 offset:38912\nTR 160,196,32768\nTR 162,196,33280\nMA 36,168,52\ns_add_u32 m0, s4, 0x1a000\ns_nop 0\n"
"buffer_load_dwordx4 v192, s[40:43], s79 offen lds\nMA 40,168,56\nv_xor_b32 v117, v95, v102\ns_cmp_lt_u32 s64, 2\ns_cselect_b32 s83, 0x1f80, 0\n"
"MA 44,168,60\nbuffer_load_dwordx4 v193, s[40:43], s79 offen offset:1024 lds\ns_add_u32 s83, s83, 0x480\nMA 48,172,48\nTR 164,196,40960\n"
"TR 166,196,41472\nds_read_b128 a[80:83], v205 offset:32768\nds_read_b128 a[84:87], v205 offset:34816\nds_read_b128 a[88:91], v205 offset:36864\n"
"ds_read_b128 a[92:95], v205 offset:38912\nMA 52,172,52\nbuffer_load_dwordx4 v194, s[40:43], s79 offen offset:2048 lds\nv_cmp_eq_u32 vcc, 7, v117\n"
"MA 56,172,56\ns_cmp_lt_u32 s64, 2\ns_cselect_b32 s31, 0x2400, 0\nMA 60,172,60\nbuffer_load_dwordx4 v195, s[40:43], s79 offen offset:3072 lds\n"
"s_cmp_lt_u32 s66, 6\ns_cselect_b32 s29, 36, 0x480\ns_waitcnt lgkmcnt(6)\nMA 0,160,64\nTR 168,197,32768\nTR 170,197,33280\nMA 4,160,68\n"
"s_add_u32 m0, s4, 0x4000\ns_mul_i32 s86, s80, 0x1200\nbuffer_load_dwordx4 v188, s[36:39], s86 offen lds\nMA 8,160,72\ns_cmp_lt_u32 s64, 2\n"
"s_cselect_b32 s30, 0, s29\nMA 12,160,76\nbuffer_load_dwordx4 v189, s[36:39], s86 offen offset:1024 lds\ns_add_u32 s31, s31, s30\n"
"s_waitcnt lgkmcnt(6)\nMA 16,164,64\nTR 172,197,40960\nTR 174,197,41472\nMA 20,164,68\nbuffer_load_dwordx4 v190, s[36:39], s86 offen offset:2048 lds\n"
"v_mov_b32 v118, s31\nMA 24,164,72\nv_cmp_eq_u32 s[34:35], 3, v117\ns_cmp_lt_u32 s64, 2\ns_cselect_b32 s31, 0x2400, 2\nMA 28,164,76\n"
"buffer_load_dwordx4 v191, s[36:39], s86 offen offset:3072 lds\nv_mov_b32 v95, s31\ns_waitcnt lgkmcnt(2)\nMA 32,168,64\nTR 160,196,36864\n"
"TR 162,196,37376\nMA 36,168,68\nv_mov_b32 v94, s83\nv_cndmask_b32 v95, v94, v95, s[34:35]\nMA 40,168,72\nv_cndmask_b32 v118, v95, v118, vcc\n"
"v_lshrrev_b32 v120, 2, v117\nMA 44,168,76\ns_cmp_lt_u32 s64, 2\ns_cselect_b32 s31, 32, 36\ns_waitcnt lgkmcnt(2)\nMA 48,172,64\nTR 164,196,45056\n"
"TR 166,196,45568\nMA 52,172,68\nv_mul_u32_u24 v120, s31, v120\ns_cmp_lt_u32 s64, 2\ns_cselect_b32 s31, 0, 2\nMA 56,172,72\nv_and_b32 v94, s31, v117\n"
"v_cmp_ne_u32 vcc, 0, v94\nMA 60,172,76\nv_and_b32 v95, 1, v117\nv_lshlrev_b32 v95, 4, v95\ns_waitcnt lgkmcnt(2)\nMA 0,160,80\nTR 168,197,36864\n"
"TR 170,197,37376\nTR 172,197,45056\nTR 174,197,45568\nMA 4,160,84\nv_cndmask_b32 v95, v95, 20, vcc\ns_lshr_b32 s31, s0, 1\nMA 8,160,88\n"
"s_lshl3_add_u32 s31, s91, s31\nv_add_u32 v94, s31, v101\nMA 12,160,92\nv_mul_u32_u24 v94, v118, v94\nv_add3_u32 v120, v120, v95, v94\n"
"s_waitcnt lgkmcnt(4)\nMA 16,164,80\nv_mov_b32 v121, 0\nv_cmp_eq_u32 vcc, 7, v117\nMA 20,164,84\nv_mov_b32 v210, s74\nv_cmp_eq_u32 s[34:35], 3, v117\n"
"MA 24,164,88\nv_mov_b32 v94, s72\nv_and_b32 v93, 2, v117\nMA 28,164,92\nv_cmp_ne_u32 s[26:27], 0, v93\nv_mov_b32 v95, s70\n"
"s_waitcnt vmcnt(20) lgkmcnt(0)\ns_barrier\nMA 32,168,80\nds_read_b128 a[96:99], v204 offset:49152\nds_read_b128 a[100:103], v204 offset:51200\n"
"ds_read_b128 a[104:107], v204 offset:53248\nds_read_b128 a[108:111], v204 offset:55296\nTR 160,196,49152\nTR 162,196,49664\nMA 36,168,84\n"
"s_add_u32 m0, s4, 0x1e000\ns_nop 0\nbuffer_load_dwordx4 v192, s[40:43], s80 offen lds\nMA 40,168,88\nv_mov_b32 v93, s68\n"
"v_cndmask_b32 v95, v93, v95, s[26:27]\nMA 44,168,92\nbuffer_load_dwordx4 v193, s[40:43], s80 offen offset:1024 lds\n"
"v_cndmask_b32 v94, v95, v94, s[34:35]\nMA 48,172,80\nTR 164,196,57344\nTR 166,196,57856\nds_read_b128 a[112:115], v205 offset:49152\n"
"ds_read_b128 a[116:119], v205 offset:51200\nds_read_b128 a[120:123], v205 offset:53248\nds_read_b128 a[124:127], v205 offset:55296\nMA 52,172,84\n"
"buffer_load_dwordx4 v194, s[40:43], s80 offen offset:2048 lds\nv_cndmask_b32 v210, v94, v210, vcc\nMA 56,172,88\nv_cmp_eq_u32 vcc, 7, v117\n"
"v_mov_b32 v211, s75\nMA 60,172,92\nbuffer_load_dwordx4 v195, s[40:43], s80 offen offset:3072 lds\nv_cmp_eq_u32 s[34:35], 3, v117\n"
"s_waitcnt lgkmcnt(6)\nMA 0,160,96\nTR 168,197,49152\nTR 170,197,49664\nMA 4,160,100\ns_add_u32 m0, s4, 0x8000\ns_mul_i32 s86, s81, 0x1200\n"
"buffer_load_dwordx4 v188, s[36:39], s86 offen lds\nMA 8,160,104\nv_mov_b32 v94, s73\nv_and_b32 v93, 2, v117\nMA 12,160,108\n"
"buffer_load_dwordx4 v189, s[36:39], s86 offen offset:1024 lds\nv_cmp_ne_u32 s[26:27], 0, v93\ns_waitcnt lgkmcnt(6)\nMA 16,164,96\nTR 172,197,57344\n"
"TR 174,197,57856\nMA 20,164,100\nbuffer_load_dwordx4 v190, s[36:39], s86 offen offset:2048 lds\nv_mov_b32 v95, s71\nMA 24,164,104\n"
"v_mov_b32 v93, s69\nv_cndmask_b32 v95, v93, v95, s[26:27]\nMA 28,164,108\nbuffer_load_dwordx4 v191, s[36:39], s86 offen offset:3072 lds\n"
"v_cndmask_b32 v94, v95, v94, s[34:35]\ns_waitcnt lgkmcnt(2)\nMA 32,168,96\nTR 160,196,53248\nTR 162,196,53760\nMA 36,168,100\n"
"v_cndmask_b32 v211, v94, v211, vcc\nv_lshl_add_u64 v[210:211], v[120:121], 0, v[210:211]\nMA 40,168,104\nv_lshlrev_b32 v212, 4, v118\n"
"v_mov_b32 v213, 0\nMA 44,168,108\ns_lshl_b32 s18, s90, 13\ns_lshl_b32 s31, s91, 10\ns_waitcnt lgkmcnt(2)\nMA 48,172,96\nTR 164,196,61440\n"
"TR 166,196,61952\nMA 52,172,100\ns_add_u32 s18, s18, s31\nv_cmp_eq_u32 vcc, 3, v117\nMA 56,172,104\ns_cmp_eq_u32 s64, 3\ns_cselect_b32 s31, 1, 0\n"
"MA 60,172,108\nv_mul_u32_u24 v119, s31, v101\nv_cndmask_b32 v119, 0, v119, vcc\ns_waitcnt lgkmcnt(2)\nMA 0,160,112\nTR 168,197,53248\n"
"TR 170,197,53760\nTR 172,197,61440\nTR 174,197,61952\nMA 4,160,116\nv_cmp_eq_u32 s[88:89], 0, v119\nv_add_u32 v119, 57, v100\nMA 8,160,120\n"
"s_cmp_eq_u32 s64, 3\ns_cselect_b32 s31, 7, 0\nMA 12,160,124\nv_cmp_gt_u32 vcc, s31, v100\nv_bfe_u32 v241, v119, 3, 1\ns_waitcnt lgkmcnt(4)\n"
"MA 16,164,112\nv_bfe_u32 v94, v119, 1, 1\nv_lshl_or_b32 v241, v241, 1, v94\nMA 20,164,116\nv_lshlrev_b32 v241, 1, v241\nv_xor_b32 v241, 3, v241\n"
"MA 24,164,120\nv_lshlrev_b32 v241, 4, v241\nv_lshl_add_u32 v241, v119, 7, v241\nMA 28,164,124\nv_add_u32 v241, 0x2000, v241\nv_mov_b32 v94, 0x100000\n"
"s_waitcnt vmcnt(20) lgkmcnt(0)\ns_barrier\nMA 32,168,112\nds_read_b128 a[128:131], v204\nds_read_b128 a[132:135], v204 offset:2048\n"
"ds_read_b128 a[136:139], v204 offset:4096\nds_read_b128 a[140:143], v204 offset:6144\nds_read_b64_tr_b16 v[160:161], v198\nTR 162,198,512\n"
"MA 36,168,116\ns_add_u32 m0, s4, 0x22000\ns_nop 0\nbuffer_load_dwordx4 v192, s[40:43], s81 offen lds\nMA 40,168,120\n"
"v_cndmask_b32 v241, v94, v241, vcc\ns_cmp_eq_u32 s64, 3\ns_cselect_b32 s31, 7, 0\nMA 44,168,124\n"
"buffer_load_dwordx4 v193, s[40:43], s81 offen offset:1024 lds\nv_cmp_gt_u32 vcc, s31, v100\nMA 48,172,112\nTR 164,198,8192\nTR 166,198,8704\n"
"ds_read_b128 a[144:147], v205\nds_read_b128 a[148:151], v205 offset:2048\nds_read_b128 a[152:155], v205 offset:4096\n"
"ds_read_b128 a[156:159], v205 offset:6144\nMA 52,172,116\nbuffer_load_dwordx4 v194, s[40:43], s81 offen offset:2048 lds\nv_lshlrev_b32 v242, 1, v100\n"
"MA 56,172,120\nv_add_u32 v242, 0x472, v242\nv_mov_b32 v94, 0x40000000\nMA 60,172,124\nbuffer_load_dwordx4 v195, s[40:43], s81 offen offset:3072 lds\n"
"v_cndmask_b32 v242, v94, v242, vcc\ns_waitcnt lgkmcnt(6)\nMA 0,160,128\nds_read_b64_tr_b16 v[168:169], v199\nTR 170,199,512\nMA 4,160,132\n"
"s_add_u32 m0, s4, 0xc000\ns_mul_i32 s86, s19, 0x1200\nbuffer_load_dwordx4 v188, s[36:39], s86 offen lds\nMA 8,160,136\n"
"buffer_load_ushort v242, v242, s[60:63], 0 offen\nv_and_b32 v94, 3, v103\nMA 12,160,140\n"
"buffer_load_dwordx4 v189, s[36:39], s86 offen offset:1024 lds\nv_lshlrev_b32 v94, 1, v94\ns_waitcnt lgkmcnt(6)\nMA 16,164,128\nTR 172,199,8192\n"
"TR 174,199,8704\nMA 20,164,132\nbuffer_load_dwordx4 v190, s[36:39], s86 offen offset:2048 lds\nv_xor_b32 v119, v94, v102\nMA 24,164,136\n"
"v_lshlrev_b32 v119, 4, v119\ns_mul_i32 s5, s64, 0xc00\nMA 28,164,140\nbuffer_load_dwordx4 v191, s[36:39], s86 offen offset:3072 lds\n"
"v_mul_u32_u24 v214, 0x480, v101\ns_waitcnt lgkmcnt(2)\nMA 32,168,128\nTR 160,198,4096\nTR 162,198,4608\nMA 36,168,132\ns_mul_i32 s31, s64, 24\n"
"s_lshl_b32 s30, s92, 5\nMA 40,168,136\ns_add_u32 s31, s31, s30\ns_mul_i32 s31, s31, 0x480\nMA 44,168,140\ns_add_u32 s31, s31, 0x800\n"
"v_add3_u32 v214, v214, v119, s31\ns_waitcnt lgkmcnt(2)\nMA 48,172,128\nTR 164,198,12288\nTR 166,198,12800\nMA 52,172,132\ns_mul_i32 s30, s64, 24\n"
"s_mul_i32 s30, s92, s30\nMA 56,172,136\ns_cmp_ge_u32 s30, 64\ns_cselect_b32 s31, 0, 0x51000\nMA 60,172,140\ns_add_u32 s31, s31, 0xfffe5000\n"
"v_add_u32 v217, s31, v214\ns_waitcnt lgkmcnt(2)\nMA 0,160,144\nTR 168,199,4096\nTR 170,199,4608\nTR 172,199,12288\nTR 174,199,12800\nMA 4,160,148\n"
"v_mul_u32_u24 v215, 0x480, v101\ns_mul_i32 s31, s64, 24\nMA 8,160,152\ns_lshl_b32 s30, s92, 5\ns_add_u32 s31, s31, s30\nMA 12,160,156\n"
"s_mul_i32 s31, s31, 0x480\ns_add_u32 s31, s31, 0x2800\ns_waitcnt lgkmcnt(4)\nMA 16,164,144\nv_add3_u32 v215, v215, v119, s31\ns_mul_i32 s30, s64, 24\n"
"MA 20,164,148\ns_add_u32 s30, s30, 8\ns_mul_i32 s30, s92, s30\nMA 24,164,152\ns_cmp_ge_u32 s30, 64\ns_cselect_b32 s31, 0, 0x51000\nMA 28,164,156\n"
"s_add_u32 s31, s31, 0xfffe5000\nv_add_u32 v218, s31, v215\ns_waitcnt vmcnt(21) lgkmcnt(0)\ns_barrier\nMA 32,168,144\n"
"ds_read_b128 a[160:163], v204 offset:16384\nds_read_b128 a[164:167], v204 offset:18432\nds_read_b128 a[168:171], v204 offset:20480\n"
"ds_read_b128 a[172:175], v204 offset:22528\nds_read_b64_tr_b16 v[160:161], v196\nTR 162,196,512\nMA 36,168,148\ns_add_u32 m0, s4, 0x16000\ns_nop 0\n"
"buffer_load_dwordx4 v192, s[40:43], s19 offen lds\nMA 40,168,152\nv_mul_u32_u24 v216, 0x480, v101\ns_mul_i32 s31, s64, 24\nMA 44,168,156\n"
"buffer_load_dwordx4 v193, s[40:43], s19 offen offset:1024 lds\ns_lshl_b32 s30, s92, 5\nMA 48,172,144\nTR 164,196,8192\nTR 166,196,8704\n"
"ds_read_b128 a[176:179], v205 offset:16384\nds_read_b128 a[180:183], v205 offset:18432\nds_read_b128 a[184:187], v205 offset:20480\n"
"ds_read_b128 a[188:191], v205 offset:22528\nMA 52,172,148\nbuffer_load_dwordx4 v194, s[40:43], s19 offen offset:2048 lds\ns_add_u32 s31, s31, s30\n"
"MA 56,172,152\ns_mul_i32 s31, s31, 0x480\ns_add_u32 s31, s31, 0x4800\nMA 60,172,156\nbuffer_load_dwordx4 v195, s[40:43], s19 offen offset:3072 lds\n"
"v_add3_u32 v216, v216, v119, s31\ns_waitcnt lgkmcnt(6)\nMA 0,160,160\nds_read_b64_tr_b16 v[168:169], v197\nTR 170,197,512\nMA 4,160,164\n"
"s_add_u32 m0, s4, 0x10000\ns_mul_i32 s86, s0, 0x1200\ns_add_u32 s86, s86, 0x80\nbuffer_load_dwordx4 v188, s[36:39], s86 offen lds\nMA 8,160,168\n"
"s_mul_i32 s30, s64, 24\ns_add_u32 s30, s30, 16\nMA 12,160,172\nbuffer_load_dwordx4 v189, s[36:39], s86 offen offset:1024 lds\n"
"s_mul_i32 s30, s92, s30\ns_waitcnt lgkmcnt(6)\nMA 16,164,160\nTR 172,197,8192\nTR 174,197,8704\nMA 20,164,164\n"
"buffer_load_dwordx4 v190, s[36:39], s86 offen offset:2048 lds\ns_cmp_ge_u32 s30, 64\ns_cselect_b32 s31, 0, 0x51000\nMA 24,164,168\n"
"s_add_u32 s31, s31, 0xfffe5000\nv_add_u32 v219, s31, v216\nMA 28,164,172\nbuffer_load_dwordx4 v191, s[36:39], s86 offen offset:3072 lds\n"
"s_lshl2_add_u32 s96, s92, s64\ns_waitcnt lgkmcnt(2)\nMA 32,168,160\nTR 160,196,4096\nTR 162,196,4608\nMA 36,168,164\nv_lshlrev_b32 v221, 3, v116\n"
"v_add_u32 v221, 0x400, v221\nMA 40,168,168\nv_cmp_gt_u32 vcc, 16, v116\nv_lshrrev_b32 v222, 3, v116\nMA 44,168,172\nv_lshlrev_b32 v94, 7, v114\n"
"v_lshl_add_u32 v222, v222, 9, v94\ns_waitcnt lgkmcnt(2)\nMA 48,172,160\nTR 164,196,12288\nTR 166,196,12800\nMA 52,172,164\n"
"v_lshl_add_u32 v222, v102, 4, v222\nv_add_u32 v222, 0x500, v222\nMA 56,172,168\nv_cmp_gt_u32 s[34:35], 19, v116\nv_xor_b32 v94, 1, v114\n"
"MA 60,172,172\nv_lshlrev_b32 v95, 4, v116\nv_lshl_add_u32 v94, v94, 7, v95\ns_waitcnt lgkmcnt(2)\nMA 0,160,176\nTR 168,197,4096\nTR 170,197,4608\n"
"TR 172,197,12288\nTR 174,197,12800\nMA 4,160,180\nv_add_u32 v94, 0x800, v94\nv_mov_b32 v95, 0x100000\nMA 8,160,184\n"
"v_cndmask_b32 v94, v95, v94, s[34:35]\nv_cndmask_b32 v222, v94, v222, vcc\nMA 12,160,188\nv_cmp_gt_u32 vcc, 16, v116\nv_lshrrev_b32 v186, 3, v116\n"
"s_waitcnt lgkmcnt(4)\nMA 16,164,176\nv_lshlrev_b32 v94, 7, v114\nv_lshl_add_u32 v186, v186, 9, v94\nMA 20,164,180\n"
"v_lshl_add_u32 v186, v102, 4, v186\nv_add_u32 v186, 0x600, v186\nMA 24,164,184\nv_cmp_gt_u32 s[34:35], 19, v116\nv_xor_b32 v94, 1, v114\n"
"MA 28,164,188\nv_lshlrev_b32 v95, 4, v116\nv_lshl_add_u32 v94, v94, 7, v95\ns_waitcnt vmcnt(21) lgkmcnt(0)\ns_barrier\nMA 32,168,176\n"
"ds_read_b128 a[192:195], v204 offset:32768\nds_read_b128 a[196:199], v204 offset:34816\nds_read_b128 a[200:203], v204 offset:36864\n"
"ds_read_b128 a[204:207], v204 offset:38912\nTR 160,196,16384\nTR 162,196,16896\nMA 36,168,180\ns_add_u32 m0, s4, 0\ns_mul_i32 s86, s1, 0x1200\n"
"s_add_u32 s86, s86, 0x80\nbuffer_load_dwordx4 v188, s[36:39], s86 offen lds\nMA 40,168,184\nv_readfirstlane_b32 s84, v70\n"
"v_readfirstlane_b32 s85, v71\ns_add_u32 s84, s84, 0x7f4a7c15\ns_addc_u32 s85, s85, 0x9e3779b9\nMA 44,168,188\n"
"buffer_load_dwordx4 v189, s[36:39], s86 offen offset:1024 lds\nv_add_u32 v94, 0x840, v94\nMA 48,172,176\nTR 164,196,24576\nTR 166,196,25088\n"
"ds_read_b128 a[208:211], v205 offset:32768\nds_read_b128 a[212:215], v205 offset:34816\nds_read_b128 a[216:219], v205 offset:36864\n"
"ds_read_b128 a[220:223], v205 offset:38912\nMA 52,172,180\nbuffer_load_dwordx4 v190, s[36:39], s86 offen offset:2048 lds\nv_mov_b32 v95, 0x100000\n"
"MA 56,172,184\nv_cndmask_b32 v94, v95, v94, s[34:35]\nv_cndmask_b32 v186, v94, v186, vcc\nMA 60,172,188\n"
"buffer_load_dwordx4 v191, s[36:39], s86 offen offset:3072 lds\nv_and_b32 v120, 15, v100\ns_waitcnt lgkmcnt(6)\nMA 0,160,192\nTR 168,197,16384\n"
"TR 170,197,16896\nMA 4,160,196\ns_and_b32 s31, s93, 7\ns_lshl1_add_u32 s31, s31, s92\nMA 8,160,200\ns_lshl2_add_u32 s31, s31, s64\n"
"s_mul_i32 s31, s31, 9\nMA 12,160,204\nv_add_u32 v120, s31, v120\nv_and_b32 v94, 47, v100\ns_waitcnt lgkmcnt(6)\nMA 16,164,192\nTR 172,197,24576\n"
"TR 174,197,25088\nMA 20,164,196\nv_cmp_gt_u32 vcc, 9, v94\nv_mul_u32_u24 v185, 0x480, v120\nMA 24,164,200\nv_cmp_gt_u32 s[34:35], 16, v100\n"
"s_lshl_b32 s31, s94, 7\nMA 28,164,204\nv_mov_b32 v94, s31\nv_mov_b32 v95, 0x400\ns_waitcnt lgkmcnt(2)\nMA 32,168,192\nTR 160,196,20480\n"
"TR 162,196,20992\nMA 36,168,196\nv_cndmask_b32 v94, v95, v94, s[34:35]\nv_add_u32 v185, v94, v185\nMA 40,168,200\nv_mov_b32 v94, 0x40000000\n"
"v_cndmask_b32 v185, v94, v185, vcc\nMA 44,168,204\ns_lshl3_add_u32 s31, s65, s96\ns_lshl_b32 s31, s31, 14\ns_waitcnt lgkmcnt(2)\nMA 48,172,192\n"
"TR 164,196,28672\nTR 166,196,29184\nMA 52,172,196\ns_add_u32 s31, s31, 0x40000\nv_add_u32 v225, s31, v220\nMA 56,172,200\n"
"v_lshlrev_b32 v187, 2, v106\nv_lshl_add_u32 v187, v103, 11, v187\nMA 60,172,204\ns_lshl_b32 s31, s65, 14\ns_lshl_b32 s30, s96, 6\n"
"s_waitcnt lgkmcnt(2)\nMA 0,160,208\nTR 168,197,20480\nTR 170,197,20992\nTR 172,197,28672\nTR 174,197,29184\nMA 4,160,212\ns_add_u32 s31, s31, s30\n"
"s_add_u32 s31, s31, 0x280000\nMA 8,160,216\nv_add_u32 v187, s31, v187\ns_lshl_b32 s31, s65, 7\nMA 12,160,220\ns_add_u32 s31, s31, s96\n"
"s_mul_i32 s31, s31, 18\ns_getpc_b64 s[22:23]\nL_pc1:\nv_add_u32 v72, 0x2000, v184\nv_min_u32 v72, L_end-L_pc1-4, v72\n"
"global_load_dword v253, v72, s[22:23]\ns_waitcnt lgkmcnt(4)\nMA 16,164,208\ns_lshl1_add_u32 s31, s94, s31\ns_lshl_b32 s31, s31, 10\nMA 20,164,212\n"
"s_add_u32 s31, s31, 0x400000\nv_add_u32 v226, s31, v220\nMA 24,164,216\ns_cmp_lt_u32 s94, 4\ns_cselect_b32 s97, 0, 0x40000000\nMA 28,164,220\n"
"s_and_b32 s31, s94, 1\ns_lshl_b32 s30, s65, 7\ns_waitcnt vmcnt(18) lgkmcnt(0)\ns_barrier\nMA 32,168,208\nds_read_b128 a[224:227], v204 offset:49152\n"
"ds_read_b128 a[228:231], v204 offset:51200\nds_read_b128 a[232:235], v204 offset:53248\nds_read_b128 a[236:239], v204 offset:55296\nTR 160,196,32768\n"
"TR 162,196,33280\nMA 36,168,212\ns_add_u32 m0, s4, 0x4000\ns_mul_i32 s86, s2, 0x1200\ns_add_u32 s86, s86, 0x80\n"
"buffer_load_dwordx4 v188, s[36:39], s86 offen lds\nMA 40,168,216\ns_add_u32 s30, s30, s96\ns_mul_i32 s30, s30, 18\nMA 44,168,220\n"
"buffer_load_dwordx4 v189, s[36:39], s86 offen offset:1024 lds\ns_lshr_b32 s29, s94, 1\nMA 48,172,208\nTR 164,196,40960\nTR 166,196,41472\n"
"ds_read_b128 a[240:243], v205 offset:49152\nds_read_b128 a[244:247], v205 offset:51200\nds_read_b128 a[248:251], v205 offset:53248\n"
"ds_read_b128 a[252:255], v205 offset:55296\nMA 52,172,212\nbuffer_load_dwordx4 v190, s[36:39], s86 offen offset:2048 lds\ns_and_b32 s29, s29, 1\n"
"MA 56,172,216\ns_add_u32 s30, s30, s29\ns_lshl_b32 s30, s30, 10\nMA 60,172,220\nbuffer_load_dwordx4 v191, s[36:39], s86 offen offset:3072 lds\n"
"s_lshl3_add_u32 s31, s31, s30\ns_waitcnt lgkmcnt(6)\nMA 0,160,224\nTR 168,197,32768\nTR 170,197,33280\nMA 4,160,228\ns_add_u32 s31, s31, s97\n"
"s_add_u32 s31, s31, 0x404000\nMA 8,160,232\nv_add_u32 v227, s31, v220\nv_mul_u32_u24 v228, 0x480, v101\nMA 12,160,236\ns_mul_i32 s31, s91, 18\n"
"s_lshl3_add_u32 s31, s90, s31\ns_waitcnt lgkmcnt(6)\nMA 16,164,224\nTR 172,197,40960\nTR 174,197,41472\nMA 20,164,228\ns_mul_i32 s31, s31, 0x480\n"
"s_lshl_b32 s30, s94, 7\nMA 24,164,232\ns_add_u32 s31, s31, s30\nv_add3_u32 v228, v228, v119, s31\nMA 28,164,236\nv_lshl_add_u32 v229, v114, 2, v115\n"
"v_mul_u32_u24 v229, 18, v229\ns_waitcnt lgkmcnt(2)\nMA 32,168,224\nTR 160,196,36864\nTR 162,196,37376\nMA 36,168,228\nv_and_b32 v94, 1, v101\n"
"v_add_u32 v229, v94, v229\nMA 40,168,232\nv_mul_u32_u24 v229, 0x480, v229\ns_lshl_b32 s31, s90, 1\nMA 44,168,236\ns_lshl3_add_u32 s31, s91, s31\n"
"s_mul_i32 s31, s31, 0x5100\ns_waitcnt lgkmcnt(2)\nMA 48,172,224\nTR 164,196,45056\nTR 166,196,45568\nMA 52,172,228\ns_lshl_b32 s30, s94, 7\n"
"s_add_u32 s31, s31, s30\nMA 56,172,232\ns_add_u32 s31, s31, 0x4800\nv_add3_u32 v229, v229, v119, s31\nMA 60,172,236\nv_and_b32 v120, 1, v100\n"
"s_and_b32 s31, s94, 3\ns_waitcnt lgkmcnt(2)\nMA 0,160,240\nTR 168,197,36864\nTR 170,197,37376\nTR 172,197,45056\nTR 174,197,45568\nMA 4,160,244\n"
"s_lshl_b32 s31, s31, 5\ns_add_u32 s31, s31, s97\nMA 8,160,248\ns_add_u32 s31, s31, 0x400\nv_lshl_add_u32 v120, v120, 4, s31\nMA 12,160,252\n"
"v_mul_u32_u24 v230, 18, v115\nv_lshl_add_u32 v230, v114, 3, v230\ns_waitcnt lgkmcnt(4)\nMA 16,164,240\nv_bfe_u32 v94, v100, 1, 3\n"
"v_add_u32 v230, v94, v230\nMA 20,164,244\nv_mul_u32_u24 v230, 0x480, v230\nv_add_u32 v230, v120, v230\nMA 24,164,248\nv_and_b32 v231, 3, v101\n"
"v_lshlrev_b32 v94, 1, v114\nMA 28,164,252\nv_lshl_add_u32 v231, v231, 2, v94\nv_bfe_u32 v94, v100, 2, 1\ns_waitcnt vmcnt(13) lgkmcnt(0)\ns_barrier\n"
"MA 32,168,240\nds_read_b128 v[128:131], v204\nds_read_b128 v[132:135], v204 offset:2048\nds_read_b128 v[136:139], v204 offset:4096\n"
"ds_read_b128 v[140:143], v204 offset:6144\nTR 160,196,49152\nTR 162,196,49664\nMA 36,168,244\ns_add_u32 m0, s4, 0x8000\ns_mul_i32 s86, s3, 0x1200\n"
"s_add_u32 s86, s86, 0x80\nbuffer_load_dwordx4 v188, s[36:39], s86 offen lds\nMA 40,168,248\nv_add_u32 v231, v94, v231\nv_mul_u32_u24 v231, 18, v231\n"
"MA 44,168,252\nbuffer_load_dwordx4 v189, s[36:39], s86 offen offset:1024 lds\nv_bfe_u32 v94, v100, 1, 1\nMA 48,172,240\nTR 164,196,57344\n"
"TR 166,196,57856\nds_read_b128 v[144:147], v205\nds_read_b128 v[148:151], v205 offset:2048\nds_read_b128 v[152:155], v205 offset:4096\n"
"ds_read_b128 v[156:159], v205 offset:6144\nMA 52,172,244\nbuffer_load_dwordx4 v190, s[36:39], s86 offen offset:2048 lds\nv_add_u32 v231, v94, v231\n"
"MA 56,172,248\nv_mul_u32_u24 v231, 0x480, v231\nv_add_u32 v231, v120, v231\nMA 60,172,252\n"
"buffer_load_dwordx4 v191, s[36:39], s86 offen offset:3072 lds\nv_add_u32 v231, 0x4800, v231\ns_waitcnt lgkmcnt(6)\n"
"v_mfma_f32_16x16x32_bf16 v[0:3], v[160:163], v[128:131], v[0:3]\nTR 168,197,49152\nTR 170,197,49664\n"
"v_mfma_f32_16x16x32_bf16 v[4:7], v[160:163], v[132:135], v[4:7]\nv_lshl_add_u32 v120, v103, 2, v107\nv_lshlrev_b32 v232, 5, v109\n"
"v_mfma_f32_16x16x32_bf16 v[8:11], v[160:163], v[136:139], v[8:11]\nv_lshl_add_u32 v232, v120, 7, v232\nv_add_u32 v232, v108, v232\n"
"v_mfma_f32_16x16x32_bf16 v[12:15], v[160:163], v[140:143], v[12:15]\nv_add_u32 v232, 0x10000, v232\nv_xor_b32 v233, 1, v109\ns_waitcnt lgkmcnt(6)\n"
"v_mfma_f32_16x16x32_bf16 v[16:19], v[164:167], v[128:131], v[16:19]\nTR 172,197,57344\nTR 174,197,57856\n"
"v_mfma_f32_16x16x32_bf16 v[20:23], v[164:167], v[132:135], v[20:23]\nv_lshlrev_b32 v233, 5, v233\nv_lshl_add_u32 v233, v120, 7, v233\n"
"v_mfma_f32_16x16x32_bf16 v[24:27], v[164:167], v[136:139], v[24:27]\nv_add_u32 v233, v108, v233\nv_add_u32 v233, 0x10000, v233\n"
"v_mfma_f32_16x16x32_bf16 v[28:31], v[164:167], v[140:143], v[28:31]\nv_xor_b32 v234, 2, v109\nv_lshlrev_b32 v234, 5, v234\ns_waitcnt lgkmcnt(2)\n"
"v_mfma_f32_16x16x32_bf16 v[32:35], v[168:171], v[128:131], v[32:35]\nTR 160,196,53248\nTR 162,196,53760\n"
"v_mfma_f32_16x16x32_bf16 v[36:39], v[168:171], v[132:135], v[36:39]\nv_lshl_add_u32 v234, v120, 7, v234\nv_add_u32 v234, v108, v234\n"
"v_mfma_f32_16x16x32_bf16 v[40:43], v[168:171], v[136:139], v[40:43]\nv_add_u32 v234, 0x10000, v234\nv_xor_b32 v235, 3, v109\n"
"v_mfma_f32_16x16x32_bf16 v[44:47], v[168:171], v[140:143], v[44:47]\nv_lshlrev_b32 v235, 5, v235\nv_lshl_add_u32 v235, v120, 7, v235\n"
"s_waitcnt lgkmcnt(2)\nv_mfma_f32_16x16x32_bf16 v[48:51], v[172:175], v[128:131], v[48:51]\nTR 164,196,61440\nTR 166,196,61952\n"
"v_mfma_f32_16x16x32_bf16 v[52:55], v[172:175], v[132:135], v[52:55]\nv_add_u32 v235, v108, v235\nv_add_u32 v235, 0x10000, v235\n"
"v_mfma_f32_16x16x32_bf16 v[56:59], v[172:175], v[136:139], v[56:59]\nv_lshl_add_u32 v236, v120, 5, v108\nv_add_u32 v236, 0x22000, v236\n"
"v_mfma_f32_16x16x32_bf16 v[60:63], v[172:175], v[140:143], v[60:63]\nv_mul_u32_u24 v120, 0x480, v106\nv_lshl_add_u32 v120, v103, 3, v120\n"
"s_waitcnt lgkmcnt(2)\nv_mfma_f32_16x16x32_bf16 v[0:3], v[160:163], v[144:147], v[0:3]\nTR 168,197,53248\nTR 170,197,53760\nTR 172,197,61440\n"
"TR 174,197,61952\nv_mfma_f32_16x16x32_bf16 v[4:7], v[160:163], v[148:151], v[4:7]\ns_lshl_b32 s31, s65, 7\ns_lshl4_add_u32 s31, s96, s31\n"
"v_mfma_f32_16x16x32_bf16 v[8:11], v[160:163], v[152:155], v[8:11]\ns_mul_i32 s31, s31, 0x480\nv_add_u32 v120, s31, v120\n"
"v_mfma_f32_16x16x32_bf16 v[12:15], v[160:163], v[156:159], v[12:15]\ns_lshl_b32 s31, s94, 7\nv_add_u32 v237, s31, v120\ns_waitcnt lgkmcnt(4)\n"
"v_mfma_f32_16x16x32_bf16 v[16:19], v[164:167], v[144:147], v[16:19]\ns_and_b32 s31, s94, 3\ns_lshl_b32 s31, s31, 5\n"
"v_mfma_f32_16x16x32_bf16 v[20:23], v[164:167], v[148:151], v[20:23]\ns_add_u32 s31, s31, s97\ns_add_u32 s31, s31, 0x400\n"
"v_mfma_f32_16x16x32_bf16 v[24:27], v[164:167], v[152:155], v[24:27]\nv_add_u32 v238, s31, v120\ns_lshl3_add_u32 s31, s91, 2\n"
"v_mfma_f32_16x16x32_bf16 v[28:31], v[164:167], v[156:159], v[28:31]\nv_cmp_eq_u32 vcc, s31, v103\ns_lshl_b32 s31, s90, 8\n"
"s_waitcnt vmcnt(13) lgkmcnt(0)\ns_barrier\nv_mfma_f32_16x16x32_bf16 v[32:35], v[168:171], v[144:147], v[32:35]\nds_read_b64_tr_b16 v[160:161], v198\n"
"TR 162,198,512\nv_mfma_f32_16x16x32_bf16 v[36:39], v[168:171], v[148:151], v[36:39]\ns_add_u32 m0, s4, 0xc000\ns_mul_i32 s86, s78, 0x1200\n"
"s_add_u32 s86, s86, 0x80\nbuffer_load_dwordx4 v188, s[36:39], s86 offen lds\nv_mfma_f32_16x16x32_bf16 v[40:43], v[168:171], v[152:155], v[40:43]\n"
"s_add_u32 s31, s31, 0x27000\nv_lshl_add_u32 v239, v106, 2, s31\nv_mfma_f32_16x16x32_bf16 v[44:47], v[168:171], v[156:159], v[44:47]\n"
"buffer_load_dwordx4 v189, s[36:39], s86 offen offset:1024 lds\nv_mov_b32 v94, 0x100000\n"
"v_mfma_f32_16x16x32_bf16 v[48:51], v[172:175], v[144:147], v[48:51]\nTR 164,198,8192\nTR 166,198,8704\n"
"v_mfma_f32_16x16x32_bf16 v[52:55], v[172:175], v[148:151], v[52:55]\nbuffer_load_dwordx4 v190, s[36:39], s86 offen offset:2048 lds\n"
"v_cndmask_b32 v239, v94, v239, vcc\nv_mfma_f32_16x16x32_bf16 v[56:59], v[172:175], v[152:155], v[56:59]\ns_lshl_b32 s31, s96, 6\n"
"s_add_u32 s31, s31, 0x27000\nv_mfma_f32_16x16x32_bf16 v[60:63], v[172:175], v[156:159], v[60:63]\n"
"buffer_load_dwordx4 v191, s[36:39], s86 offen offset:3072 lds\nv_lshl_add_u32 v240, v106, 2, s31\nv_min_u32 v243, 47, v100\n"
"v_lshlrev_b32 v243, 4, v243\nv_add_u32 v243, 0x1000, v243\ns_lshl_b32 s31, s65, 7\ns_add_u32 s31, s31, 0x2000\nv_lshl_add_u32 v244, v102, 4, s31\n"
"v_lshlrev_b32 v245, 4, v116\nv_add_u32 v245, 0x3000, v245\nv_lshlrev_b32 v94, 2, v103\nv_subrev_u32 v120, v94, v106\nv_cmp_eq_u32 vcc, 0, v120\n"
"v_mov_b32 v246, 0x3f80\nv_cmp_eq_u32 s[34:35], 1, v120\nv_mov_b32 v94, 0x3f800000\ns_nop 0\nv_cndmask_b32 v94, 0, v94, s[34:35]\n"
"v_cndmask_b32 v246, v94, v246, vcc\nv_cmp_eq_u32 vcc, 2, v120\nv_mov_b32 v247, 0x3f80\nv_cmp_eq_u32 s[34:35], 3, v120\nv_mov_b32 v94, 0x3f800000\n"
"s_nop 0\nv_cndmask_b32 v94, 0, v94, s[34:35]\nv_cndmask_b32 v247, v94, v247, vcc\nv_cmp_eq_u32 vcc, 1, v103\ns_nop 1\nv_cndmask_b32 v249, 0, 1, vcc\n"
"v_mov_b32 v250, v220\nv_lshlrev_b32 v251, 5, v100\nv_add_u32 v251, 0x100, v251\ns_cmp_eq_u32 s91, 0\ns_cselect_b32 s31, 16, 0\n"
"s_add_u32 s31, s66, s31\ns_cmp_lt_u32 s31, 6\ns_cselect_b32 s7, s66, 15\ns_lshl_b32 s98, s90, 1\ns_lshl2_add_u32 s98, s65, s98\n"
"s_mul_i32 s98, s98, 0xa00\ns_add_u32 s98, s98, 0x4000\nv_mov_b32 v94, s66\nv_cmp_gt_u32 vcc, 2, v94\nv_cmp_le_u32 s[34:35], 2, v103\n"
"v_lshlrev_b32 v180, 3, v103\nv_lshl_add_u32 v180, v106, 4, v180\ns_and_b32 s31, s66, 1\ns_lshl_b32 s31, s31, 9\ns_add_u32 s31, s98, s31\n"
"s_add_u32 s31, s31, 0xfffffff0\nv_add_u32 v180, s31, v180\nv_mov_b32 v94, 0x40000000\nv_cndmask_b32 v180, v94, v180, s[34:35]\nv_mov_b32 v95, s66\n"
"v_cmp_eq_u32 s[34:35], 2, v95\nv_cmp_eq_u32 s[26:27], 3, v103\ns_add_u32 s31, s98, 0x400\nv_lshl_add_u32 v94, v106, 3, s31\n"
"v_mov_b32 v95, 0x40000000\nv_cndmask_b32 v94, v95, v94, s[26:27]\nv_mov_b32 v93, s66\nv_cmp_eq_u32 s[26:27], 5, v93\nv_cmp_gt_u32 s[24:25], 32, v100\n"
"v_bfe_u32 v95, v102, 1, 1\nv_and_b32 v93, 1, v102\nv_xor_b32 v93, 1, v93\nv_lshrrev_b32 v92, 2, v102\nv_mul_u32_u24 v92, 0xa00, v92\n"
"v_lshl_add_u32 v93, v93, 7, v92\nv_lshl_add_u32 v95, v95, 6, v93\nv_lshl_add_u32 v95, v101, 4, v95\ns_add_u32 s31, s98, 0x900\n"
"v_add_u32 v95, s31, v95\nv_mov_b32 v93, 0x40000000\nv_cndmask_b32 v95, v93, v95, s[24:25]\nv_and_b32 v93, 3, v102\nv_lshrrev_b32 v92, 2, v102\n"
"v_mul_u32_u24 v92, 0xa00, v92\nv_lshl_add_u32 v93, v93, 7, v92\nv_lshl_add_u32 v93, v101, 4, v93\ns_lshl_b32 s31, s66, 9\ns_add_u32 s31, s98, s31\n"
"s_add_u32 s31, s31, 0xffffff00\nv_add_u32 v93, s31, v93\nv_cndmask_b32 v95, v93, v95, s[26:27]\nv_cndmask_b32 v94, v95, v94, s[34:35]\n"
"v_cndmask_b32 v180, v94, v180, vcc\nv_cmp_le_u32 vcc, 2, v103\nv_lshlrev_b32 v181, 1, v106\nv_lshl_add_u32 v181, v103, 9, v181\n"
"s_lshl_b32 s31, s90, 10\ns_add_u32 s31, s31, 0x27000\nv_add_u32 v181, s31, v181\nv_mov_b32 v94, 0x100000\nv_cndmask_b32 v181, v94, v181, vcc\n"
"s_cmp_eq_u32 s66, 5\ns_cselect_b32 s31, 16, 64\nv_cmp_gt_u32 vcc, s31, v100\nv_lshlrev_b32 v182, 4, v102\nv_lshl_add_u32 v182, v101, 7, v182\n"
"s_lshl_b32 s31, s90, 10\ns_cmp_eq_u32 s66, 5\ns_cselect_b32 s30, 0x300, 0\ns_add_u32 s31, s31, s30\ns_add_u32 s31, s31, 0x27400\n"
"v_add_u32 v182, s31, v182\nv_mov_b32 v94, 0x100000\nv_cndmask_b32 v182, v94, v182, vcc\ns_cmp_eq_u32 s66, 5\ns_cselect_b32 s31, 2, 64\n"
"v_cmp_eq_u32 vcc, s31, v101\nv_mov_b32 v183, 0x3f803f80\ns_nop 0\nv_cndmask_b32 v183, 0, v183, vcc\ns_mul_i32 s9, s65, 6\ns_add_u32 s9, s9, s66\n"
"s_lshl3_add_u32 s9, s9, 0x1000\ns_lshl4_add_u32 s10, s65, s66\ns_lshl3_add_u32 s10, s10, 0x2000\ns_lshl1_add_u32 s11, s65, s90\n"
"s_lshl_b32 s31, s7, 2\ns_lshl4_add_u32 s11, s11, s31\ns_add_u32 s11, s11, 0x3000\ns_mul_i32 s12, s64, 0xa200\ns_mul_i32 s13, s91, 0x51000\n"
"s_add_u32 s31, s67, s64\ns_cmp_eq_u32 s31, 0\ns_cselect_b32 s15, 1, 0\ns_lshl4_add_u32 s31, s64, s66\ns_cmp_lt_u32 s31, 6\ns_cselect_b32 s16, 1, 0\n"
"s_lshl_b32 s17, s91, 10\ns_add_u32 s17, s17, 0x26000\nbuffer_load_dword v253, v248, s[48:51], s9 offen sc1\n"
"buffer_load_dword v253, v248, s[48:51], s10 offen sc1\nbuffer_load_dword v253, v248, s[48:51], s11 offen sc1\n"
"buffer_load_dword v253, v248, s[48:51], s8 offen sc1\ns_waitcnt lgkmcnt(2)\nv_mfma_f32_16x16x32_bf16 v[64:67], v[160:163], a[0:3], 0\n"
"ds_read_b64_tr_b16 v[168:169], v199\nTR 170,199,512\nv_mfma_f32_16x16x32_bf16 v[68:71], v[160:163], a[4:7], 0\nMU 176,0,0\nMU 177,1,1\n"
"v_mfma_f32_16x16x32_bf16 v[72:75], v[160:163], a[8:11], 0\nMU 178,2,2\nMU 179,3,3\nv_mfma_f32_16x16x32_bf16 v[76:79], v[160:163], a[12:15], 0\n"
"v_fmaak_f32 v176, v252, v176, 0xc0135761\nv_fmaak_f32 v177, v252, v177, 0xc0135761\ns_waitcnt lgkmcnt(2)\n"
"v_mfma_f32_16x16x32_bf16 v[80:83], v[164:167], a[0:3], 0\nTR 172,199,8192\nTR 174,199,8704\nv_mfma_f32_16x16x32_bf16 v[84:87], v[164:167], a[4:7], 0\n"
"v_fmaak_f32 v178, v252, v178, 0xc0135761\nv_fmaak_f32 v179, v252, v179, 0xc0135761\nv_mfma_f32_16x16x32_bf16 v[88:91], v[164:167], a[8:11], 0\n"
"MU 16,0,16\nMU 17,1,17\nv_mfma_f32_16x16x32_bf16 v[92:95], v[164:167], a[12:15], 0\nMU 18,2,18\nMU 19,3,19\ns_waitcnt lgkmcnt(2)\n"
"v_mfma_f32_16x16x32_bf16 v[96:99], v[168:171], a[0:3], 0\nTR 160,198,4096\nTR 162,198,4608\n"
"v_mfma_f32_16x16x32_bf16 v[100:103], v[168:171], a[4:7], 0\nMU 0,176,0\nMU 1,177,1\nv_mfma_f32_16x16x32_bf16 v[104:107], v[168:171], a[8:11], 0\n"
"MU 2,178,2\nMU 3,179,3\nv_mfma_f32_16x16x32_bf16 v[108:111], v[168:171], a[12:15], 0\nEX 0\ns_waitcnt lgkmcnt(2)\n"
"v_mfma_f32_16x16x32_bf16 v[112:115], v[172:175], a[0:3], 0\nTR 164,198,12288\nTR 166,198,12800\n"
"v_mfma_f32_16x16x32_bf16 v[116:119], v[172:175], a[4:7], 0\nEX 1\nv_mfma_f32_16x16x32_bf16 v[120:123], v[172:175], a[8:11], 0\nEX 2\n"
"v_mfma_f32_16x16x32_bf16 v[124:127], v[172:175], a[12:15], 0\nEX 3\ns_waitcnt lgkmcnt(2)\nMA 64,160,16\nTR 168,199,4096\nTR 170,199,4608\n"
"TR 172,199,12288\nTR 174,199,12800\nMA 68,160,20\nv_add_f32 v0, 1.0, v0\nv_add_f32 v1, 1.0, v1\nMA 72,160,24\nv_add_f32 v2, 1.0, v2\n"
"v_add_f32 v3, 1.0, v3\nMA 76,160,28\nv_rcp_f32 v0, v0\ns_waitcnt lgkmcnt(4)\nMA 80,164,16\nv_rcp_f32 v1, v1\nMA 84,164,20\nv_rcp_f32 v2, v2\n"
"MA 88,164,24\nv_rcp_f32 v3, v3\nMA 92,164,28\nMU 16,16,0\nMU 17,17,1\ns_waitcnt vmcnt(17) lgkmcnt(0)\ns_barrier\nMA 96,168,16\n"
"ds_read_b64_tr_b16 v[160:161], v196\nTR 162,196,512\nMA 100,168,20\ns_add_u32 m0, s4, 0x10000\ns_mul_i32 s86, s79, 0x1200\ns_add_u32 s86, s86, 0x80\n"
"buffer_load_dwordx4 v188, s[36:39], s86 offen lds\nMA 104,168,24\nMU 18,18,2\nMU 19,19,3\nMA 108,168,28\nMU 176,32,32\nMU 177,33,33\nMA 112,172,16\n"
"TR 164,196,8192\nTR 166,196,8704\nMA 116,172,20\nbuffer_load_dwordx4 v189, s[36:39], s86 offen offset:1024 lds\nMU 178,34,34\nMA 120,172,24\n"
"MU 179,35,35\nv_fmaak_f32 v176, v252, v176, 0xc0135761\nMA 124,172,28\nv_fmaak_f32 v177, v252, v177, 0xc0135761\n"
"v_fmaak_f32 v178, v252, v178, 0xc0135761\ns_waitcnt lgkmcnt(2)\nMA 64,160,32\nds_read_b64_tr_b16 v[168:169], v197\nTR 170,197,512\nMA 68,160,36\n"
"buffer_load_dwordx4 v190, s[36:39], s86 offen offset:2048 lds\nv_fmaak_f32 v179, v252, v179, 0xc0135761\nMA 72,160,40\nMU 48,32,48\nMU 49,33,49\n"
"MA 76,160,44\nMU 50,34,50\nMU 51,35,51\ns_waitcnt lgkmcnt(2)\nMA 80,164,32\nTR 172,197,8192\nTR 174,197,8704\nMA 84,164,36\n"
"buffer_load_dwordx4 v191, s[36:39], s86 offen offset:3072 lds\nMU 32,176,32\nMA 88,164,40\nMU 33,177,33\nMU 34,178,34\nMA 92,164,44\nMU 35,179,35\n"
"EX 32\ns_waitcnt lgkmcnt(2)\nMA 96,168,32\nTR 160,196,4096\nTR 162,196,4608\nMA 100,168,36\nEX 33\nMA 104,168,40\nEX 34\nMA 108,168,44\nEX 35\n"
"s_waitcnt lgkmcnt(2)\nMA 112,172,32\nTR 164,196,12288\nTR 166,196,12800\nMA 116,172,36\nv_add_f32 v32, 1.0, v32\nv_add_f32 v33, 1.0, v33\n"
"MA 120,172,40\nv_add_f32 v34, 1.0, v34\nv_add_f32 v35, 1.0, v35\nMA 124,172,44\nv_rcp_f32 v32, v32\ns_waitcnt lgkmcnt(2)\nMA 64,160,48\n"
"TR 168,197,4096\nTR 170,197,4608\nTR 172,197,12288\nTR 174,197,12800\nMA 68,160,52\nv_rcp_f32 v33, v33\nMA 72,160,56\nv_rcp_f32 v34, v34\n"
"MA 76,160,60\nv_rcp_f32 v35, v35\ns_waitcnt lgkmcnt(4)\nMA 80,164,48\nMU 48,48,32\nMU 49,49,33\nMA 84,164,52\nMU 50,50,34\nMU 51,51,35\nMA 88,164,56\n"
"v_cvt_pk_bf16_f32 v0, v16, v17\nv_cvt_pk_bf16_f32 v1, v18, v19\nMA 92,164,60\nv_cvt_pk_bf16_f32 v2, v48, v49\nv_cvt_pk_bf16_f32 v3, v50, v51\n"
"s_waitcnt vmcnt(16) lgkmcnt(0)\ns_barrier\nMA 96,168,48\nTR 160,196,16384\nTR 162,196,16896\nMA 100,168,52\ns_add_u32 m0, s4, 0\n"
"s_mul_i32 s86, s80, 0x1200\ns_add_u32 s86, s86, 0x80\nbuffer_load_dwordx4 v188, s[36:39], s86 offen lds\nMA 104,168,56\nds_write_b128 v206, v[0:3]\n"
"MU 176,4,4\nMA 108,168,60\nMU 177,5,5\nMU 178,6,6\nMA 112,172,48\nTR 164,196,24576\nTR 166,196,25088\nMA 116,172,52\n"
"buffer_load_dwordx4 v189, s[36:39], s86 offen offset:1024 lds\nMU 179,7,7\nMA 120,172,56\nv_fmaak_f32 v176, v252, v176, 0xc0135761\n"
"v_fmaak_f32 v177, v252, v177, 0xc0135761\nMA 124,172,60\nv_fmaak_f32 v178, v252, v178, 0xc0135761\nv_fmaak_f32 v179, v252, v179, 0xc0135761\n"
"s_waitcnt lgkmcnt(3)\nMA 64,160,64\nTR 168,197,16384\nTR 170,197,16896\nMA 68,160,68\nbuffer_load_dwordx4 v190, s[36:39], s86 offen offset:2048 lds\n"
"MU 20,4,20\nMA 72,160,72\nMU 21,5,21\nMU 22,6,22\nMA 76,160,76\nMU 23,7,23\nMU 4,176,4\ns_waitcnt lgkmcnt(2)\nMA 80,164,64\nTR 172,197,24576\n"
"TR 174,197,25088\nMA 84,164,68\nbuffer_load_dwordx4 v191, s[36:39], s86 offen offset:3072 lds\nMU 5,177,5\nMA 88,164,72\nMU 6,178,6\nMU 7,179,7\n"
"MA 92,164,76\nEX 4\ns_waitcnt lgkmcnt(2)\nMA 96,168,64\nTR 160,196,20480\nTR 162,196,20992\nMA 100,168,68\nEX 5\nMA 104,168,72\nEX 6\nMA 108,168,76\n"
"EX 7\ns_waitcnt lgkmcnt(2)\nMA 112,172,64\nTR 164,196,28672\nTR 166,196,29184\nMA 116,172,68\nv_add_f32 v4, 1.0, v4\nv_add_f32 v5, 1.0, v5\n"
"MA 120,172,72\nv_add_f32 v6, 1.0, v6\nv_add_f32 v7, 1.0, v7\nMA 124,172,76\nv_rcp_f32 v4, v4\ns_waitcnt lgkmcnt(2)\nMA 64,160,80\nTR 168,197,20480\n"
"TR 170,197,20992\nTR 172,197,28672\nTR 174,197,29184\nMA 68,160,84\nv_rcp_f32 v5, v5\nMA 72,160,88\nv_rcp_f32 v6, v6\nMA 76,160,92\nv_rcp_f32 v7, v7\n"
"s_waitcnt lgkmcnt(4)\nMA 80,164,80\nMU 20,20,4\nMU 21,21,5\nMA 84,164,84\nMU 22,22,6\nMU 23,23,7\nMA 88,164,88\nMU 176,36,36\nMU 177,37,37\n"
"MA 92,164,92\nMU 178,38,38\nMU 179,39,39\ns_waitcnt vmcnt(16) lgkmcnt(0)\ns_barrier\nMA 96,168,80\nTR 160,196,32768\nTR 162,196,33280\nMA 100,168,84\n"
"s_add_u32 m0, s4, 0x4000\ns_mul_i32 s86, s81, 0x1200\ns_add_u32 s86, s86, 0x80\nbuffer_load_dwordx4 v188, s[36:39], s86 offen lds\nMA 104,168,88\n"
"v_fmaak_f32 v176, v252, v176, 0xc0135761\nv_fmaak_f32 v177, v252, v177, 0xc0135761\nMA 108,168,92\nv_fmaak_f32 v178, v252, v178, 0xc0135761\n"
"v_fmaak_f32 v179, v252, v179, 0xc0135761\nMA 112,172,80\nTR 164,196,40960\nTR 166,196,41472\nMA 116,172,84\n"
"buffer_load_dwordx4 v189, s[36:39], s86 offen offset:1024 lds\nMU 52,36,52\nMA 120,172,88\nMU 53,37,53\nMU 54,38,54\nMA 124,172,92\nMU 55,39,55\n"
"MU 36,176,36\ns_waitcnt lgkmcnt(2)\nMA 64,160,96\nTR 168,197,32768\nTR 170,197,33280\nMA 68,160,100\n"
"buffer_load_dwordx4 v190, s[36:39], s86 offen offset:2048 lds\nMU 37,177,37\nMA 72,160,104\nMU 38,178,38\nMU 39,179,39\nMA 76,160,108\nEX 36\n"
"s_waitcnt lgkmcnt(2)\nMA 80,164,96\nTR 172,197,40960\nTR 174,197,41472\nMA 84,164,100\nbuffer_load_dwordx4 v191, s[36:39], s86 offen offset:3072 lds\n"
"EX 37\nMA 88,164,104\nEX 38\nMA 92,164,108\nEX 39\ns_waitcnt lgkmcnt(2)\nMA 96,168,96\nTR 160,196,36864\nTR 162,196,37376\nMA 100,168,100\n"
"v_add_f32 v36, 1.0, v36\nv_add_f32 v37, 1.0, v37\nMA 104,168,104\nv_add_f32 v38, 1.0, v38\nv_add_f32 v39, 1.0, v39\nMA 108,168,108\n"
"v_rcp_f32 v36, v36\ns_waitcnt lgkmcnt(2)\nMA 112,172,96\nTR 164,196,45056\nTR 166,196,45568\nMA 116,172,100\nv_rcp_f32 v37, v37\nMA 120,172,104\n"
"v_rcp_f32 v38, v38\nMA 124,172,108\nv_rcp_f32 v39, v39\ns_waitcnt lgkmcnt(2)\nMA 64,160,112\nTR 168,197,36864\nTR 170,197,37376\nTR 172,197,45056\n"
"TR 174,197,45568\nMA 68,160,116\nMU 52,52,36\nMU 53,53,37\nMA 72,160,120\nMU 54,54,38\nMU 55,55,39\nMA 76,160,124\nv_cvt_pk_bf16_f32 v4, v20, v21\n"
"v_cvt_pk_bf16_f32 v5, v22, v23\ns_waitcnt lgkmcnt(4)\nMA 80,164,112\nv_cvt_pk_bf16_f32 v6, v52, v53\nv_cvt_pk_bf16_f32 v7, v54, v55\nMA 84,164,116\n"
"ds_write_b128 v206, v[4:7] offset:1024\nMU 176,8,8\nMA 88,164,120\nMU 177,9,9\nMU 178,10,10\nMA 92,164,124\nMU 179,11,11\n"
"v_fmaak_f32 v176, v252, v176, 0xc0135761\ns_waitcnt vmcnt(16) lgkmcnt(0)\ns_barrier\nMA 96,168,112\nTR 160,196,49152\nTR 162,196,49664\n"
"MA 100,168,116\ns_add_u32 m0, s4, 0x8000\ns_mul_i32 s86, s19, 0x1200\ns_add_u32 s86, s86, 0x80\nbuffer_load_dwordx4 v188, s[36:39], s86 offen lds\n"
"MA 104,168,120\nv_fmaak_f32 v177, v252, v177, 0xc0135761\nv_fmaak_f32 v178, v252, v178, 0xc0135761\nMA 108,168,124\n"
"v_fmaak_f32 v179, v252, v179, 0xc0135761\nMU 24,8,24\nMA 112,172,112\nTR 164,196,57344\nTR 166,196,57856\nMA 116,172,116\n"
"buffer_load_dwordx4 v189, s[36:39], s86 offen offset:1024 lds\nMU 25,9,25\nMA 120,172,120\nMU 26,10,26\nMU 27,11,27\nMA 124,172,124\nMU 8,176,8\n"
"MU 9,177,9\ns_waitcnt lgkmcnt(2)\nMA 64,160,128\nTR 168,197,49152\nTR 170,197,49664\nMA 68,160,132\n"
"buffer_load_dwordx4 v190, s[36:39], s86 offen offset:2048 lds\nMU 10,178,10\nMA 72,160,136\nMU 11,179,11\nEX 8\nMA 76,160,140\nEX 9\n"
"s_waitcnt lgkmcnt(2)\nMA 80,164,128\nTR 172,197,57344\nTR 174,197,57856\nMA 84,164,132\n"
"buffer_load_dwordx4 v191, s[36:39], s86 offen offset:3072 lds\nEX 10\nMA 88,164,136\nEX 11\nMA 92,164,140\nv_add_f32 v8, 1.0, v8\n"
"v_add_f32 v9, 1.0, v9\ns_waitcnt lgkmcnt(2)\nMA 96,168,128\nTR 160,196,53248\nTR 162,196,53760\nMA 100,168,132\nv_add_f32 v10, 1.0, v10\n"
"v_add_f32 v11, 1.0, v11\nMA 104,168,136\nv_rcp_f32 v8, v8\nMA 108,168,140\nv_rcp_f32 v9, v9\ns_waitcnt lgkmcnt(2)\nMA 112,172,128\nTR 164,196,61440\n"
"TR 166,196,61952\nMA 116,172,132\nv_rcp_f32 v10, v10\nMA 120,172,136\nv_rcp_f32 v11, v11\nMA 124,172,140\nMU 24,24,8\nMU 25,25,9\n"
"s_waitcnt lgkmcnt(2)\nMA 64,160,144\nTR 168,197,53248\nTR 170,197,53760\nTR 172,197,61440\nTR 174,197,61952\nMA 68,160,148\nMU 26,26,10\nMU 27,27,11\n"
"MA 72,160,152\nMU 176,40,40\nMU 177,41,41\nMA 76,160,156\nMU 178,42,42\nMU 179,43,43\ns_waitcnt lgkmcnt(4)\nMA 80,164,144\n"
"v_fmaak_f32 v176, v252, v176, 0xc0135761\nv_fmaak_f32 v177, v252, v177, 0xc0135761\nMA 84,164,148\nv_fmaak_f32 v178, v252, v178, 0xc0135761\n"
"v_fmaak_f32 v179, v252, v179, 0xc0135761\nMA 88,164,152\nMU 56,40,56\nMU 57,41,57\nMA 92,164,156\nMU 58,42,58\nMU 59,43,59\n"
"s_waitcnt vmcnt(12) lgkmcnt(0)\ns_barrier\nMA 96,168,144\nds_read_b64_tr_b16 v[160:161], v198\nTR 162,198,512\nMA 100,168,148\n"
"s_add_u32 m0, s4, 0xc000\ns_mul_i32 s86, s0, 0x1200\ns_add_u32 s86, s86, 0x100\nbuffer_load_dwordx4 v188, s[36:39], s86 offen lds\nMA 104,168,152\n"
"MU 40,176,40\nMU 41,177,41\nMA 108,168,156\nMU 42,178,42\nMU 43,179,43\nMA 112,172,144\nTR 164,198,8192\nTR 166,198,8704\nMA 116,172,148\n"
"buffer_load_dwordx4 v189, s[36:39], s86 offen offset:1024 lds\nEX 40\nMA 120,172,152\nEX 41\nMA 124,172,156\nEX 42\ns_waitcnt lgkmcnt(2)\n"
"MA 64,160,160\nds_read_b64_tr_b16 v[168:169], v199\nTR 170,199,512\nMA 68,160,164\nbuffer_load_dwordx4 v190, s[36:39], s86 offen offset:2048 lds\n"
"EX 43\nMA 72,160,168\nv_add_f32 v40, 1.0, v40\nv_add_f32 v41, 1.0, v41\nMA 76,160,172\nv_add_f32 v42, 1.0, v42\nv_add_f32 v43, 1.0, v43\n"
"s_waitcnt lgkmcnt(2)\nMA 80,164,160\nTR 172,199,8192\nTR 174,199,8704\nMA 84,164,164\nbuffer_load_dwordx4 v191, s[36:39], s86 offen offset:3072 lds\n"
"v_rcp_f32 v40, v40\nMA 88,164,168\nv_rcp_f32 v41, v41\nMA 92,164,172\nv_rcp_f32 v42, v42\ns_waitcnt lgkmcnt(2)\nMA 96,168,160\nTR 160,198,4096\n"
"TR 162,198,4608\nMA 100,168,164\nv_rcp_f32 v43, v43\nMA 104,168,168\nMU 56,56,40\nMU 57,57,41\nMA 108,168,172\nMU 58,58,42\nMU 59,59,43\n"
"s_waitcnt lgkmcnt(2)\nMA 112,172,160\nTR 164,198,12288\nTR 166,198,12800\nMA 116,172,164\nv_cvt_pk_bf16_f32 v8, v24, v25\n"
"v_cvt_pk_bf16_f32 v9, v26, v27\nMA 120,172,168\nv_cvt_pk_bf16_f32 v10, v56, v57\nv_cvt_pk_bf16_f32 v11, v58, v59\nMA 124,172,172\n"
"ds_write_b128 v206, v[8:11] offset:2048\nMU 176,12,12\ns_waitcnt lgkmcnt(3)\nMA 64,160,176\nTR 168,199,4096\nTR 170,199,4608\nTR 172,199,12288\n"
"TR 174,199,12800\nMA 68,160,180\nMU 177,13,13\nMU 178,14,14\nMA 72,160,184\nMU 179,15,15\nv_fmaak_f32 v176, v252, v176, 0xc0135761\nMA 76,160,188\n"
"v_fmaak_f32 v177, v252, v177, 0xc0135761\nv_fmaak_f32 v178, v252, v178, 0xc0135761\ns_waitcnt lgkmcnt(5)\nMA 80,164,176\n"
"v_fmaak_f32 v179, v252, v179, 0xc0135761\nMU 28,12,28\nMA 84,164,180\nMU 29,13,29\nMU 30,14,30\nMA 88,164,184\nMU 31,15,31\nMU 12,176,12\n"
"MA 92,164,188\nMU 13,177,13\nMU 14,178,14\ns_waitcnt vmcnt(12) lgkmcnt(0)\ns_barrier\nMA 96,168,176\nds_read_b64_tr_b16 v[160:161], v196\n"
"TR 162,196,512\nMA 100,168,180\ns_add_u32 m0, s4, 0x10000\ns_mul_i32 s86, s1, 0x1200\ns_add_u32 s86, s86, 0x100\n"
"buffer_load_dwordx4 v188, s[36:39], s86 offen lds\nMA 104,168,184\nMU 15,179,15\nEX 12\nMA 108,168,188\nEX 13\nMA 112,172,176\nTR 164,196,8192\n"
"TR 166,196,8704\nMA 116,172,180\nbuffer_load_dwordx4 v189, s[36:39], s86 offen offset:1024 lds\nEX 14\nMA 120,172,184\nEX 15\nMA 124,172,188\n"
"v_add_f32 v12, 1.0, v12\nv_add_f32 v13, 1.0, v13\ns_waitcnt lgkmcnt(2)\nMA 64,160,192\nds_read_b64_tr_b16 v[168:169], v197\nTR 170,197,512\n"
"MA 68,160,196\nbuffer_load_dwordx4 v190, s[36:39], s86 offen offset:2048 lds\nv_add_f32 v14, 1.0, v14\nMA 72,160,200\nv_add_f32 v15, 1.0, v15\n"
"v_rcp_f32 v12, v12\nMA 76,160,204\nv_rcp_f32 v13, v13\ns_waitcnt lgkmcnt(2)\nMA 80,164,192\nTR 172,197,8192\nTR 174,197,8704\nMA 84,164,196\n"
"buffer_load_dwordx4 v191, s[36:39], s86 offen offset:3072 lds\nv_rcp_f32 v14, v14\nMA 88,164,200\nv_rcp_f32 v15, v15\nMA 92,164,204\nMU 28,28,12\n"
"MU 29,29,13\ns_waitcnt lgkmcnt(2)\nMA 96,168,192\nTR 160,196,4096\nTR 162,196,4608\nMA 100,168,196\nMU 30,30,14\nMU 31,31,15\nMA 104,168,200\n"
"MU 176,44,44\nMU 177,45,45\nMA 108,168,204\nMU 178,46,46\nMU 179,47,47\ns_waitcnt lgkmcnt(2)\nMA 112,172,192\nTR 164,196,12288\nTR 166,196,12800\n"
"MA 116,172,196\nv_fmaak_f32 v176, v252, v176, 0xc0135761\nv_fmaak_f32 v177, v252, v177, 0xc0135761\nMA 120,172,200\n"
"v_fmaak_f32 v178, v252, v178, 0xc0135761\nv_fmaak_f32 v179, v252, v179, 0xc0135761\nMA 124,172,204\nMU 60,44,60\nMU 61,45,61\ns_waitcnt lgkmcnt(2)\n"
"MA 64,160,208\nTR 168,197,4096\nTR 170,197,4608\nTR 172,197,12288\nTR 174,197,12800\nMA 68,160,212\nMU 62,46,62\nMU 63,47,63\nMA 72,160,216\n"
"MU 44,176,44\nMU 45,177,45\nMA 76,160,220\nMU 46,178,46\nMU 47,179,47\ns_waitcnt lgkmcnt(4)\nMA 80,164,208\nEX 44\nMA 84,164,212\nEX 45\n"
"MA 88,164,216\nEX 46\nMA 92,164,220\nEX 47\ns_waitcnt vmcnt(12) lgkmcnt(0)\ns_barrier\nMA 96,168,208\nTR 160,196,16384\nTR 162,196,16896\n"
"MA 100,168,212\ns_add_u32 m0, s4, 0\ns_mul_i32 s86, s2, 0x1200\ns_add_u32 s86, s86, 0x100\nbuffer_load_dwordx4 v188, s[36:39], s86 offen lds\n"
"MA 104,168,216\nv_add_f32 v44, 1.0, v44\nv_add_f32 v45, 1.0, v45\nMA 108,168,220\nv_add_f32 v46, 1.0, v46\nv_add_f32 v47, 1.0, v47\nMA 112,172,208\n"
"TR 164,196,24576\nTR 166,196,25088\nMA 116,172,212\nbuffer_load_dwordx4 v189, s[36:39], s86 offen offset:1024 lds\nv_rcp_f32 v44, v44\n"
"MA 120,172,216\nv_rcp_f32 v45, v45\nMA 124,172,220\nv_rcp_f32 v46, v46\ns_waitcnt lgkmcnt(2)\nMA 64,160,224\nTR 168,197,16384\nTR 170,197,16896\n"
"MA 68,160,228\nbuffer_load_dwordx4 v190, s[36:39], s86 offen offset:2048 lds\nv_rcp_f32 v47, v47\nMA 72,160,232\nMU 60,60,44\nMU 61,61,45\n"
"MA 76,160,236\nMU 62,62,46\nMU 63,63,47\ns_waitcnt lgkmcnt(2)\nMA 80,164,224\nTR 172,197,24576\nTR 174,197,25088\nMA 84,164,228\n"
"buffer_load_dwordx4 v191, s[36:39], s86 offen offset:3072 lds\nv_cvt_pk_bf16_f32 v12, v28, v29\nMA 88,164,232\nv_cvt_pk_bf16_f32 v13, v30, v31\n"
"v_cvt_pk_bf16_f32 v14, v60, v61\nMA 92,164,236\nv_cvt_pk_bf16_f32 v15, v62, v63\nds_write_b128 v206, v[12:15] offset:3072\ns_waitcnt lgkmcnt(3)\n"
"MA 96,168,224\nTR 160,196,20480\nTR 162,196,20992\nMA 100,168,228\nMA 104,168,232\nMA 108,168,236\ns_waitcnt lgkmcnt(3)\nMA 112,172,224\n"
"TR 164,196,28672\nTR 166,196,29184\nMA 116,172,228\nMA 120,172,232\nMA 124,172,236\ns_waitcnt lgkmcnt(2)\nMA 64,160,240\nTR 168,197,20480\n"
"TR 170,197,20992\nTR 172,197,28672\nTR 174,197,29184\nMA 68,160,244\nMA 72,160,248\nMA 76,160,252\ns_waitcnt lgkmcnt(4)\nMA 80,164,240\n"
"MA 84,164,244\nMA 88,164,248\nMA 92,164,252\ns_waitcnt vmcnt(12) lgkmcnt(0)\ns_barrier\nMA 96,168,240\nTR 160,196,32768\nTR 162,196,33280\n"
"MA 100,168,244\ns_add_u32 m0, s4, 0x4000\ns_mul_i32 s86, s3, 0x1200\ns_add_u32 s86, s86, 0x100\nbuffer_load_dwordx4 v188, s[36:39], s86 offen lds\n"
"MA 104,168,248\nMA 108,168,252\nMA 112,172,240\nTR 164,196,40960\nTR 166,196,41472\nMA 116,172,244\n"
"buffer_load_dwordx4 v189, s[36:39], s86 offen offset:1024 lds\nMA 120,172,248\nMA 124,172,252\ns_waitcnt lgkmcnt(2)\n"
"v_mfma_f32_16x16x32_bf16 v[64:67], v[160:163], v[128:131], v[64:67]\nTR 168,197,32768\nTR 170,197,33280\n"
"v_mfma_f32_16x16x32_bf16 v[68:71], v[160:163], v[132:135], v[68:71]\nbuffer_load_dwordx4 v190, s[36:39], s86 offen offset:2048 lds\n"
"v_mfma_f32_16x16x32_bf16 v[72:75], v[160:163], v[136:139], v[72:75]\nv_mfma_f32_16x16x32_bf16 v[76:79], v[160:163], v[140:143], v[76:79]\n"
"s_waitcnt lgkmcnt(2)\nv_mfma_f32_16x16x32_bf16 v[80:83], v[164:167], v[128:131], v[80:83]\nTR 172,197,40960\nTR 174,197,41472\n"
"v_mfma_f32_16x16x32_bf16 v[84:87], v[164:167], v[132:135], v[84:87]\nbuffer_load_dwordx4 v191, s[36:39], s86 offen offset:3072 lds\n"
"v_mfma_f32_16x16x32_bf16 v[88:91], v[164:167], v[136:139], v[88:91]\nv_mfma_f32_16x16x32_bf16 v[92:95], v[164:167], v[140:143], v[92:95]\n"
"s_waitcnt lgkmcnt(2)\nv_mfma_f32_16x16x32_bf16 v[96:99], v[168:171], v[128:131], v[96:99]\nTR 160,196,36864\nTR 162,196,37376\n"
"v_mfma_f32_16x16x32_bf16 v[100:103], v[168:171], v[132:135], v[100:103]\nv_mfma_f32_16x16x32_bf16 v[104:107], v[168:171], v[136:139], v[104:107]\n"
"v_mfma_f32_16x16x32_bf16 v[108:111], v[168:171], v[140:143], v[108:111]\ns_waitcnt lgkmcnt(2)\n"
"v_mfma_f32_16x16x32_bf16 v[112:115], v[172:175], v[128:131], v[112:115]\nTR 164,196,45056\nTR 166,196,45568\n"
"v_mfma_f32_16x16x32_bf16 v[116:119], v[172:175], v[132:135], v[116:119]\nv_mfma_f32_16x16x32_bf16 v[120:123], v[172:175], v[136:139], v[120:123]\n"
"v_mfma_f32_16x16x32_bf16 v[124:127], v[172:175], v[140:143], v[124:127]\ns_waitcnt lgkmcnt(2)\n"
"v_mfma_f32_16x16x32_bf16 v[64:67], v[160:163], v[144:147], v[64:67]\nTR 168,197,36864\nTR 170,197,37376\nTR 172,197,45056\nTR 174,197,45568\n"
"v_mfma_f32_16x16x32_bf16 v[68:71], v[160:163], v[148:151], v[68:71]\nv_mfma_f32_16x16x32_bf16 v[72:75], v[160:163], v[152:155], v[72:75]\n"
"v_mfma_f32_16x16x32_bf16 v[76:79], v[160:163], v[156:159], v[76:79]\ns_waitcnt lgkmcnt(4)\n"
"v_mfma_f32_16x16x32_bf16 v[80:83], v[164:167], v[144:147], v[80:83]\nv_mfma_f32_16x16x32_bf16 v[84:87], v[164:167], v[148:151], v[84:87]\n"
"v_mfma_f32_16x16x32_bf16 v[88:91], v[164:167], v[152:155], v[88:91]\nv_mfma_f32_16x16x32_bf16 v[92:95], v[164:167], v[156:159], v[92:95]\n"
"s_waitcnt vmcnt(12) lgkmcnt(0)\ns_barrier\nv_mfma_f32_16x16x32_bf16 v[96:99], v[168:171], v[144:147], v[96:99]\nTR 160,196,49152\nTR 162,196,49664\n"
"v_mfma_f32_16x16x32_bf16 v[100:103], v[168:171], v[148:151], v[100:103]\ns_add_u32 m0, s4, 0x8000\ns_mul_i32 s86, s78, 0x1200\n"
"s_add_u32 s86, s86, 0x100\nbuffer_load_dwordx4 v188, s[36:39], s86 offen lds\n"
"v_mfma_f32_16x16x32_bf16 v[104:107], v[168:171], v[152:155], v[104:107]\nv_mfma_f32_16x16x32_bf16 v[108:111], v[168:171], v[156:159], v[108:111]\n"
"v_mfma_f32_16x16x32_bf16 v[112:115], v[172:175], v[144:147], v[112:115]\nTR 164,196,57344\nTR 166,196,57856\n"
"v_mfma_f32_16x16x32_bf16 v[116:119], v[172:175], v[148:151], v[116:119]\nbuffer_load_dwordx4 v189, s[36:39], s86 offen offset:1024 lds\n"
"v_mfma_f32_16x16x32_bf16 v[120:123], v[172:175], v[152:155], v[120:123]\nv_mfma_f32_16x16x32_bf16 v[124:127], v[172:175], v[156:159], v[124:127]\n"
"s_waitcnt lgkmcnt(2)\nv_mfma_f32_16x16x32_bf16 v[0:3], v[160:163], a[0:3], 0\nTR 168,197,49152\nTR 170,197,49664\n"
"v_mfma_f32_16x16x32_bf16 v[4:7], v[160:163], a[4:7], 0\nbuffer_load_dwordx4 v190, s[36:39], s86 offen offset:2048 lds\nMU 176,64,64\n"
"v_mfma_f32_16x16x32_bf16 v[8:11], v[160:163], a[8:11], 0\nMU 177,65,65\nMU 178,66,66\nv_mfma_f32_16x16x32_bf16 v[12:15], v[160:163], a[12:15], 0\n"
"MU 179,67,67\nv_fmaak_f32 v176, v252, v176, 0xc0135761\ns_waitcnt lgkmcnt(2)\nv_mfma_f32_16x16x32_bf16 v[16:19], v[164:167], a[0:3], 0\n"
"TR 172,197,57344\nTR 174,197,57856\nv_mfma_f32_16x16x32_bf16 v[20:23], v[164:167], a[4:7], 0\n"
"buffer_load_dwordx4 v191, s[36:39], s86 offen offset:3072 lds\nv_fmaak_f32 v177, v252, v177, 0xc0135761\n"
"v_mfma_f32_16x16x32_bf16 v[24:27], v[164:167], a[8:11], 0\nv_fmaak_f32 v178, v252, v178, 0xc0135761\nv_fmaak_f32 v179, v252, v179, 0xc0135761\n"
"v_mfma_f32_16x16x32_bf16 v[28:31], v[164:167], a[12:15], 0\nMU 80,64,80\nMU 81,65,81\ns_waitcnt lgkmcnt(2)\n"
"v_mfma_f32_16x16x32_bf16 v[32:35], v[168:171], a[0:3], 0\nTR 160,196,53248\nTR 162,196,53760\n"
"v_mfma_f32_16x16x32_bf16 v[36:39], v[168:171], a[4:7], 0\nMU 82,66,82\nMU 83,67,83\nv_mfma_f32_16x16x32_bf16 v[40:43], v[168:171], a[8:11], 0\n"
"MU 64,176,64\nMU 65,177,65\nv_mfma_f32_16x16x32_bf16 v[44:47], v[168:171], a[12:15], 0\nMU 66,178,66\nMU 67,179,67\ns_waitcnt lgkmcnt(2)\n"
"v_mfma_f32_16x16x32_bf16 v[48:51], v[172:175], a[0:3], 0\nTR 164,196,61440\nTR 166,196,61952\n"
"v_mfma_f32_16x16x32_bf16 v[52:55], v[172:175], a[4:7], 0\nEX 64\nv_mfma_f32_16x16x32_bf16 v[56:59], v[172:175], a[8:11], 0\nEX 65\n"
"v_mfma_f32_16x16x32_bf16 v[60:63], v[172:175], a[12:15], 0\nEX 66\ns_waitcnt lgkmcnt(2)\nMA 0,160,16\nTR 168,197,53248\nTR 170,197,53760\n"
"TR 172,197,61440\nTR 174,197,61952\nMA 4,160,20\nEX 67\nMA 8,160,24\nv_add_f32 v64, 1.0, v64\nv_add_f32 v65, 1.0, v65\nMA 12,160,28\n"
"v_add_f32 v66, 1.0, v66\nv_add_f32 v67, 1.0, v67\ns_waitcnt lgkmcnt(4)\nMA 16,164,16\nv_rcp_f32 v64, v64\nMA 20,164,20\nv_rcp_f32 v65, v65\n"
"MA 24,164,24\nv_rcp_f32 v66, v66\nMA 28,164,28\nv_rcp_f32 v67, v67\ns_waitcnt vmcnt(12) lgkmcnt(0)\ns_barrier\nMA 32,168,16\n"
"ds_read_b64_tr_b16 v[160:161], v198\nTR 162,198,512\nMA 36,168,20\ns_add_u32 m0, s4, 0xc000\ns_mul_i32 s86, s79, 0x1200\ns_add_u32 s86, s86, 0x100\n"
"buffer_load_dwordx4 v188, s[36:39], s86 offen lds\nMA 40,168,24\nMU 80,80,64\nMU 81,81,65\nMA 44,168,28\nMU 82,82,66\nMU 83,83,67\nMA 48,172,16\n"
"TR 164,198,8192\nTR 166,198,8704\nMA 52,172,20\nbuffer_load_dwordx4 v189, s[36:39], s86 offen offset:1024 lds\nMU 176,96,96\nMA 56,172,24\n"
"MU 177,97,97\nMU 178,98,98\nMA 60,172,28\nMU 179,99,99\nv_fmaak_f32 v176, v252, v176, 0xc0135761\ns_waitcnt lgkmcnt(2)\nMA 0,160,32\n"
"ds_read_b64_tr_b16 v[168:169], v199\nTR 170,199,512\nMA 4,160,36\nbuffer_load_dwordx4 v190, s[36:39], s86 offen offset:2048 lds\n"
"v_fmaak_f32 v177, v252, v177, 0xc0135761\nMA 8,160,40\nv_fmaak_f32 v178, v252, v178, 0xc0135761\nv_fmaak_f32 v179, v252, v179, 0xc0135761\n"
"MA 12,160,44\nMU 112,96,112\nMU 113,97,113\ns_waitcnt lgkmcnt(2)\nMA 16,164,32\nTR 172,199,8192\nTR 174,199,8704\nMA 20,164,36\n"
"buffer_load_dwordx4 v191, s[36:39], s86 offen offset:3072 lds\nMU 114,98,114\nMA 24,164,40\nMU 115,99,115\nMU 96,176,96\nMA 28,164,44\nMU 97,177,97\n"
"MU 98,178,98\ns_waitcnt lgkmcnt(2)\nMA 32,168,32\nTR 160,198,4096\nTR 162,198,4608\nMA 36,168,36\nMU 99,179,99\nEX 96\nMA 40,168,40\nEX 97\n"
"MA 44,168,44\nEX 98\ns_waitcnt lgkmcnt(2)\nMA 48,172,32\nTR 164,198,12288\nTR 166,198,12800\nMA 52,172,36\nEX 99\nMA 56,172,40\n"
"v_add_f32 v96, 1.0, v96\nv_add_f32 v97, 1.0, v97\nMA 60,172,44\nv_add_f32 v98, 1.0, v98\nv_add_f32 v99, 1.0, v99\ns_waitcnt lgkmcnt(2)\nMA 0,160,48\n"
"TR 168,199,4096\nTR 170,199,4608\nTR 172,199,12288\nTR 174,199,12800\nMA 4,160,52\nv_rcp_f32 v96, v96\nMA 8,160,56\nv_rcp_f32 v97, v97\nMA 12,160,60\n"
"v_rcp_f32 v98, v98\ns_waitcnt lgkmcnt(4)\nMA 16,164,48\nv_rcp_f32 v99, v99\nMA 20,164,52\nMU 112,112,96\nMU 113,113,97\nMA 24,164,56\nMU 114,114,98\n"
"MU 115,115,99\nMA 28,164,60\nv_cvt_pk_bf16_f32 v64, v80, v81\nv_cvt_pk_bf16_f32 v65, v82, v83\ns_waitcnt vmcnt(12) lgkmcnt(0)\ns_barrier\n"
"MA 32,168,48\nds_read_b64_tr_b16 v[160:161], v196\nTR 162,196,512\nMA 36,168,52\ns_add_u32 m0, s4, 0x10000\ns_mul_i32 s86, s80, 0x1200\n"
"s_add_u32 s86, s86, 0x100\nbuffer_load_dwordx4 v188, s[36:39], s86 offen lds\nMA 40,168,56\nv_cvt_pk_bf16_f32 v66, v112, v113\n"
"v_cvt_pk_bf16_f32 v67, v114, v115\nMA 44,168,60\nds_write_b128 v206, v[64:67] offset:8192\nMU 176,68,68\nMA 48,172,48\nTR 164,196,8192\n"
"TR 166,196,8704\nMA 52,172,52\nbuffer_load_dwordx4 v189, s[36:39], s86 offen offset:1024 lds\nMU 177,69,69\nMA 56,172,56\nMU 178,70,70\nMU 179,71,71\n"
"MA 60,172,60\nv_fmaak_f32 v176, v252, v176, 0xc0135761\nv_fmaak_f32 v177, v252, v177, 0xc0135761\ns_waitcnt lgkmcnt(3)\nMA 0,160,64\n"
"ds_read_b64_tr_b16 v[168:169], v197\nTR 170,197,512\nMA 4,160,68\nbuffer_load_dwordx4 v190, s[36:39], s86 offen offset:2048 lds\n"
"v_fmaak_f32 v178, v252, v178, 0xc0135761\nMA 8,160,72\nv_fmaak_f32 v179, v252, v179, 0xc0135761\nMU 84,68,84\nMA 12,160,76\nMU 85,69,85\nMU 86,70,86\n"
"s_waitcnt lgkmcnt(2)\nMA 16,164,64\nTR 172,197,8192\nTR 174,197,8704\nMA 20,164,68\nbuffer_load_dwordx4 v191, s[36:39], s86 offen offset:3072 lds\n"
"MU 87,71,87\nMA 24,164,72\nMU 68,176,68\nMU 69,177,69\nMA 28,164,76\nMU 70,178,70\nMU 71,179,71\ns_waitcnt lgkmcnt(2)\nMA 32,168,64\nTR 160,196,4096\n"
"TR 162,196,4608\nMA 36,168,68\nEX 68\nMA 40,168,72\nEX 69\nMA 44,168,76\nEX 70\ns_waitcnt lgkmcnt(2)\nMA 48,172,64\nTR 164,196,12288\n"
"TR 166,196,12800\nMA 52,172,68\nEX 71\nMA 56,172,72\nv_add_f32 v68, 1.0, v68\nv_add_f32 v69, 1.0, v69\nMA 60,172,76\nv_add_f32 v70, 1.0, v70\n"
"v_add_f32 v71, 1.0, v71\ns_waitcnt lgkmcnt(2)\nMA 0,160,80\nTR 168,197,4096\nTR 170,197,4608\nTR 172,197,12288\nTR 174,197,12800\nMA 4,160,84\n"
"v_rcp_f32 v68, v68\nMA 8,160,88\nv_rcp_f32 v69, v69\nMA 12,160,92\nv_rcp_f32 v70, v70\ns_waitcnt lgkmcnt(4)\nMA 16,164,80\nv_rcp_f32 v71, v71\n"
"MA 20,164,84\nMU 84,84,68\nMU 85,85,69\nMA 24,164,88\nMU 86,86,70\nMU 87,87,71\nMA 28,164,92\nMU 176,100,100\nMU 177,101,101\n"
"s_waitcnt vmcnt(12) lgkmcnt(0)\ns_barrier\nMA 32,168,80\nTR 160,196,16384\nTR 162,196,16896\nMA 36,168,84\ns_add_u32 m0, s4, 0\n"
"s_mul_i32 s86, s81, 0x1200\ns_add_u32 s86, s86, 0x100\nbuffer_load_dwordx4 v188, s[36:39], s86 offen lds\nMA 40,168,88\nMU 178,102,102\n"
"MU 179,103,103\nMA 44,168,92\nv_fmaak_f32 v176, v252, v176, 0xc0135761\nv_fmaak_f32 v177, v252, v177, 0xc0135761\nMA 48,172,80\nTR 164,196,24576\n"
"TR 166,196,25088\nMA 52,172,84\nbuffer_load_dwordx4 v189, s[36:39], s86 offen offset:1024 lds\nv_fmaak_f32 v178, v252, v178, 0xc0135761\n"
"MA 56,172,88\nv_fmaak_f32 v179, v252, v179, 0xc0135761\nMU 116,100,116\nMA 60,172,92\nMU 117,101,117\nMU 118,102,118\ns_waitcnt lgkmcnt(2)\n"
"MA 0,160,96\nTR 168,197,16384\nTR 170,197,16896\nMA 4,160,100\nbuffer_load_dwordx4 v190, s[36:39], s86 offen offset:2048 lds\nMU 119,103,119\n"
"MA 8,160,104\nMU 100,176,100\nMU 101,177,101\nMA 12,160,108\nMU 102,178,102\nMU 103,179,103\ns_waitcnt lgkmcnt(2)\nMA 16,164,96\nTR 172,197,24576\n"
"TR 174,197,25088\nMA 20,164,100\nbuffer_load_dwordx4 v191, s[36:39], s86 offen offset:3072 lds\nEX 100\nMA 24,164,104\nEX 101\nMA 28,164,108\nEX 102\n"
"s_waitcnt lgkmcnt(2)\nMA 32,168,96\nTR 160,196,20480\nTR 162,196,20992\nMA 36,168,100\nEX 103\nMA 40,168,104\nv_add_f32 v100, 1.0, v100\n"
"v_add_f32 v101, 1.0, v101\nMA 44,168,108\nv_add_f32 v102, 1.0, v102\nv_add_f32 v103, 1.0, v103\ns_waitcnt lgkmcnt(2)\nMA 48,172,96\nTR 164,196,28672\n"
"TR 166,196,29184\nMA 52,172,100\nv_rcp_f32 v100, v100\nMA 56,172,104\nv_rcp_f32 v101, v101\nMA 60,172,108\nv_rcp_f32 v102, v102\n"
"s_waitcnt lgkmcnt(2)\nMA 0,160,112\nTR 168,197,20480\nTR 170,197,20992\nTR 172,197,28672\nTR 174,197,29184\nMA 4,160,116\nv_rcp_f32 v103, v103\n"
"MA 8,160,120\nMU 116,116,100\nMU 117,117,101\nMA 12,160,124\nMU 118,118,102\nMU 119,119,103\ns_waitcnt lgkmcnt(4)\nMA 16,164,112\n"
"v_cvt_pk_bf16_f32 v68, v84, v85\nv_cvt_pk_bf16_f32 v69, v86, v87\nMA 20,164,116\nv_cvt_pk_bf16_f32 v70, v116, v117\n"
"v_cvt_pk_bf16_f32 v71, v118, v119\nMA 24,164,120\nds_write_b128 v206, v[68:71] offset:9216\nMU 176,72,72\nMA 28,164,124\nMU 177,73,73\nMU 178,74,74\n"
"s_waitcnt vmcnt(12) lgkmcnt(0)\ns_barrier\nMA 32,168,112\nTR 160,196,32768\nTR 162,196,33280\nMA 36,168,116\ns_add_u32 m0, s4, 0x4000\n"
"s_mul_i32 s86, s19, 0x1200\ns_add_u32 s86, s86, 0x100\nbuffer_load_dwordx4 v188, s[36:39], s86 offen lds\nMA 40,168,120\nMU 179,75,75\n"
"v_fmaak_f32 v176, v252, v176, 0xc0135761\nMA 44,168,124\nv_fmaak_f32 v177, v252, v177, 0xc0135761\nv_fmaak_f32 v178, v252, v178, 0xc0135761\n"
"MA 48,172,112\nTR 164,196,40960\nTR 166,196,41472\nMA 52,172,116\nbuffer_load_dwordx4 v189, s[36:39], s86 offen offset:1024 lds\n"
"v_fmaak_f32 v179, v252, v179, 0xc0135761\nMA 56,172,120\nMU 88,72,88\nMU 89,73,89\nMA 60,172,124\nMU 90,74,90\nMU 91,75,91\ns_waitcnt lgkmcnt(2)\n"
"MA 0,160,128\nTR 168,197,32768\nTR 170,197,33280\nMA 4,160,132\nbuffer_load_dwordx4 v190, s[36:39], s86 offen offset:2048 lds\nMU 72,176,72\n"
"MA 8,160,136\nMU 73,177,73\nMU 74,178,74\nMA 12,160,140\nMU 75,179,75\nEX 72\ns_waitcnt lgkmcnt(2)\nMA 16,164,128\nTR 172,197,40960\n"
"TR 174,197,41472\nMA 20,164,132\nbuffer_load_dwordx4 v191, s[36:39], s86 offen offset:3072 lds\nEX 73\nMA 24,164,136\nEX 74\nMA 28,164,140\nEX 75\n"
"s_waitcnt lgkmcnt(2)\nMA 32,168,128\nTR 160,196,36864\nTR 162,196,37376\nMA 36,168,132\nv_add_f32 v72, 1.0, v72\nv_add_f32 v73, 1.0, v73\n"
"MA 40,168,136\nv_add_f32 v74, 1.0, v74\nv_add_f32 v75, 1.0, v75\nMA 44,168,140\nv_rcp_f32 v72, v72\ns_waitcnt lgkmcnt(2)\nMA 48,172,128\n"
"TR 164,196,45056\nTR 166,196,45568\nMA 52,172,132\nv_rcp_f32 v73, v73\nMA 56,172,136\nv_rcp_f32 v74, v74\nMA 60,172,140\nv_rcp_f32 v75, v75\n"
"s_waitcnt lgkmcnt(2)\nMA 0,160,144\nTR 168,197,36864\nTR 170,197,37376\nTR 172,197,45056\nTR 174,197,45568\nMA 4,160,148\nMU 88,88,72\nMU 89,89,73\n"
"MA 8,160,152\nMU 90,90,74\nMU 91,91,75\nMA 12,160,156\nMU 176,104,104\nMU 177,105,105\ns_waitcnt lgkmcnt(4)\nMA 16,164,144\nMU 178,106,106\n"
"MU 179,107,107\nMA 20,164,148\nv_fmaak_f32 v176, v252, v176, 0xc0135761\nv_fmaak_f32 v177, v252, v177, 0xc0135761\nMA 24,164,152\n"
"v_fmaak_f32 v178, v252, v178, 0xc0135761\nv_fmaak_f32 v179, v252, v179, 0xc0135761\nMA 28,164,156\nMU 120,104,120\nMU 121,105,121\n"
"s_waitcnt vmcnt(12) lgkmcnt(0)\ns_barrier\nMA 32,168,144\nTR 160,196,49152\nTR 162,196,49664\nMA 36,168,148\ns_add_u32 m0, s4, 0x8000\n"
"s_mul_i32 s86, s0, 0x1200\ns_add_u32 s86, s86, 0x180\nbuffer_load_dwordx4 v188, s[36:39], s86 offen lds\nMA 40,168,152\nMU 122,106,122\n"
"MU 123,107,123\nMA 44,168,156\nMU 104,176,104\nMU 105,177,105\nMA 48,172,144\nTR 164,196,57344\nTR 166,196,57856\nMA 52,172,148\n"
"buffer_load_dwordx4 v189, s[36:39], s86 offen offset:1024 lds\nMU 106,178,106\nMA 56,172,152\nMU 107,179,107\nEX 104\nMA 60,172,156\nEX 105\n"
"s_waitcnt lgkmcnt(2)\nMA 0,160,160\nTR 168,197,49152\nTR 170,197,49664\nMA 4,160,164\nbuffer_load_dwordx4 v190, s[36:39], s86 offen offset:2048 lds\n"
"EX 106\nMA 8,160,168\nEX 107\nMA 12,160,172\nv_add_f32 v104, 1.0, v104\nv_add_f32 v105, 1.0, v105\ns_waitcnt lgkmcnt(2)\nMA 16,164,160\n"
"TR 172,197,57344\nTR 174,197,57856\nMA 20,164,164\nbuffer_load_dwordx4 v191, s[36:39], s86 offen offset:3072 lds\nv_add_f32 v106, 1.0, v106\n"
"MA 24,164,168\nv_add_f32 v107, 1.0, v107\nv_rcp_f32 v104, v104\nMA 28,164,172\nv_rcp_f32 v105, v105\ns_waitcnt lgkmcnt(2)\nMA 32,168,160\n"
"TR 160,196,53248\nTR 162,196,53760\nMA 36,168,164\nv_rcp_f32 v106, v106\nMA 40,168,168\nv_rcp_f32 v107, v107\nMA 44,168,172\nMU 120,120,104\n"
"MU 121,121,105\ns_waitcnt lgkmcnt(2)\nMA 48,172,160\nTR 164,196,61440\nTR 166,196,61952\nMA 52,172,164\nMU 122,122,106\nMU 123,123,107\n"
"MA 56,172,168\nv_cvt_pk_bf16_f32 v72, v88, v89\nv_cvt_pk_bf16_f32 v73, v90, v91\nMA 60,172,172\nv_cvt_pk_bf16_f32 v74, v120, v121\n"
"v_cvt_pk_bf16_f32 v75, v122, v123\ns_waitcnt lgkmcnt(2)\nMA 0,160,176\nTR 168,197,53248\nTR 170,197,53760\nTR 172,197,61440\nTR 174,197,61952\n"
"MA 4,160,180\nds_write_b128 v206, v[72:75] offset:10240\nMU 176,76,76\nMA 8,160,184\nMU 177,77,77\nMU 178,78,78\nMA 12,160,188\nMU 179,79,79\n"
"v_fmaak_f32 v176, v252, v176, 0xc0135761\ns_waitcnt lgkmcnt(5)\nMA 16,164,176\nv_fmaak_f32 v177, v252, v177, 0xc0135761\n"
"v_fmaak_f32 v178, v252, v178, 0xc0135761\nMA 20,164,180\nv_fmaak_f32 v179, v252, v179, 0xc0135761\nMU 92,76,92\nMA 24,164,184\nMU 93,77,93\n"
"MU 94,78,94\nMA 28,164,188\nMU 95,79,95\nMU 76,176,76\ns_waitcnt vmcnt(12) lgkmcnt(0)\ns_barrier\nMA 32,168,176\nds_read_b64_tr_b16 v[160:161], v198\n"
"TR 162,198,512\nMA 36,168,180\ns_add_u32 m0, s4, 0xc000\ns_mul_i32 s86, s1, 0x1200\ns_add_u32 s86, s86, 0x180\n"
"buffer_load_dwordx4 v188, s[36:39], s86 offen lds\nMA 40,168,184\nMU 77,177,77\nMU 78,178,78\nMA 44,168,188\nMU 79,179,79\nEX 76\nMA 48,172,176\n"
"TR 164,198,8192\nTR 166,198,8704\nMA 52,172,180\nbuffer_load_dwordx4 v189, s[36:39], s86 offen offset:1024 lds\nEX 77\nMA 56,172,184\nEX 78\n"
"MA 60,172,188\nEX 79\ns_waitcnt lgkmcnt(2)\nMA 0,160,192\nds_read_b64_tr_b16 v[168:169], v199\nTR 170,199,512\nMA 4,160,196\n"
"buffer_load_dwordx4 v190, s[36:39], s86 offen offset:2048 lds\nv_add_f32 v76, 1.0, v76\nMA 8,160,200\nv_add_f32 v77, 1.0, v77\n"
"v_add_f32 v78, 1.0, v78\nMA 12,160,204\nv_add_f32 v79, 1.0, v79\nv_rcp_f32 v76, v76\ns_waitcnt lgkmcnt(2)\nMA 16,164,192\nTR 172,199,8192\n"
"TR 174,199,8704\nMA 20,164,196\nbuffer_load_dwordx4 v191, s[36:39], s86 offen offset:3072 lds\nv_rcp_f32 v77, v77\nMA 24,164,200\nv_rcp_f32 v78, v78\n"
"MA 28,164,204\nv_rcp_f32 v79, v79\ns_waitcnt lgkmcnt(2)\nMA 32,168,192\nTR 160,198,4096\nTR 162,198,4608\nMA 36,168,196\nMU 92,92,76\nMU 93,93,77\n"
"MA 40,168,200\nMU 94,94,78\nMU 95,95,79\nMA 44,168,204\nMU 176,108,108\nMU 177,109,109\ns_waitcnt lgkmcnt(2)\nMA 48,172,192\nTR 164,198,12288\n"
"TR 166,198,12800\nMA 52,172,196\nMU 178,110,110\nMU 179,111,111\nMA 56,172,200\nv_fmaak_f32 v176, v252, v176, 0xc0135761\n"
"v_fmaak_f32 v177, v252, v177, 0xc0135761\nMA 60,172,204\nv_fmaak_f32 v178, v252, v178, 0xc0135761\nv_fmaak_f32 v179, v252, v179, 0xc0135761\n"
"s_waitcnt lgkmcnt(2)\nMA 0,160,208\nTR 168,199,4096\nTR 170,199,4608\nTR 172,199,12288\nTR 174,199,12800\nMA 4,160,212\nMU 124,108,124\n"
"MU 125,109,125\nMA 8,160,216\nMU 126,110,126\nMU 127,111,127\nMA 12,160,220\nMU 108,176,108\nMU 109,177,109\ns_waitcnt lgkmcnt(4)\nMA 16,164,208\n"
"MU 110,178,110\nMU 111,179,111\nMA 20,164,212\nEX 108\nMA 24,164,216\nEX 109\nMA 28,164,220\nEX 110\ns_waitcnt vmcnt(12) lgkmcnt(0)\ns_barrier\n"
"MA 32,168,208\nds_read_b64_tr_b16 v[160:161], v196\nTR 162,196,512\nMA 36,168,212\ns_add_u32 m0, s4, 0x10000\ns_mul_i32 s86, s2, 0x1200\n"
"s_add_u32 s86, s86, 0x180\nbuffer_load_dwordx4 v188, s[36:39], s86 offen lds\nMA 40,168,216\nEX 111\nMA 44,168,220\nv_add_f32 v108, 1.0, v108\n"
"v_add_f32 v109, 1.0, v109\nMA 48,172,208\nTR 164,196,8192\nTR 166,196,8704\nMA 52,172,212\n"
"buffer_load_dwordx4 v189, s[36:39], s86 offen offset:1024 lds\nv_add_f32 v110, 1.0, v110\nMA 56,172,216\nv_add_f32 v111, 1.0, v111\n"
"v_rcp_f32 v108, v108\nMA 60,172,220\nv_rcp_f32 v109, v109\ns_waitcnt lgkmcnt(2)\nMA 0,160,224\nds_read_b64_tr_b16 v[168:169], v197\nTR 170,197,512\n"
"MA 4,160,228\nbuffer_load_dwordx4 v190, s[36:39], s86 offen offset:2048 lds\nv_rcp_f32 v110, v110\nMA 8,160,232\nv_rcp_f32 v111, v111\nMA 12,160,236\n"
"MU 124,124,108\nMU 125,125,109\ns_waitcnt lgkmcnt(2)\nMA 16,164,224\nTR 172,197,8192\nTR 174,197,8704\nMA 20,164,228\n"
"buffer_load_dwordx4 v191, s[36:39], s86 offen offset:3072 lds\nMU 126,126,110\nMA 24,164,232\nMU 127,127,111\nv_cvt_pk_bf16_f32 v76, v92, v93\n"
"MA 28,164,236\nv_cvt_pk_bf16_f32 v77, v94, v95\nv_cvt_pk_bf16_f32 v78, v124, v125\ns_waitcnt lgkmcnt(2)\nMA 32,168,224\nTR 160,196,4096\n"
"TR 162,196,4608\nMA 36,168,228\nv_cvt_pk_bf16_f32 v79, v126, v127\nds_write_b128 v206, v[76:79] offset:11264\nMA 40,168,232\nMA 44,168,236\n"
"s_waitcnt lgkmcnt(3)\nMA 48,172,224\nTR 164,196,12288\nTR 166,196,12800\nMA 52,172,228\nMA 56,172,232\nMA 60,172,236\ns_waitcnt lgkmcnt(3)\n"
"MA 0,160,240\nTR 168,197,4096\nTR 170,197,4608\nTR 172,197,12288\nTR 174,197,12800\nMA 4,160,244\nMA 8,160,248\nMA 12,160,252\ns_waitcnt lgkmcnt(4)\n"
"MA 16,164,240\nMA 20,164,244\nMA 24,164,248\nMA 28,164,252\ns_waitcnt vmcnt(12) lgkmcnt(0)\ns_barrier\nMA 32,168,240\nTR 160,196,16384\n"
"TR 162,196,16896\nMA 36,168,244\ns_add_u32 m0, s4, 0\ns_mul_i32 s86, s3, 0x1200\ns_add_u32 s86, s86, 0x180\n"
"buffer_load_dwordx4 v188, s[36:39], s86 offen lds\nMA 40,168,248\nMA 44,168,252\nMA 48,172,240\nTR 164,196,24576\nTR 166,196,25088\nMA 52,172,244\n"
"buffer_load_dwordx4 v189, s[36:39], s86 offen offset:1024 lds\nMA 56,172,248\nMA 60,172,252\ns_waitcnt lgkmcnt(2)\n"
"v_mfma_f32_16x16x32_bf16 v[0:3], v[160:163], v[128:131], v[0:3]\nTR 168,197,16384\nTR 170,197,16896\n"
"v_mfma_f32_16x16x32_bf16 v[4:7], v[160:163], v[132:135], v[4:7]\nbuffer_load_dwordx4 v190, s[36:39], s86 offen offset:2048 lds\n"
"v_mfma_f32_16x16x32_bf16 v[8:11], v[160:163], v[136:139], v[8:11]\nv_mfma_f32_16x16x32_bf16 v[12:15], v[160:163], v[140:143], v[12:15]\n"
"s_waitcnt lgkmcnt(2)\nv_mfma_f32_16x16x32_bf16 v[16:19], v[164:167], v[128:131], v[16:19]\nTR 172,197,24576\nTR 174,197,25088\n"
"v_mfma_f32_16x16x32_bf16 v[20:23], v[164:167], v[132:135], v[20:23]\nbuffer_load_dwordx4 v191, s[36:39], s86 offen offset:3072 lds\n"
"v_mfma_f32_16x16x32_bf16 v[24:27], v[164:167], v[136:139], v[24:27]\nv_mfma_f32_16x16x32_bf16 v[28:31], v[164:167], v[140:143], v[28:31]\n"
"s_waitcnt lgkmcnt(2)\nv_mfma_f32_16x16x32_bf16 v[32:35], v[168:171], v[128:131], v[32:35]\nTR 160,196,20480\nTR 162,196,20992\n"
"v_mfma_f32_16x16x32_bf16 v[36:39], v[168:171], v[132:135], v[36:39]\nv_mfma_f32_16x16x32_bf16 v[40:43], v[168:171], v[136:139], v[40:43]\n"
"v_mfma_f32_16x16x32_bf16 v[44:47], v[168:171], v[140:143], v[44:47]\ns_waitcnt lgkmcnt(2)\n"
"v_mfma_f32_16x16x32_bf16 v[48:51], v[172:175], v[128:131], v[48:51]\nTR 164,196,28672\nTR 166,196,29184\n"
"v_mfma_f32_16x16x32_bf16 v[52:55], v[172:175], v[132:135], v[52:55]\nv_mfma_f32_16x16x32_bf16 v[56:59], v[172:175], v[136:139], v[56:59]\n"
"v_mfma_f32_16x16x32_bf16 v[60:63], v[172:175], v[140:143], v[60:63]\ns_waitcnt lgkmcnt(2)\n"
"v_mfma_f32_16x16x32_bf16 v[0:3], v[160:163], v[144:147], v[0:3]\nTR 168,197,20480\nTR 170,197,20992\nTR 172,197,28672\nTR 174,197,29184\n"
"v_mfma_f32_16x16x32_bf16 v[4:7], v[160:163], v[148:151], v[4:7]\nv_mfma_f32_16x16x32_bf16 v[8:11], v[160:163], v[152:155], v[8:11]\n"
"v_mfma_f32_16x16x32_bf16 v[12:15], v[160:163], v[156:159], v[12:15]\ns_waitcnt lgkmcnt(4)\n"
"v_mfma_f32_16x16x32_bf16 v[16:19], v[164:167], v[144:147], v[16:19]\nv_mfma_f32_16x16x32_bf16 v[20:23], v[164:167], v[148:151], v[20:23]\n"
"v_mfma_f32_16x16x32_bf16 v[24:27], v[164:167], v[152:155], v[24:27]\nv_mfma_f32_16x16x32_bf16 v[28:31], v[164:167], v[156:159], v[28:31]\n"
"s_waitcnt vmcnt(12) lgkmcnt(0)\ns_barrier\nv_mfma_f32_16x16x32_bf16 v[32:35], v[168:171], v[144:147], v[32:35]\nTR 160,196,32768\nTR 162,196,33280\n"
"v_mfma_f32_16x16x32_bf16 v[36:39], v[168:171], v[148:151], v[36:39]\ns_add_u32 m0, s4, 0x4000\ns_mul_i32 s86, s78, 0x1200\ns_add_u32 s86, s86, 0x180\n"
"buffer_load_dwordx4 v188, s[36:39], s86 offen lds\nv_mfma_f32_16x16x32_bf16 v[40:43], v[168:171], v[152:155], v[40:43]\n"
"v_mfma_f32_16x16x32_bf16 v[44:47], v[168:171], v[156:159], v[44:47]\nv_mfma_f32_16x16x32_bf16 v[48:51], v[172:175], v[144:147], v[48:51]\n"
"TR 164,196,40960\nTR 166,196,41472\nv_mfma_f32_16x16x32_bf16 v[52:55], v[172:175], v[148:151], v[52:55]\n"
"buffer_load_dwordx4 v189, s[36:39], s86 offen offset:1024 lds\nv_mfma_f32_16x16x32_bf16 v[56:59], v[172:175], v[152:155], v[56:59]\n"
"v_mfma_f32_16x16x32_bf16 v[60:63], v[172:175], v[156:159], v[60:63]\ns_waitcnt lgkmcnt(2)\nv_mfma_f32_16x16x32_bf16 v[64:67], v[160:163], a[0:3], 0\n"
"TR 168,197,32768\nTR 170,197,33280\nv_mfma_f32_16x16x32_bf16 v[68:71], v[160:163], a[4:7], 0\n"
"buffer_load_dwordx4 v190, s[36:39], s86 offen offset:2048 lds\nMU 176,0,0\nv_mfma_f32_16x16x32_bf16 v[72:75], v[160:163], a[8:11], 0\nMU 177,1,1\n"
"MU 178,2,2\nv_mfma_f32_16x16x32_bf16 v[76:79], v[160:163], a[12:15], 0\nMU 179,3,3\nv_fmaak_f32 v176, v252, v176, 0xc0135761\ns_waitcnt lgkmcnt(2)\n"
"v_mfma_f32_16x16x32_bf16 v[80:83], v[164:167], a[0:3], 0\nTR 172,197,40960\nTR 174,197,41472\n"
"v_mfma_f32_16x16x32_bf16 v[84:87], v[164:167], a[4:7], 0\nbuffer_load_dwordx4 v191, s[36:39], s86 offen offset:3072 lds\n"
"v_fmaak_f32 v177, v252, v177, 0xc0135761\nv_mfma_f32_16x16x32_bf16 v[88:91], v[164:167], a[8:11], 0\nv_fmaak_f32 v178, v252, v178, 0xc0135761\n"
"v_fmaak_f32 v179, v252, v179, 0xc0135761\nv_mfma_f32_16x16x32_bf16 v[92:95], v[164:167], a[12:15], 0\nMU 16,0,16\nMU 17,1,17\ns_waitcnt lgkmcnt(2)\n"
"v_mfma_f32_16x16x32_bf16 v[96:99], v[168:171], a[0:3], 0\nTR 160,196,36864\nTR 162,196,37376\n"
"v_mfma_f32_16x16x32_bf16 v[100:103], v[168:171], a[4:7], 0\nMU 18,2,18\nMU 19,3,19\nv_mfma_f32_16x16x32_bf16 v[104:107], v[168:171], a[8:11], 0\n"
"MU 0,176,0\nMU 1,177,1\nv_mfma_f32_16x16x32_bf16 v[108:111], v[168:171], a[12:15], 0\nMU 2,178,2\nMU 3,179,3\ns_waitcnt lgkmcnt(2)\n"
"v_mfma_f32_16x16x32_bf16 v[112:115], v[172:175], a[0:3], 0\nTR 164,196,45056\nTR 166,196,45568\n"
"v_mfma_f32_16x16x32_bf16 v[116:119], v[172:175], a[4:7], 0\nEX 0\nv_mfma_f32_16x16x32_bf16 v[120:123], v[172:175], a[8:11], 0\nEX 1\n"
"v_mfma_f32_16x16x32_bf16 v[124:127], v[172:175], a[12:15], 0\nEX 2\ns_waitcnt lgkmcnt(2)\nMA 64,160,16\nTR 168,197,36864\nTR 170,197,37376\n"
"TR 172,197,45056\nTR 174,197,45568\nMA 68,160,20\nEX 3\nMA 72,160,24\nv_add_f32 v0, 1.0, v0\nv_add_f32 v1, 1.0, v1\nMA 76,160,28\n"
"v_add_f32 v2, 1.0, v2\nv_add_f32 v3, 1.0, v3\ns_waitcnt lgkmcnt(4)\nMA 80,164,16\nv_rcp_f32 v0, v0\nMA 84,164,20\nv_rcp_f32 v1, v1\nMA 88,164,24\n"
"v_rcp_f32 v2, v2\nMA 92,164,28\nv_rcp_f32 v3, v3\ns_waitcnt vmcnt(12) lgkmcnt(0)\ns_barrier\nMA 96,168,16\nTR 160,196,49152\nTR 162,196,49664\n"
"MA 100,168,20\ns_add_u32 m0, s4, 0x8000\ns_mul_i32 s86, s79, 0x1200\ns_add_u32 s86, s86, 0x180\nbuffer_load_dwordx4 v188, s[36:39], s86 offen lds\n"
"MA 104,168,24\nMU 16,16,0\nMU 17,17,1\nMA 108,168,28\nMU 18,18,2\nMU 19,19,3\nMA 112,172,16\nTR 164,196,57344\nTR 166,196,57856\nMA 116,172,20\n"
"buffer_load_dwordx4 v189, s[36:39], s86 offen offset:1024 lds\nMU 176,32,32\nMA 120,172,24\nMU 177,33,33\nMU 178,34,34\nMA 124,172,28\nMU 179,35,35\n"
"v_fmaak_f32 v176, v252, v176, 0xc0135761\ns_waitcnt lgkmcnt(2)\nMA 64,160,32\nTR 168,197,49152\nTR 170,197,49664\nMA 68,160,36\n"
"buffer_load_dwordx4 v190, s[36:39], s86 offen offset:2048 lds\nv_fmaak_f32 v177, v252, v177, 0xc0135761\nMA 72,160,40\n"
"v_fmaak_f32 v178, v252, v178, 0xc0135761\nv_fmaak_f32 v179, v252, v179, 0xc0135761\nMA 76,160,44\nMU 48,32,48\nMU 49,33,49\ns_waitcnt lgkmcnt(2)\n"
"MA 80,164,32\nTR 172,197,57344\nTR 174,197,57856\nMA 84,164,36\nbuffer_load_dwordx4 v191, s[36:39], s86 offen offset:3072 lds\nMU 50,34,50\n"
"MA 88,164,40\nMU 51,35,51\nMU 32,176,32\nMA 92,164,44\nMU 33,177,33\nMU 34,178,34\ns_waitcnt lgkmcnt(2)\nMA 96,168,32\nTR 160,196,53248\n"
"TR 162,196,53760\nMA 100,168,36\nMU 35,179,35\nEX 32\nMA 104,168,40\nEX 33\nMA 108,168,44\nEX 34\ns_waitcnt lgkmcnt(2)\nMA 112,172,32\n"
"TR 164,196,61440\nTR 166,196,61952\nMA 116,172,36\nEX 35\nMA 120,172,40\nv_add_f32 v32, 1.0, v32\nv_add_f32 v33, 1.0, v33\nMA 124,172,44\n"
"v_add_f32 v34, 1.0, v34\nv_add_f32 v35, 1.0, v35\ns_waitcnt lgkmcnt(2)\nMA 64,160,48\nTR 168,197,53248\nTR 170,197,53760\nTR 172,197,61440\n"
"TR 174,197,61952\nMA 68,160,52\nv_rcp_f32 v32, v32\nMA 72,160,56\nv_rcp_f32 v33, v33\nMA 76,160,60\nv_rcp_f32 v34, v34\ns_waitcnt lgkmcnt(4)\n"
"MA 80,164,48\nv_rcp_f32 v35, v35\nMA 84,164,52\nMU 48,48,32\nMU 49,49,33\nMA 88,164,56\nMU 50,50,34\nMU 51,51,35\nMA 92,164,60\n"
"v_cvt_pk_bf16_f32 v0, v16, v17\nv_cvt_pk_bf16_f32 v1, v18, v19\ns_waitcnt vmcnt(12) lgkmcnt(0)\ns_barrier\nMA 96,168,48\n"
"ds_read_b64_tr_b16 v[160:161], v198\nTR 162,198,512\nMA 100,168,52\ns_add_u32 m0, s4, 0xc000\ns_mul_i32 s86, s80, 0x1200\ns_add_u32 s86, s86, 0x180\n"
"buffer_load_dwordx4 v188, s[36:39], s86 offen lds\nMA 104,168,56\nv_cvt_pk_bf16_f32 v2, v48, v49\nv_cvt_pk_bf16_f32 v3, v50, v51\nMA 108,168,60\n"
"ds_write_b128 v206, v[0:3] offset:16384\nMU 176,4,4\nMA 112,172,48\nTR 164,198,8192\nTR 166,198,8704\nMA 116,172,52\n"
"buffer_load_dwordx4 v189, s[36:39], s86 offen offset:1024 lds\nMU 177,5,5\nMA 120,172,56\nMU 178,6,6\nMU 179,7,7\nMA 124,172,60\n"
"v_fmaak_f32 v176, v252, v176, 0xc0135761\nv_fmaak_f32 v177, v252, v177, 0xc0135761\ns_waitcnt lgkmcnt(3)\nMA 64,160,64\n"
"ds_read_b64_tr_b16 v[168:169], v199\nTR 170,199,512\nMA 68,160,68\nbuffer_load_dwordx4 v190, s[36:39], s86 offen offset:2048 lds\n"
"v_fmaak_f32 v178, v252, v178, 0xc0135761\nMA 72,160,72\nv_fmaak_f32 v179, v252, v179, 0xc0135761\nMU 20,4,20\nMA 76,160,76\nMU 21,5,21\nMU 22,6,22\n"
"s_waitcnt lgkmcnt(2)\nMA 80,164,64\nTR 172,199,8192\nTR 174,199,8704\nMA 84,164,68\nbuffer_load_dwordx4 v191, s[36:39], s86 offen offset:3072 lds\n"
"MU 23,7,23\nMA 88,164,72\nMU 4,176,4\nMU 5,177,5\nMA 92,164,76\nMU 6,178,6\nMU 7,179,7\ns_waitcnt lgkmcnt(2)\nMA 96,168,64\nTR 160,198,4096\n"
"TR 162,198,4608\nMA 100,168,68\nEX 4\nMA 104,168,72\nEX 5\nMA 108,168,76\nEX 6\ns_waitcnt lgkmcnt(2)\nMA 112,172,64\nTR 164,198,12288\n"
"TR 166,198,12800\nMA 116,172,68\nEX 7\nMA 120,172,72\nv_add_f32 v4, 1.0, v4\nv_add_f32 v5, 1.0, v5\nMA 124,172,76\nv_add_f32 v6, 1.0, v6\n"
"v_add_f32 v7, 1.0, v7\ns_waitcnt lgkmcnt(2)\nMA 64,160,80\nTR 168,199,4096\nTR 170,199,4608\nTR 172,199,12288\nTR 174,199,12800\nMA 68,160,84\n"
"v_rcp_f32 v4, v4\nMA 72,160,88\nv_rcp_f32 v5, v5\nMA 76,160,92\nv_rcp_f32 v6, v6\ns_waitcnt lgkmcnt(4)\nMA 80,164,80\nv_rcp_f32 v7, v7\nMA 84,164,84\n"
"MU 20,20,4\nMU 21,21,5\nMA 88,164,88\nMU 22,22,6\nMU 23,23,7\nMA 92,164,92\nMU 176,36,36\nMU 177,37,37\ns_waitcnt vmcnt(12) lgkmcnt(0)\ns_barrier\n"
"MA 96,168,80\nds_read_b64_tr_b16 v[160:161], v196\nTR 162,196,512\nMA 100,168,84\ns_add_u32 m0, s4, 0x10000\ns_mul_i32 s86, s81, 0x1200\n"
"s_add_u32 s86, s86, 0x180\nbuffer_load_dwordx4 v188, s[36:39], s86 offen lds\nMA 104,168,88\nMU 178,38,38\nMU 179,39,39\nMA 108,168,92\n"
"v_fmaak_f32 v176, v252, v176, 0xc0135761\nv_fmaak_f32 v177, v252, v177, 0xc0135761\nMA 112,172,80\nTR 164,196,8192\nTR 166,196,8704\nMA 116,172,84\n"
"buffer_load_dwordx4 v189, s[36:39], s86 offen offset:1024 lds\nv_fmaak_f32 v178, v252, v178, 0xc0135761\nMA 120,172,88\n"
"v_fmaak_f32 v179, v252, v179, 0xc0135761\nMU 52,36,52\nMA 124,172,92\nMU 53,37,53\nMU 54,38,54\ns_waitcnt lgkmcnt(2)\nMA 64,160,96\n"
"ds_read_b64_tr_b16 v[168:169], v197\nTR 170,197,512\nMA 68,160,100\nbuffer_load_dwordx4 v190, s[36:39], s86 offen offset:2048 lds\nMU 55,39,55\n"
"MA 72,160,104\nMU 36,176,36\nMU 37,177,37\nMA 76,160,108\nMU 38,178,38\nMU 39,179,39\ns_waitcnt lgkmcnt(2)\nMA 80,164,96\nTR 172,197,8192\n"
"TR 174,197,8704\nMA 84,164,100\nbuffer_load_dwordx4 v191, s[36:39], s86 offen offset:3072 lds\nEX 36\nMA 88,164,104\nEX 37\nMA 92,164,108\nEX 38\n"
"s_waitcnt lgkmcnt(2)\nMA 96,168,96\nTR 160,196,4096\nTR 162,196,4608\nMA 100,168,100\nEX 39\nMA 104,168,104\nv_add_f32 v36, 1.0, v36\n"
"v_add_f32 v37, 1.0, v37\nMA 108,168,108\nv_add_f32 v38, 1.0, v38\nv_add_f32 v39, 1.0, v39\ns_waitcnt lgkmcnt(2)\nMA 112,172,96\nTR 164,196,12288\n"
"TR 166,196,12800\nMA 116,172,100\nv_rcp_f32 v36, v36\nMA 120,172,104\nv_rcp_f32 v37, v37\nMA 124,172,108\nv_rcp_f32 v38, v38\ns_waitcnt lgkmcnt(2)\n"
"MA 64,160,112\nTR 168,197,4096\nTR 170,197,4608\nTR 172,197,12288\nTR 174,197,12800\nMA 68,160,116\nv_rcp_f32 v39, v39\nMA 72,160,120\nMU 52,52,36\n"
"MU 53,53,37\nMA 76,160,124\nMU 54,54,38\nMU 55,55,39\ns_waitcnt lgkmcnt(4)\nMA 80,164,112\nv_cvt_pk_bf16_f32 v4, v20, v21\n"
"v_cvt_pk_bf16_f32 v5, v22, v23\nMA 84,164,116\nv_cvt_pk_bf16_f32 v6, v52, v53\nv_cvt_pk_bf16_f32 v7, v54, v55\nMA 88,164,120\n"
"ds_write_b128 v206, v[4:7] offset:17408\nMU 176,8,8\nMA 92,164,124\nMU 177,9,9\nMU 178,10,10\ns_waitcnt vmcnt(12) lgkmcnt(0)\ns_barrier\n"
"MA 96,168,112\nTR 160,196,16384\nTR 162,196,16896\nMA 100,168,116\ns_add_u32 m0, s4, 0\ns_mul_i32 s86, s19, 0x1200\ns_add_u32 s86, s86, 0x180\n"
"buffer_load_dwordx4 v188, s[36:39], s86 offen lds\nMA 104,168,120\nMU 179,11,11\nv_fmaak_f32 v176, v252, v176, 0xc0135761\nMA 108,168,124\n"
"v_fmaak_f32 v177, v252, v177, 0xc0135761\nv_fmaak_f32 v178, v252, v178, 0xc0135761\nMA 112,172,112\nTR 164,196,24576\nTR 166,196,25088\n"
"MA 116,172,116\nbuffer_load_dwordx4 v189, s[36:39], s86 offen offset:1024 lds\nv_fmaak_f32 v179, v252, v179, 0xc0135761\nMA 120,172,120\nMU 24,8,24\n"
"MU 25,9,25\nMA 124,172,124\nMU 26,10,26\nMU 27,11,27\ns_getpc_b64 s[22:23]\nL_pc2:\nv_add_u32 v192, 0x2000, v184\n"
"v_min_u32 v192, L_end-L_pc2-4, v192\nglobal_load_dword v253, v192, s[22:23]\ns_waitcnt lgkmcnt(2)\nMA 64,160,128\nTR 168,197,16384\nTR 170,197,16896\n"
"MA 68,160,132\nbuffer_load_dwordx4 v190, s[36:39], s86 offen offset:2048 lds\nMU 8,176,8\nMA 72,160,136\nMU 9,177,9\nMU 10,178,10\nMA 76,160,140\n"
"MU 11,179,11\nEX 8\ns_waitcnt lgkmcnt(2)\nMA 80,164,128\nTR 172,197,24576\nTR 174,197,25088\nMA 84,164,132\n"
"buffer_load_dwordx4 v191, s[36:39], s86 offen offset:3072 lds\nEX 9\nMA 88,164,136\nEX 10\nMA 92,164,140\nEX 11\ns_waitcnt lgkmcnt(2)\nMA 96,168,128\n"
"TR 160,196,20480\nTR 162,196,20992\nMA 100,168,132\nv_add_f32 v8, 1.0, v8\nv_add_f32 v9, 1.0, v9\nMA 104,168,136\nv_add_f32 v10, 1.0, v10\n"
"v_add_f32 v11, 1.0, v11\nMA 108,168,140\nv_rcp_f32 v8, v8\ns_waitcnt lgkmcnt(2)\nMA 112,172,128\nTR 164,196,28672\nTR 166,196,29184\nMA 116,172,132\n"
"v_rcp_f32 v9, v9\nMA 120,172,136\nv_rcp_f32 v10, v10\nMA 124,172,140\nv_rcp_f32 v11, v11\ns_waitcnt lgkmcnt(2)\nMA 64,160,144\nTR 168,197,20480\n"
"TR 170,197,20992\nTR 172,197,28672\nTR 174,197,29184\nMA 68,160,148\nMU 24,24,8\nMU 25,25,9\nMA 72,160,152\nMU 26,26,10\nMU 27,27,11\nMA 76,160,156\n"
"MU 176,40,40\nMU 177,41,41\ns_waitcnt lgkmcnt(4)\nMA 80,164,144\nMU 178,42,42\nMU 179,43,43\nMA 84,164,148\nv_fmaak_f32 v176, v252, v176, 0xc0135761\n"
"v_fmaak_f32 v177, v252, v177, 0xc0135761\nMA 88,164,152\nv_fmaak_f32 v178, v252, v178, 0xc0135761\nv_fmaak_f32 v179, v252, v179, 0xc0135761\n"
"MA 92,164,156\nMU 56,40,56\nMU 57,41,57\ns_waitcnt vmcnt(13) lgkmcnt(0)\ns_barrier\nMA 96,168,144\nTR 160,196,32768\nTR 162,196,33280\n"
"MA 100,168,148\ns_add_u32 m0, s18, 0x4000\ns_nop 0\nglobal_load_lds_dwordx4 v[210:211], off\nv_lshl_add_u64 v[210:211], v[212:213], 0, v[210:211]\n"
"MA 104,168,152\nMU 58,42,58\nMU 59,43,59\nMA 108,168,156\nMU 40,176,40\nMU 41,177,41\nMA 112,172,144\nTR 164,196,40960\nTR 166,196,41472\n"
"MA 116,172,148\ns_add_u32 m0, s18, 0x4800\ns_nop 0\nglobal_load_lds_dwordx4 v[210:211], off\nv_lshl_add_u64 v[210:211], v[212:213], 0, v[210:211]\n"
"MA 120,172,152\nMU 42,178,42\nMU 43,179,43\nMA 124,172,156\nEX 40\ns_waitcnt lgkmcnt(2)\nMA 64,160,160\nTR 168,197,32768\nTR 170,197,33280\n"
"MA 68,160,164\ns_add_u32 m0, s18, 0x5000\ns_nop 0\nglobal_load_lds_dwordx4 v[210:211], off\nv_lshl_add_u64 v[210:211], v[212:213], 0, v[210:211]\n"
"MA 72,160,168\nEX 41\nMA 76,160,172\nEX 42\ns_waitcnt lgkmcnt(2)\nMA 80,164,160\nTR 172,197,40960\nTR 174,197,41472\nMA 84,164,164\n"
"s_add_u32 m0, s18, 0x5800\ns_cmp_eq_u32 s0, 0x400\ns_cbranch_scc0 L_mx36\nv_add_u32 v193, 0x4000, v241\ns_mov_b64 exec, s[88:89]\n"
"ds_write_b16 v193, v242\nL_mx36:\nglobal_load_lds_dwordx4 v[210:211], off\ns_mov_b64 exec, -1\ns_cmp_eq_u32 s1, 0\ns_cselect_b32 s22, 0xffffffdd, 1\n"
"v_mad_i64_i32 v[210:211], s[26:27], s22, v212, v[210:211]\nMA 88,164,168\nEX 43\nMA 92,164,172\nv_add_f32 v40, 1.0, v40\nv_add_f32 v41, 1.0, v41\n"
"s_waitcnt lgkmcnt(2)\nMA 96,168,160\nTR 160,196,36864\nTR 162,196,37376\nMA 100,168,164\nv_add_f32 v42, 1.0, v42\nv_add_f32 v43, 1.0, v43\n"
"MA 104,168,168\nv_rcp_f32 v40, v40\nMA 108,168,172\nv_rcp_f32 v41, v41\ns_waitcnt lgkmcnt(2)\nMA 112,172,160\nTR 164,196,45056\nTR 166,196,45568\n"
"MA 116,172,164\nv_rcp_f32 v42, v42\nMA 120,172,168\nv_rcp_f32 v43, v43\nMA 124,172,172\nMU 56,56,40\nMU 57,57,41\ns_waitcnt lgkmcnt(2)\n"
"MA 64,160,176\nTR 168,197,36864\nTR 170,197,37376\nTR 172,197,45056\nTR 174,197,45568\nMA 68,160,180\nMU 58,58,42\nMU 59,59,43\nMA 72,160,184\n"
"v_cvt_pk_bf16_f32 v8, v24, v25\nv_cvt_pk_bf16_f32 v9, v26, v27\nMA 76,160,188\nv_cvt_pk_bf16_f32 v10, v56, v57\nv_cvt_pk_bf16_f32 v11, v58, v59\n"
"s_waitcnt lgkmcnt(4)\nMA 80,164,176\nds_write_b128 v206, v[8:11] offset:18432\nMU 176,12,12\nMA 84,164,180\nMU 177,13,13\nMU 178,14,14\n"
"MA 88,164,184\nMU 179,15,15\nv_fmaak_f32 v176, v252, v176, 0xc0135761\nMA 92,164,188\nv_fmaak_f32 v177, v252, v177, 0xc0135761\n"
"v_fmaak_f32 v178, v252, v178, 0xc0135761\ns_waitcnt vmcnt(13) lgkmcnt(0)\ns_barrier\nMA 96,168,176\nTR 160,196,49152\nTR 162,196,49664\n"
"MA 100,168,180\ns_add_u32 m0, s18, 0x8000\ns_nop 0\nglobal_load_lds_dwordx4 v[210:211], off\nv_lshl_add_u64 v[210:211], v[212:213], 0, v[210:211]\n"
"MA 104,168,184\nv_fmaak_f32 v179, v252, v179, 0xc0135761\nMU 28,12,28\nMA 108,168,188\nMU 29,13,29\nMU 30,14,30\nMA 112,172,176\nTR 164,196,57344\n"
"TR 166,196,57856\nMA 116,172,180\ns_add_u32 m0, s18, 0x8800\ns_nop 0\nglobal_load_lds_dwordx4 v[210:211], off\n"
"v_lshl_add_u64 v[210:211], v[212:213], 0, v[210:211]\nMA 120,172,184\nMU 31,15,31\nMU 12,176,12\nMA 124,172,188\nMU 13,177,13\nMU 14,178,14\n"
"s_waitcnt lgkmcnt(2)\nMA 64,160,192\nTR 168,197,49152\nTR 170,197,49664\nMA 68,160,196\ns_add_u32 m0, s18, 0x9000\ns_nop 0\n"
"global_load_lds_dwordx4 v[210:211], off\nv_lshl_add_u64 v[210:211], v[212:213], 0, v[210:211]\nMA 72,160,200\nMU 15,179,15\nEX 12\nMA 76,160,204\n"
"EX 13\ns_waitcnt lgkmcnt(2)\nMA 80,164,192\nTR 172,197,57344\nTR 174,197,57856\nMA 84,164,196\ns_add_u32 m0, s18, 0x9800\ns_cmp_eq_u32 s1, 0x400\n"
"s_cbranch_scc0 L_mx37\nv_add_u32 v193, 0x8000, v241\ns_mov_b64 exec, s[88:89]\nds_write_b16 v193, v242\nL_mx37:\n"
"global_load_lds_dwordx4 v[210:211], off\ns_mov_b64 exec, -1\ns_cmp_eq_u32 s2, 0\ns_cselect_b32 s22, 0xffffffdd, 1\n"
"v_mad_i64_i32 v[210:211], s[26:27], s22, v212, v[210:211]\nMA 88,164,200\nEX 14\nMA 92,164,204\nEX 15\ns_waitcnt lgkmcnt(2)\nMA 96,168,192\n"
"TR 160,196,53248\nTR 162,196,53760\nMA 100,168,196\nv_add_f32 v12, 1.0, v12\nv_add_f32 v13, 1.0, v13\nMA 104,168,200\nv_add_f32 v14, 1.0, v14\n"
"v_add_f32 v15, 1.0, v15\nMA 108,168,204\nv_rcp_f32 v12, v12\ns_waitcnt lgkmcnt(2)\nMA 112,172,192\nTR 164,196,61440\nTR 166,196,61952\n"
"MA 116,172,196\nv_rcp_f32 v13, v13\nMA 120,172,200\nv_rcp_f32 v14, v14\nMA 124,172,204\nv_rcp_f32 v15, v15\ns_waitcnt lgkmcnt(2)\nMA 64,160,208\n"
"TR 168,197,53248\nTR 170,197,53760\nTR 172,197,61440\nTR 174,197,61952\nMA 68,160,212\nMU 28,28,12\nMU 29,29,13\nMA 72,160,216\nMU 30,30,14\n"
"MU 31,31,15\nMA 76,160,220\nMU 176,44,44\nMU 177,45,45\ns_waitcnt lgkmcnt(4)\nMA 80,164,208\nMU 178,46,46\nMU 179,47,47\nMA 84,164,212\n"
"v_fmaak_f32 v176, v252, v176, 0xc0135761\nv_fmaak_f32 v177, v252, v177, 0xc0135761\nMA 88,164,216\nv_fmaak_f32 v178, v252, v178, 0xc0135761\n"
"v_fmaak_f32 v179, v252, v179, 0xc0135761\nMA 92,164,220\nMU 60,44,60\nMU 61,45,61\ns_waitcnt vmcnt(13) lgkmcnt(0)\ns_barrier\nMA 96,168,208\n"
"ds_read_b64_tr_b16 v[160:161], v198\nTR 162,198,512\nMA 100,168,212\ns_add_u32 m0, s18, 0xc000\ns_nop 0\nglobal_load_lds_dwordx4 v[210:211], off\n"
"v_lshl_add_u64 v[210:211], v[212:213], 0, v[210:211]\nMA 104,168,216\nMU 62,46,62\nMU 63,47,63\nMA 108,168,220\nMU 44,176,44\nMU 45,177,45\n"
"MA 112,172,208\nTR 164,198,8192\nTR 166,198,8704\nMA 116,172,212\ns_add_u32 m0, s18, 0xc800\ns_nop 0\nglobal_load_lds_dwordx4 v[210:211], off\n"
"v_lshl_add_u64 v[210:211], v[212:213], 0, v[210:211]\nMA 120,172,216\nMU 46,178,46\nMU 47,179,47\nMA 124,172,220\nEX 44\ns_waitcnt lgkmcnt(2)\n"
"MA 64,160,224\nds_read_b64_tr_b16 v[168:169], v199\nTR 170,199,512\nMA 68,160,228\ns_add_u32 m0, s18, 0xd000\ns_nop 0\n"
"global_load_lds_dwordx4 v[210:211], off\nv_lshl_add_u64 v[210:211], v[212:213], 0, v[210:211]\nMA 72,160,232\nEX 45\nMA 76,160,236\nEX 46\n"
"s_waitcnt lgkmcnt(2)\nMA 80,164,224\nTR 172,199,8192\nTR 174,199,8704\nMA 84,164,228\ns_add_u32 m0, s18, 0xd800\ns_cmp_eq_u32 s2, 0x400\n"
"s_cbranch_scc0 L_mx38\nv_add_u32 v193, 0xc000, v241\ns_mov_b64 exec, s[88:89]\nds_write_b16 v193, v242\nL_mx38:\n"
"global_load_lds_dwordx4 v[210:211], off\ns_mov_b64 exec, -1\ns_cmp_eq_u32 s3, 0\ns_cselect_b32 s22, 0xffffffdd, 1\n"
"v_mad_i64_i32 v[210:211], s[26:27], s22, v212, v[210:211]\nMA 88,164,232\nEX 47\nMA 92,164,236\nv_add_f32 v44, 1.0, v44\nv_add_f32 v45, 1.0, v45\n"
"s_waitcnt lgkmcnt(2)\nMA 96,168,224\nTR 160,198,4096\nTR 162,198,4608\nMA 100,168,228\nv_add_f32 v46, 1.0, v46\nv_add_f32 v47, 1.0, v47\n"
"MA 104,168,232\nv_rcp_f32 v44, v44\nMA 108,168,236\nv_rcp_f32 v45, v45\ns_waitcnt lgkmcnt(2)\nMA 112,172,224\nTR 164,198,12288\nTR 166,198,12800\n"
"MA 116,172,228\nv_rcp_f32 v46, v46\nMA 120,172,232\nv_rcp_f32 v47, v47\nMA 124,172,236\nMU 60,60,44\nMU 61,61,45\ns_waitcnt lgkmcnt(2)\n"
"MA 64,160,240\nTR 168,199,4096\nTR 170,199,4608\nTR 172,199,12288\nTR 174,199,12800\nMA 68,160,244\nMU 62,62,46\nMU 63,63,47\nMA 72,160,248\n"
"v_cvt_pk_bf16_f32 v12, v28, v29\nv_cvt_pk_bf16_f32 v13, v30, v31\nMA 76,160,252\nv_cvt_pk_bf16_f32 v14, v60, v61\nv_cvt_pk_bf16_f32 v15, v62, v63\n"
"s_waitcnt lgkmcnt(4)\nMA 80,164,240\nds_write_b128 v206, v[12:15] offset:19456\nMA 84,164,244\nMA 88,164,248\nMA 92,164,252\n"
"s_waitcnt vmcnt(12) lgkmcnt(0)\ns_barrier\nMA 96,168,240\nds_read_b64_tr_b16 v[160:161], v196\nTR 162,196,512\nMA 100,168,244\n"
"s_add_u32 m0, s18, 0x10000\ns_nop 0\nglobal_load_lds_dwordx4 v[210:211], off\nv_lshl_add_u64 v[210:211], v[212:213], 0, v[210:211]\nMA 104,168,248\n"
"MA 108,168,252\nMA 112,172,240\nTR 164,196,8192\nTR 166,196,8704\nMA 116,172,244\ns_add_u32 m0, s18, 0x10800\ns_nop 0\n"
"global_load_lds_dwordx4 v[210:211], off\nv_lshl_add_u64 v[210:211], v[212:213], 0, v[210:211]\nMA 120,172,248\nMA 124,172,252\ns_waitcnt lgkmcnt(2)\n"
"v_mfma_f32_16x16x32_bf16 v[64:67], v[160:163], v[128:131], v[64:67]\nds_read_b64_tr_b16 v[168:169], v197\nTR 170,197,512\n"
"v_mfma_f32_16x16x32_bf16 v[68:71], v[160:163], v[132:135], v[68:71]\ns_add_u32 m0, s18, 0x11000\ns_nop 0\nglobal_load_lds_dwordx4 v[210:211], off\n"
"v_lshl_add_u64 v[210:211], v[212:213], 0, v[210:211]\nv_mfma_f32_16x16x32_bf16 v[72:75], v[160:163], v[136:139], v[72:75]\n"
"v_mfma_f32_16x16x32_bf16 v[76:79], v[160:163], v[140:143], v[76:79]\ns_waitcnt lgkmcnt(2)\n"
"v_mfma_f32_16x16x32_bf16 v[80:83], v[164:167], v[128:131], v[80:83]\nTR 172,197,8192\nTR 174,197,8704\n"
"v_mfma_f32_16x16x32_bf16 v[84:87], v[164:167], v[132:135], v[84:87]\ns_add_u32 m0, s18, 0x11800\ns_cmp_eq_u32 s3, 0x400\ns_cbranch_scc0 L_mx39\n"
"v_add_u32 v193, 0x10000, v241\ns_mov_b64 exec, s[88:89]\nds_write_b16 v193, v242\nL_mx39:\nglobal_load_lds_dwordx4 v[210:211], off\n"
"s_mov_b64 exec, -1\ns_cmp_eq_u32 s78, 0\ns_cselect_b32 s22, 0xffffffdd, 1\nv_mad_i64_i32 v[210:211], s[26:27], s22, v212, v[210:211]\n"
"v_mfma_f32_16x16x32_bf16 v[88:91], v[164:167], v[136:139], v[88:91]\nv_mfma_f32_16x16x32_bf16 v[92:95], v[164:167], v[140:143], v[92:95]\n"
"s_waitcnt lgkmcnt(2)\nv_mfma_f32_16x16x32_bf16 v[96:99], v[168:171], v[128:131], v[96:99]\nTR 160,196,4096\nTR 162,196,4608\n"
"v_mfma_f32_16x16x32_bf16 v[100:103], v[168:171], v[132:135], v[100:103]\nv_mfma_f32_16x16x32_bf16 v[104:107], v[168:171], v[136:139], v[104:107]\n"
"v_mfma_f32_16x16x32_bf16 v[108:111], v[168:171], v[140:143], v[108:111]\ns_waitcnt lgkmcnt(2)\n"
"v_mfma_f32_16x16x32_bf16 v[112:115], v[172:175], v[128:131], v[112:115]\nTR 164,196,12288\nTR 166,196,12800\n"
"v_mfma_f32_16x16x32_bf16 v[116:119], v[172:175], v[132:135], v[116:119]\nv_mfma_f32_16x16x32_bf16 v[120:123], v[172:175], v[136:139], v[120:123]\n"
"v_mfma_f32_16x16x32_bf16 v[124:127], v[172:175], v[140:143], v[124:127]\ns_waitcnt lgkmcnt(2)\n"
"v_mfma_f32_16x16x32_bf16 v[64:67], v[160:163], v[144:147], v[64:67]\nTR 168,197,4096\nTR 170,197,4608\nTR 172,197,12288\nTR 174,197,12800\n"
"v_mfma_f32_16x16x32_bf16 v[68:71], v[160:163], v[148:151], v[68:71]\nv_mfma_f32_16x16x32_bf16 v[72:75], v[160:163], v[152:155], v[72:75]\n"
"v_mfma_f32_16x16x32_bf16 v[76:79], v[160:163], v[156:159], v[76:79]\ns_waitcnt lgkmcnt(4)\n"
"v_mfma_f32_16x16x32_bf16 v[80:83], v[164:167], v[144:147], v[80:83]\nv_mfma_f32_16x16x32_bf16 v[84:87], v[164:167], v[148:151], v[84:87]\n"
"v_mfma_f32_16x16x32_bf16 v[88:91], v[164:167], v[152:155], v[88:91]\nv_mfma_f32_16x16x32_bf16 v[92:95], v[164:167], v[156:159], v[92:95]\n"
"s_waitcnt vmcnt(12) lgkmcnt(0)\ns_barrier\nv_mfma_f32_16x16x32_bf16 v[96:99], v[168:171], v[144:147], v[96:99]\nTR 160,196,16384\nTR 162,196,16896\n"
"v_mfma_f32_16x16x32_bf16 v[100:103], v[168:171], v[148:151], v[100:103]\ns_add_u32 m0, s18, 0\ns_nop 0\nglobal_load_lds_dwordx4 v[210:211], off\n"
"v_lshl_add_u64 v[210:211], v[212:213], 0, v[210:211]\nv_mfma_f32_16x16x32_bf16 v[104:107], v[168:171], v[152:155], v[104:107]\n"
"v_mfma_f32_16x16x32_bf16 v[108:111], v[168:171], v[156:159], v[108:111]\nv_mfma_f32_16x16x32_bf16 v[112:115], v[172:175], v[144:147], v[112:115]\n"
"TR 164,197,16384\nTR 166,197,16896\nv_mfma_f32_16x16x32_bf16 v[116:119], v[172:175], v[148:151], v[116:119]\ns_add_u32 m0, s18, 0x800\ns_nop 0\n"
"global_load_lds_dwordx4 v[210:211], off\nv_lshl_add_u64 v[210:211], v[212:213], 0, v[210:211]\n"
"v_mfma_f32_16x16x32_bf16 v[120:123], v[172:175], v[152:155], v[120:123]\nv_mfma_f32_16x16x32_bf16 v[124:127], v[172:175], v[156:159], v[124:127]\n"
"s_waitcnt lgkmcnt(2)\nv_mfma_f32_16x16x32_bf16 v[0:3], v[160:163], a[0:3], 0\nTR 168,196,24576\nTR 170,196,25088\n"
"v_mfma_f32_16x16x32_bf16 v[4:7], v[160:163], a[4:7], 0\ns_add_u32 m0, s18, 0x1000\ns_nop 0\nglobal_load_lds_dwordx4 v[210:211], off\n"
"v_lshl_add_u64 v[210:211], v[212:213], 0, v[210:211]\nv_mfma_f32_16x16x32_bf16 v[8:11], v[160:163], a[8:11], 0\nMU 176,64,64\nMU 177,65,65\n"
"v_mfma_f32_16x16x32_bf16 v[12:15], v[160:163], a[12:15], 0\nMU 178,66,66\nMU 179,67,67\ns_waitcnt lgkmcnt(2)\n"
"v_mfma_f32_16x16x32_bf16 v[16:19], v[164:167], a[0:3], 0\nTR 172,197,24576\nTR 174,197,25088\n"
"v_mfma_f32_16x16x32_bf16 v[20:23], v[164:167], a[4:7], 0\ns_add_u32 m0, s18, 0x1800\ns_cmp_eq_u32 s78, 0x400\ns_cbranch_scc0 L_mx40\n"
"v_add_u32 v193, 0, v241\ns_mov_b64 exec, s[88:89]\nds_write_b16 v193, v242\nL_mx40:\nglobal_load_lds_dwordx4 v[210:211], off\ns_mov_b64 exec, -1\n"
"s_cmp_eq_u32 s79, 0\ns_cselect_b32 s22, 0xffffffdd, 1\nv_mad_i64_i32 v[210:211], s[26:27], s22, v212, v[210:211]\n"
"v_mfma_f32_16x16x32_bf16 v[24:27], v[164:167], a[8:11], 0\nv_fmaak_f32 v176, v252, v176, 0xc0135761\nv_fmaak_f32 v177, v252, v177, 0xc0135761\n"
"v_mfma_f32_16x16x32_bf16 v[28:31], v[164:167], a[12:15], 0\nv_fmaak_f32 v178, v252, v178, 0xc0135761\nv_fmaak_f32 v179, v252, v179, 0xc0135761\n"
"s_waitcnt lgkmcnt(2)\nv_mfma_f32_16x16x32_bf16 v[32:35], v[168:171], a[0:3], 0\nTR 160,196,20480\nTR 162,196,20992\n"
"v_mfma_f32_16x16x32_bf16 v[36:39], v[168:171], a[4:7], 0\nMU 80,64,80\nMU 81,65,81\nv_mfma_f32_16x16x32_bf16 v[40:43], v[168:171], a[8:11], 0\n"
"MU 82,66,82\nMU 83,67,83\nv_mfma_f32_16x16x32_bf16 v[44:47], v[168:171], a[12:15], 0\nMU 64,176,64\nMU 65,177,65\ns_waitcnt lgkmcnt(2)\n"
"v_mfma_f32_16x16x32_bf16 v[48:51], v[172:175], a[0:3], 0\nTR 164,197,20480\nTR 166,197,20992\n"
"v_mfma_f32_16x16x32_bf16 v[52:55], v[172:175], a[4:7], 0\nMU 66,178,66\nMU 67,179,67\nv_mfma_f32_16x16x32_bf16 v[56:59], v[172:175], a[8:11], 0\n"
"EX 64\nv_mfma_f32_16x16x32_bf16 v[60:63], v[172:175], a[12:15], 0\nEX 65\ns_waitcnt lgkmcnt(2)\nMA 0,160,16\nTR 168,196,28672\nTR 170,196,29184\n"
"TR 172,197,28672\nTR 174,197,29184\nMA 4,160,20\nds_read_b128 a[0:3], v208\nds_read_b128 a[4:7], v208 offset:1024\nMA 8,160,24\n"
"ds_read_b128 a[8:11], v208 offset:2048\nds_read_b128 a[12:15], v208 offset:3072\nMA 12,160,28\nEX 66\ns_waitcnt lgkmcnt(8)\nMA 16,164,16\nEX 67\n"
"MA 20,164,20\nv_add_f32 v64, 1.0, v64\nv_add_f32 v65, 1.0, v65\nMA 24,164,24\nv_add_f32 v66, 1.0, v66\nv_add_f32 v67, 1.0, v67\nMA 28,164,28\n"
"v_rcp_f32 v64, v64\ns_waitcnt vmcnt(12) lgkmcnt(0)\ns_barrier\nMA 32,168,16\nTR 160,196,32768\nTR 162,196,33280\nMA 36,168,20\n"
"s_add_u32 m0, s18, 0x4000\ns_nop 0\nglobal_load_lds_dwordx4 v[210:211], off\nv_lshl_add_u64 v[210:211], v[212:213], 0, v[210:211]\nMA 40,168,24\n"
"v_rcp_f32 v65, v65\nMA 44,168,28\nv_rcp_f32 v66, v66\nMA 48,172,16\nTR 164,197,32768\nTR 166,197,33280\nMA 52,172,20\ns_add_u32 m0, s18, 0x4800\n"
"s_nop 0\nglobal_load_lds_dwordx4 v[210:211], off\nv_lshl_add_u64 v[210:211], v[212:213], 0, v[210:211]\nMA 56,172,24\nv_rcp_f32 v67, v67\n"
"MA 60,172,28\nMU 80,80,64\nMU 81,81,65\ns_waitcnt lgkmcnt(2)\nMA 0,160,32\nTR 168,196,40960\nTR 170,196,41472\nMA 4,160,36\n"
"s_add_u32 m0, s18, 0x5000\ns_nop 0\nglobal_load_lds_dwordx4 v[210:211], off\nv_lshl_add_u64 v[210:211], v[212:213], 0, v[210:211]\nMA 8,160,40\n"
"ds_read_b128 a[16:19], v208 offset:4096\nds_read_b128 a[20:23], v208 offset:5120\nMA 12,160,44\nds_read_b128 a[24:27], v208 offset:6144\n"
"ds_read_b128 a[28:31], v208 offset:7168\ns_waitcnt lgkmcnt(6)\nMA 16,164,32\nTR 172,197,40960\nTR 174,197,41472\nMA 20,164,36\n"
"s_add_u32 m0, s18, 0x5800\ns_cmp_eq_u32 s79, 0x400\ns_cbranch_scc0 L_mx41\nv_add_u32 v193, 0x4000, v241\ns_mov_b64 exec, s[88:89]\n"
"ds_write_b16 v193, v242\nL_mx41:\nglobal_load_lds_dwordx4 v[210:211], off\ns_mov_b64 exec, -1\ns_cmp_eq_u32 s80, 0\ns_cselect_b32 s22, 0xffffffdd, 1\n"
"v_mad_i64_i32 v[210:211], s[26:27], s22, v212, v[210:211]\nMA 24,164,40\nMU 82,82,66\nMU 83,83,67\nMA 28,164,44\nMU 176,96,96\nMU 177,97,97\n"
"s_waitcnt lgkmcnt(6)\nMA 32,168,32\nTR 160,196,36864\nTR 162,196,37376\nMA 36,168,36\nMU 178,98,98\nMU 179,99,99\nMA 40,168,40\n"
"v_fmaak_f32 v176, v252, v176, 0xc0135761\nv_fmaak_f32 v177, v252, v177, 0xc0135761\nMA 44,168,44\nv_fmaak_f32 v178, v252, v178, 0xc0135761\n"
"v_fmaak_f32 v179, v252, v179, 0xc0135761\ns_waitcnt lgkmcnt(2)\nMA 48,172,32\nTR 164,197,36864\nTR 166,197,37376\nMA 52,172,36\nMU 112,96,112\n"
"MU 113,97,113\nMA 56,172,40\nMU 114,98,114\nMU 115,99,115\nMA 60,172,44\nMU 96,176,96\nMU 97,177,97\ns_waitcnt lgkmcnt(2)\nMA 0,160,48\n"
"TR 168,196,45056\nTR 170,196,45568\nTR 172,197,45056\nTR 174,197,45568\nMA 4,160,52\nds_read_b128 a[32:35], v208 offset:8192\n"
"ds_read_b128 a[36:39], v208 offset:9216\nMA 8,160,56\nds_read_b128 a[40:43], v208 offset:10240\nds_read_b128 a[44:47], v208 offset:11264\n"
"MA 12,160,60\nMU 98,178,98\nMU 99,179,99\ns_waitcnt lgkmcnt(8)\nMA 16,164,48\nEX 96\nMA 20,164,52\nEX 97\nMA 24,164,56\nEX 98\nMA 28,164,60\nEX 99\n"
"s_waitcnt vmcnt(12) lgkmcnt(0)\ns_barrier\nMA 32,168,48\nTR 160,196,49152\nTR 162,196,49664\nMA 36,168,52\ns_add_u32 m0, s18, 0x8000\ns_nop 0\n"
"global_load_lds_dwordx4 v[210:211], off\nv_lshl_add_u64 v[210:211], v[212:213], 0, v[210:211]\nMA 40,168,56\nv_add_f32 v96, 1.0, v96\n"
"v_add_f32 v97, 1.0, v97\nMA 44,168,60\nv_add_f32 v98, 1.0, v98\nv_add_f32 v99, 1.0, v99\nMA 48,172,48\nTR 164,197,49152\nTR 166,197,49664\n"
"MA 52,172,52\ns_add_u32 m0, s18, 0x8800\ns_nop 0\nglobal_load_lds_dwordx4 v[210:211], off\nv_lshl_add_u64 v[210:211], v[212:213], 0, v[210:211]\n"
"MA 56,172,56\nv_rcp_f32 v96, v96\nMA 60,172,60\nv_rcp_f32 v97, v97\ns_waitcnt lgkmcnt(2)\nMA 0,160,64\nTR 168,196,57344\nTR 170,196,57856\n"
"MA 4,160,68\ns_add_u32 m0, s18, 0x9000\ns_nop 0\nglobal_load_lds_dwordx4 v[210:211], off\nv_lshl_add_u64 v[210:211], v[212:213], 0, v[210:211]\n"
"MA 8,160,72\nds_read_b128 a[48:51], v208 offset:12288\nds_read_b128 a[52:55], v208 offset:13312\nMA 12,160,76\n"
"ds_read_b128 a[56:59], v208 offset:14336\nds_read_b128 a[60:63], v208 offset:15360\ns_waitcnt lgkmcnt(6)\nMA 16,164,64\nTR 172,197,57344\n"
"TR 174,197,57856\nMA 20,164,68\ns_add_u32 m0, s18, 0x9800\ns_cmp_eq_u32 s80, 0x400\ns_cbranch_scc0 L_mx42\nv_add_u32 v193, 0x8000, v241\n"
"s_mov_b64 exec, s[88:89]\nds_write_b16 v193, v242\nL_mx42:\nglobal_load_lds_dwordx4 v[210:211], off\ns_mov_b64 exec, -1\ns_cmp_eq_u32 s81, 0\n"
"s_cselect_b32 s22, 0xffffffdd, 1\nv_mad_i64_i32 v[210:211], s[26:27], s22, v212, v[210:211]\nMA 24,164,72\nv_rcp_f32 v98, v98\nMA 28,164,76\n"
"v_rcp_f32 v99, v99\ns_waitcnt lgkmcnt(6)\nMA 32,168,64\nTR 160,196,53248\nTR 162,196,53760\nMA 36,168,68\nMU 112,112,96\nMU 113,113,97\nMA 40,168,72\n"
"MU 114,114,98\nMU 115,115,99\nMA 44,168,76\nv_cvt_pk_bf16_f32 v64, v80, v81\nv_cvt_pk_bf16_f32 v65, v82, v83\ns_waitcnt lgkmcnt(2)\nMA 48,172,64\n"
"TR 164,197,53248\nTR 166,197,53760\nMA 52,172,68\nv_cvt_pk_bf16_f32 v66, v112, v113\nv_cvt_pk_bf16_f32 v67, v114, v115\nMA 56,172,72\n"
"ds_write_b128 v206, v[64:67] offset:24576\nMU 176,68,68\nMA 60,172,76\nMU 177,69,69\nMU 178,70,70\ns_waitcnt lgkmcnt(3)\nMA 0,160,80\n"
"TR 168,196,61440\nTR 170,196,61952\nTR 172,197,61440\nTR 174,197,61952\nMA 4,160,84\nds_read_b128 a[64:67], v208 offset:16384\n"
"ds_read_b128 a[68:71], v208 offset:17408\nMA 8,160,88\nds_read_b128 a[72:75], v208 offset:18432\nds_read_b128 a[76:79], v208 offset:19456\n"
"MA 12,160,92\nMU 179,71,71\nv_fmaak_f32 v176, v252, v176, 0xc0135761\ns_waitcnt lgkmcnt(9)\nMA 16,164,80\nv_fmaak_f32 v177, v252, v177, 0xc0135761\n"
"v_fmaak_f32 v178, v252, v178, 0xc0135761\nMA 20,164,84\nv_fmaak_f32 v179, v252, v179, 0xc0135761\nMU 84,68,84\nMA 24,164,88\nMU 85,69,85\n"
"MU 86,70,86\nMA 28,164,92\nMU 87,71,87\nMU 68,176,68\ns_waitcnt vmcnt(12) lgkmcnt(0)\ns_barrier\nMA 32,168,80\nds_read_b64_tr_b16 v[160:161], v198\n"
"TR 162,198,512\nMA 36,168,84\ns_add_u32 m0, s18, 0xc000\ns_nop 0\nglobal_load_lds_dwordx4 v[210:211], off\n"
"v_lshl_add_u64 v[210:211], v[212:213], 0, v[210:211]\nMA 40,168,88\nMU 69,177,69\nMU 70,178,70\nMA 44,168,92\nMU 71,179,71\nEX 68\nMA 48,172,80\n"
"ds_read_b64_tr_b16 v[164:165], v199\nTR 166,199,512\nMA 52,172,84\ns_add_u32 m0, s18, 0xc800\ns_nop 0\nglobal_load_lds_dwordx4 v[210:211], off\n"
"v_lshl_add_u64 v[210:211], v[212:213], 0, v[210:211]\nMA 56,172,88\nEX 69\nMA 60,172,92\nEX 70\ns_waitcnt lgkmcnt(2)\nMA 0,160,96\nTR 168,198,8192\n"
"TR 170,198,8704\nMA 4,160,100\ns_add_u32 m0, s18, 0xd000\ns_nop 0\nglobal_load_lds_dwordx4 v[210:211], off\n"
"v_lshl_add_u64 v[210:211], v[212:213], 0, v[210:211]\nMA 8,160,104\nds_read_b128 a[80:83], v208 offset:20480\n"
"ds_read_b128 a[84:87], v208 offset:21504\nMA 12,160,108\nds_read_b128 a[88:91], v208 offset:22528\nds_read_b128 a[92:95], v208 offset:23552\n"
"s_waitcnt lgkmcnt(6)\nMA 16,164,96\nTR 172,199,8192\nTR 174,199,8704\nMA 20,164,100\ns_add_u32 m0, s18, 0xd800\ns_cmp_eq_u32 s81, 0x400\n"
"s_cbranch_scc0 L_mx43\nv_add_u32 v193, 0xc000, v241\ns_mov_b64 exec, s[88:89]\nds_write_b16 v193, v242\nL_mx43:\n"
"global_load_lds_dwordx4 v[210:211], off\ns_mov_b64 exec, -1\ns_cmp_eq_u32 s19, 0\ns_cselect_b32 s22, 0xffffffdd, 1\n"
"v_mad_i64_i32 v[210:211], s[26:27], s22, v212, v[210:211]\nMA 24,164,104\nEX 71\nMA 28,164,108\nv_add_f32 v68, 1.0, v68\nv_add_f32 v69, 1.0, v69\n"
"s_waitcnt lgkmcnt(6)\nMA 32,168,96\nTR 160,198,4096\nTR 162,198,4608\nMA 36,168,100\nv_add_f32 v70, 1.0, v70\nv_add_f32 v71, 1.0, v71\nMA 40,168,104\n"
"v_rcp_f32 v68, v68\nMA 44,168,108\nv_rcp_f32 v69, v69\ns_waitcnt lgkmcnt(2)\nMA 48,172,96\nTR 164,199,4096\nTR 166,199,4608\nMA 52,172,100\n"
"v_rcp_f32 v70, v70\nMA 56,172,104\nv_rcp_f32 v71, v71\nMA 60,172,108\nMU 84,84,68\nMU 85,85,69\ns_waitcnt lgkmcnt(2)\nMA 0,160,112\nTR 168,198,12288\n"
"TR 170,198,12800\nTR 172,199,12288\nTR 174,199,12800\nMA 4,160,116\nMU 86,86,70\nMU 87,87,71\nMA 8,160,120\nMU 176,100,100\nMU 177,101,101\n"
"MA 12,160,124\nMU 178,102,102\nMU 179,103,103\ns_waitcnt lgkmcnt(4)\nMA 16,164,112\nv_fmaak_f32 v176, v252, v176, 0xc0135761\n"
"v_fmaak_f32 v177, v252, v177, 0xc0135761\nMA 20,164,116\nv_fmaak_f32 v178, v252, v178, 0xc0135761\nv_fmaak_f32 v179, v252, v179, 0xc0135761\n"
"MA 24,164,120\nMU 116,100,116\nMU 117,101,117\nMA 28,164,124\nMU 118,102,118\nMU 119,103,119\ns_waitcnt vmcnt(12) lgkmcnt(0)\ns_barrier\n"
"MA 32,168,112\nds_read_b64_tr_b16 v[160:161], v196\nTR 162,196,512\nMA 36,168,116\ns_add_u32 m0, s18, 0x10000\ns_nop 0\n"
"global_load_lds_dwordx4 v[210:211], off\nv_lshl_add_u64 v[210:211], v[212:213], 0, v[210:211]\nMA 40,168,120\nMU 100,176,100\nMU 101,177,101\n"
"MA 44,168,124\nMU 102,178,102\nMU 103,179,103\nMA 48,172,112\nds_read_b64_tr_b16 v[164:165], v197\nTR 166,197,512\nMA 52,172,116\n"
"s_add_u32 m0, s18, 0x10800\ns_nop 0\nglobal_load_lds_dwordx4 v[210:211], off\nv_lshl_add_u64 v[210:211], v[212:213], 0, v[210:211]\nMA 56,172,120\n"
"EX 100\nMA 60,172,124\nEX 101\ns_waitcnt lgkmcnt(2)\nMA 0,160,128\nTR 168,196,8192\nTR 170,196,8704\nMA 4,160,132\ns_add_u32 m0, s18, 0x11000\n"
"s_nop 0\nglobal_load_lds_dwordx4 v[210:211], off\nv_lshl_add_u64 v[210:211], v[212:213], 0, v[210:211]\nMA 8,160,136\nEX 102\nMA 12,160,140\nEX 103\n"
"s_waitcnt lgkmcnt(2)\nMA 16,164,128\nTR 172,197,8192\nTR 174,197,8704\nMA 20,164,132\ns_add_u32 m0, s18, 0x11800\ns_cmp_eq_u32 s19, 0x400\n"
"s_cbranch_scc0 L_mx44\nv_add_u32 v193, 0x10000, v241\ns_mov_b64 exec, s[88:89]\nds_write_b16 v193, v242\nL_mx44:\n"
"global_load_lds_dwordx4 v[210:211], off\ns_mov_b64 exec, -1\nMA 24,164,136\nv_add_f32 v100, 1.0, v100\nv_add_f32 v101, 1.0, v101\nMA 28,164,140\n"
"v_add_f32 v102, 1.0, v102\nv_add_f32 v103, 1.0, v103\ns_waitcnt lgkmcnt(2)\nMA 32,168,128\nTR 160,196,4096\nTR 162,196,4608\nMA 36,168,132\n"
"v_rcp_f32 v100, v100\nMA 40,168,136\nv_rcp_f32 v101, v101\nMA 44,168,140\nv_rcp_f32 v102, v102\ns_waitcnt lgkmcnt(2)\nMA 48,172,128\nTR 164,197,4096\n"
"TR 166,197,4608\nMA 52,172,132\nv_rcp_f32 v103, v103\nMA 56,172,136\nMU 116,116,100\nMU 117,117,101\nMA 60,172,140\nMU 118,118,102\nMU 119,119,103\n"
"s_waitcnt lgkmcnt(2)\nMA 0,160,144\nTR 168,196,12288\nTR 170,196,12800\nTR 172,197,12288\nTR 174,197,12800\nMA 4,160,148\n"
"v_cvt_pk_bf16_f32 v68, v84, v85\nv_cvt_pk_bf16_f32 v69, v86, v87\nMA 8,160,152\nv_cvt_pk_bf16_f32 v70, v116, v117\nv_cvt_pk_bf16_f32 v71, v118, v119\n"
"MA 12,160,156\nds_write_b128 v206, v[68:71] offset:25600\nMU 176,72,72\ns_waitcnt lgkmcnt(5)\nMA 16,164,144\nMU 177,73,73\nMU 178,74,74\n"
"MA 20,164,148\nMU 179,75,75\nv_fmaak_f32 v176, v252, v176, 0xc0135761\nMA 24,164,152\nv_fmaak_f32 v177, v252, v177, 0xc0135761\n"
"v_fmaak_f32 v178, v252, v178, 0xc0135761\nMA 28,164,156\nv_fmaak_f32 v179, v252, v179, 0xc0135761\nMU 88,72,88\ns_waitcnt vmcnt(12) lgkmcnt(0)\n"
"s_barrier\nMA 32,168,144\nTR 160,196,16384\nTR 162,196,16896\nMA 36,168,148\ns_add_u32 m0, s5, 0\ns_add_u32 s86, s0, 0\n"
"buffer_load_dwordx4 v214, s[44:47], s86 offen lds\nMA 40,168,152\nMU 89,73,89\nMU 90,74,90\nMA 44,168,156\nMU 91,75,91\nMU 72,176,72\nMA 48,172,144\n"
"TR 164,197,16384\nTR 166,197,16896\nMA 52,172,148\nbuffer_load_dwordx4 v215, s[44:47], s86 offen offset:1024 lds\nMU 73,177,73\nMA 56,172,152\n"
"MU 74,178,74\nMU 75,179,75\nMA 60,172,156\nEX 72\ns_waitcnt lgkmcnt(2)\nMA 0,160,160\nTR 168,196,24576\nTR 170,196,25088\nMA 4,160,164\n"
"buffer_load_dwordx4 v216, s[44:47], s86 offen offset:2048 lds\nEX 73\nMA 8,160,168\nEX 74\nMA 12,160,172\nEX 75\ns_waitcnt lgkmcnt(2)\nMA 16,164,160\n"
"TR 172,197,24576\nTR 174,197,25088\nMA 20,164,164\nv_add_f32 v72, 1.0, v72\nv_add_f32 v73, 1.0, v73\nMA 24,164,168\nv_add_f32 v74, 1.0, v74\n"
"v_add_f32 v75, 1.0, v75\nMA 28,164,172\nv_rcp_f32 v72, v72\ns_waitcnt lgkmcnt(2)\nMA 32,168,160\nTR 160,196,20480\nTR 162,196,20992\nMA 36,168,164\n"
"v_rcp_f32 v73, v73\nMA 40,168,168\nv_rcp_f32 v74, v74\nMA 44,168,172\nv_rcp_f32 v75, v75\ns_waitcnt lgkmcnt(2)\nMA 48,172,160\nTR 164,197,20480\n"
"TR 166,197,20992\nMA 52,172,164\nMU 88,88,72\nMU 89,89,73\nMA 56,172,168\nMU 90,90,74\nMU 91,91,75\nMA 60,172,172\nMU 176,104,104\nMU 177,105,105\n"
"s_waitcnt lgkmcnt(2)\nMA 0,160,176\nTR 168,196,28672\nTR 170,196,29184\nTR 172,197,28672\nTR 174,197,29184\nMA 4,160,180\nMU 178,106,106\n"
"MU 179,107,107\nMA 8,160,184\nv_fmaak_f32 v176, v252, v176, 0xc0135761\nv_fmaak_f32 v177, v252, v177, 0xc0135761\nMA 12,160,188\n"
"v_fmaak_f32 v178, v252, v178, 0xc0135761\nv_fmaak_f32 v179, v252, v179, 0xc0135761\ns_waitcnt lgkmcnt(4)\nMA 16,164,176\nMU 120,104,120\n"
"MU 121,105,121\nMA 20,164,180\nMU 122,106,122\nMU 123,107,123\nMA 24,164,184\nMU 104,176,104\nMU 105,177,105\nMA 28,164,188\nMU 106,178,106\n"
"MU 107,179,107\ns_waitcnt vmcnt(11) lgkmcnt(0)\ns_barrier\nMA 32,168,176\nTR 160,196,32768\nTR 162,196,33280\nMA 36,168,180\n"
"s_add_u32 m0, s5, 0x3000\ns_add_u32 s86, s0, 0x1b000\nbuffer_load_dwordx4 v214, s[44:47], s86 offen lds\nMA 40,168,184\nEX 104\nMA 44,168,188\n"
"EX 105\nMA 48,172,176\nTR 164,197,32768\nTR 166,197,33280\nMA 52,172,180\nbuffer_load_dwordx4 v215, s[44:47], s86 offen offset:1024 lds\nEX 106\n"
"MA 56,172,184\nEX 107\nMA 60,172,188\nv_add_f32 v104, 1.0, v104\nv_add_f32 v105, 1.0, v105\ns_waitcnt lgkmcnt(2)\nMA 0,160,192\nTR 168,196,40960\n"
"TR 170,196,41472\nMA 4,160,196\nbuffer_load_dwordx4 v216, s[44:47], s86 offen offset:2048 lds\nv_add_f32 v106, 1.0, v106\nMA 8,160,200\n"
"v_add_f32 v107, 1.0, v107\nv_rcp_f32 v104, v104\nMA 12,160,204\nv_rcp_f32 v105, v105\ns_waitcnt lgkmcnt(2)\nMA 16,164,192\nTR 172,197,40960\n"
"TR 174,197,41472\nMA 20,164,196\nv_rcp_f32 v106, v106\nMA 24,164,200\nv_rcp_f32 v107, v107\nMA 28,164,204\nMU 120,120,104\nMU 121,121,105\n"
"s_waitcnt lgkmcnt(2)\nMA 32,168,192\nTR 160,196,36864\nTR 162,196,37376\nMA 36,168,196\nMU 122,122,106\nMU 123,123,107\nMA 40,168,200\n"
"v_cvt_pk_bf16_f32 v72, v88, v89\nv_cvt_pk_bf16_f32 v73, v90, v91\nMA 44,168,204\nv_cvt_pk_bf16_f32 v74, v120, v121\n"
"v_cvt_pk_bf16_f32 v75, v122, v123\ns_waitcnt lgkmcnt(2)\nMA 48,172,192\nTR 164,197,36864\nTR 166,197,37376\nMA 52,172,196\n"
"ds_write_b128 v206, v[72:75] offset:26624\nMU 176,76,76\nMA 56,172,200\nMU 177,77,77\nMU 178,78,78\nMA 60,172,204\nMU 179,79,79\n"
"v_fmaak_f32 v176, v252, v176, 0xc0135761\ns_waitcnt lgkmcnt(3)\nMA 0,160,208\nTR 168,196,45056\nTR 170,196,45568\nTR 172,197,45056\nTR 174,197,45568\n"
"MA 4,160,212\nv_fmaak_f32 v177, v252, v177, 0xc0135761\nv_fmaak_f32 v178, v252, v178, 0xc0135761\nMA 8,160,216\n"
"v_fmaak_f32 v179, v252, v179, 0xc0135761\nMU 92,76,92\nMA 12,160,220\nMU 93,77,93\nMU 94,78,94\ns_waitcnt lgkmcnt(5)\nMA 16,164,208\nMU 95,79,95\n"
"MU 76,176,76\nMA 20,164,212\nMU 77,177,77\nMU 78,178,78\nMA 24,164,216\nMU 79,179,79\nEX 76\nMA 28,164,220\nEX 77\ns_waitcnt vmcnt(10) lgkmcnt(0)\n"
"s_barrier\nMA 32,168,208\nTR 160,196,49152\nTR 162,196,49664\nMA 36,168,212\ns_add_u32 m0, s5, 0x6000\ns_add_u32 s86, s1, 0\n"
"buffer_load_dwordx4 v214, s[44:47], s86 offen lds\nMA 40,168,216\nEX 78\nMA 44,168,220\nEX 79\nMA 48,172,208\nTR 164,197,49152\nTR 166,197,49664\n"
"MA 52,172,212\nbuffer_load_dwordx4 v215, s[44:47], s86 offen offset:1024 lds\nv_add_f32 v76, 1.0, v76\nMA 56,172,216\nv_add_f32 v77, 1.0, v77\n"
"v_add_f32 v78, 1.0, v78\nMA 60,172,220\nv_add_f32 v79, 1.0, v79\nv_rcp_f32 v76, v76\ns_waitcnt lgkmcnt(2)\nMA 0,160,224\nTR 168,196,57344\n"
"TR 170,196,57856\nMA 4,160,228\nbuffer_load_dwordx4 v216, s[44:47], s86 offen offset:2048 lds\nv_rcp_f32 v77, v77\nMA 8,160,232\nv_rcp_f32 v78, v78\n"
"MA 12,160,236\nv_rcp_f32 v79, v79\ns_waitcnt lgkmcnt(2)\nMA 16,164,224\nTR 172,197,57344\nTR 174,197,57856\nMA 20,164,228\ns_add_u32 m0, s5, 0x9000\n"
"s_add_u32 s86, s1, 0x1b000\nbuffer_load_dwordx4 v214, s[44:47], s86 offen lds\nMA 24,164,232\nMU 92,92,76\nMU 93,93,77\nMA 28,164,236\nMU 94,94,78\n"
"MU 95,95,79\ns_waitcnt lgkmcnt(2)\nMA 32,168,224\nTR 160,196,53248\nTR 162,196,53760\nMA 36,168,228\n"
"buffer_load_dwordx4 v215, s[44:47], s86 offen offset:1024 lds\nMU 176,108,108\nMA 40,168,232\nMU 177,109,109\nMU 178,110,110\nMA 44,168,236\n"
"MU 179,111,111\nv_fmaak_f32 v176, v252, v176, 0xc0135761\ns_waitcnt lgkmcnt(2)\nMA 48,172,224\nTR 164,197,53248\nTR 166,197,53760\nMA 52,172,228\n"
"buffer_load_dwordx4 v216, s[44:47], s86 offen offset:2048 lds\nv_fmaak_f32 v177, v252, v177, 0xc0135761\nMA 56,172,232\n"
"v_fmaak_f32 v178, v252, v178, 0xc0135761\nv_fmaak_f32 v179, v252, v179, 0xc0135761\nMA 60,172,236\nMU 124,108,124\nMU 125,109,125\n"
"s_waitcnt lgkmcnt(2)\nMA 0,160,240\nTR 168,196,61440\nTR 170,196,61952\nTR 172,197,61440\nTR 174,197,61952\nMA 4,160,244\nMU 126,110,126\n"
"MU 127,111,127\nMA 8,160,248\nMU 108,176,108\nMU 109,177,109\nMA 12,160,252\nMU 110,178,110\nMU 111,179,111\ns_waitcnt lgkmcnt(4)\nMA 16,164,240\n"
"EX 108\nMA 20,164,244\nEX 109\nMA 24,164,248\nEX 110\nMA 28,164,252\nEX 111\nv_add_f32 v108, 1.0, v108\nv_add_f32 v109, 1.0, v109\n"
"v_add_f32 v110, 1.0, v110\nv_add_f32 v111, 1.0, v111\nv_rcp_f32 v108, v108\nv_rcp_f32 v109, v109\nv_rcp_f32 v110, v110\nv_rcp_f32 v111, v111\n"
"MU 124,124,108\nMU 125,125,109\nMU 126,126,110\nMU 127,127,111\nv_cvt_pk_bf16_f32 v76, v92, v93\nv_cvt_pk_bf16_f32 v77, v94, v95\n"
"v_cvt_pk_bf16_f32 v78, v124, v125\nv_cvt_pk_bf16_f32 v79, v126, v127\nds_write_b128 v206, v[76:79] offset:27648\ns_waitcnt vmcnt(12) lgkmcnt(0)\n"
"s_barrier\nMA 32,168,240\nds_read_b64_tr_b16 v[160:161], v198\nTR 162,198,512\nMA 36,168,244\ns_add_u32 m0, s5, 0xc000\ns_add_u32 s86, s0, 0\n"
"buffer_load_dwordx4 v217, s[44:47], s86 offen lds\nMA 40,168,248\nds_read_b128 a[96:99], v208 offset:24576\n"
"ds_read_b128 a[100:103], v208 offset:25600\nMA 44,168,252\nds_read_b128 a[104:107], v208 offset:26624\nds_read_b128 a[108:111], v208 offset:27648\n"
"MA 48,172,240\nds_read_b64_tr_b16 v[164:165], v199\nTR 166,199,512\nMA 52,172,244\nbuffer_load_dwordx4 v218, s[44:47], s86 offen offset:1024 lds\n"
"ds_read_b128 a[112:115], v208 offset:28672\nMA 56,172,248\nds_read_b128 a[116:119], v208 offset:29696\nds_read_b128 a[120:123], v208 offset:30720\n"
"MA 60,172,252\nds_read_b128 a[124:127], v208 offset:31744\ns_waitcnt lgkmcnt(10)\nv_mfma_f32_16x16x32_bf16 v[0:3], v[160:163], v[128:131], v[0:3]\n"
"TR 168,198,8192\nTR 170,198,8704\nv_mfma_f32_16x16x32_bf16 v[4:7], v[160:163], v[132:135], v[4:7]\n"
"buffer_load_dwordx4 v219, s[44:47], s86 offen offset:2048 lds\nv_mfma_f32_16x16x32_bf16 v[8:11], v[160:163], v[136:139], v[8:11]\n"
"v_mfma_f32_16x16x32_bf16 v[12:15], v[160:163], v[140:143], v[12:15]\ns_waitcnt lgkmcnt(6)\n"
"v_mfma_f32_16x16x32_bf16 v[16:19], v[164:167], v[128:131], v[16:19]\nTR 172,199,8192\nTR 174,199,8704\n"
"v_mfma_f32_16x16x32_bf16 v[20:23], v[164:167], v[132:135], v[20:23]\nv_mfma_f32_16x16x32_bf16 v[24:27], v[164:167], v[136:139], v[24:27]\n"
"v_mfma_f32_16x16x32_bf16 v[28:31], v[164:167], v[140:143], v[28:31]\ns_waitcnt lgkmcnt(2)\n"
"v_mfma_f32_16x16x32_bf16 v[32:35], v[168:171], v[128:131], v[32:35]\nTR 160,198,4096\nTR 162,198,4608\n"
"v_mfma_f32_16x16x32_bf16 v[36:39], v[168:171], v[132:135], v[36:39]\nv_mfma_f32_16x16x32_bf16 v[40:43], v[168:171], v[136:139], v[40:43]\n"
"v_mfma_f32_16x16x32_bf16 v[44:47], v[168:171], v[140:143], v[44:47]\ns_waitcnt lgkmcnt(2)\n"
"v_mfma_f32_16x16x32_bf16 v[48:51], v[172:175], v[128:131], v[48:51]\nTR 164,199,4096\nTR 166,199,4608\n"
"v_mfma_f32_16x16x32_bf16 v[52:55], v[172:175], v[132:135], v[52:55]\nv_mfma_f32_16x16x32_bf16 v[56:59], v[172:175], v[136:139], v[56:59]\n"
"v_mfma_f32_16x16x32_bf16 v[60:63], v[172:175], v[140:143], v[60:63]\ns_waitcnt lgkmcnt(2)\n"
"v_mfma_f32_16x16x32_bf16 v[0:3], v[160:163], v[144:147], v[0:3]\nTR 168,198,12288\nTR 170,198,12800\nTR 172,199,12288\nTR 174,199,12800\n"
"v_mfma_f32_16x16x32_bf16 v[4:7], v[160:163], v[148:151], v[4:7]\nv_mfma_f32_16x16x32_bf16 v[8:11], v[160:163], v[152:155], v[8:11]\n"
"v_mfma_f32_16x16x32_bf16 v[12:15], v[160:163], v[156:159], v[12:15]\ns_waitcnt lgkmcnt(4)\n"
"v_mfma_f32_16x16x32_bf16 v[16:19], v[164:167], v[144:147], v[16:19]\nv_mfma_f32_16x16x32_bf16 v[20:23], v[164:167], v[148:151], v[20:23]\n"
"v_mfma_f32_16x16x32_bf16 v[24:27], v[164:167], v[152:155], v[24:27]\nv_mfma_f32_16x16x32_bf16 v[28:31], v[164:167], v[156:159], v[28:31]\n"
"s_waitcnt vmcnt(12) lgkmcnt(0)\ns_barrier\nv_mfma_f32_16x16x32_bf16 v[32:35], v[168:171], v[144:147], v[32:35]\nds_read_b64_tr_b16 v[160:161], v200\n"
"TR 162,200,2048\nv_mfma_f32_16x16x32_bf16 v[36:39], v[168:171], v[148:151], v[36:39]\ns_add_u32 m0, s5, 0xf000\ns_add_u32 s86, s1, 0\n"
"buffer_load_dwordx4 v217, s[44:47], s86 offen lds\nv_mfma_f32_16x16x32_bf16 v[40:43], v[168:171], v[152:155], v[40:43]\n"
"v_mfma_f32_16x16x32_bf16 v[44:47], v[168:171], v[156:159], v[44:47]\nv_mfma_f32_16x16x32_bf16 v[48:51], v[172:175], v[144:147], v[48:51]\n"
"ds_read_b64_tr_b16 v[164:165], v201\nTR 166,201,2048\nv_mfma_f32_16x16x32_bf16 v[52:55], v[172:175], v[148:151], v[52:55]\n"
"buffer_load_dwordx4 v218, s[44:47], s86 offen offset:1024 lds\nv_mfma_f32_16x16x32_bf16 v[56:59], v[172:175], v[152:155], v[56:59]\n"
"v_mfma_f32_16x16x32_bf16 v[60:63], v[172:175], v[156:159], v[60:63]\ns_waitcnt lgkmcnt(2)\nv_mfma_f32_16x16x32_bf16 v[64:67], v[160:163], a[0:3], 0\n"
"TR 168,200,4096\nTR 170,200,6144\nv_mfma_f32_16x16x32_bf16 v[68:71], v[160:163], a[4:7], 0\n"
"buffer_load_dwordx4 v219, s[44:47], s86 offen offset:2048 lds\nMU 176,0,0\nv_mfma_f32_16x16x32_bf16 v[72:75], v[160:163], a[8:11], 0\nMU 177,1,1\n"
"MU 178,2,2\nv_mfma_f32_16x16x32_bf16 v[76:79], v[160:163], a[12:15], 0\nMU 179,3,3\nv_fmaak_f32 v176, v252, v176, 0xc0135761\ns_waitcnt lgkmcnt(2)\n"
"v_mfma_f32_16x16x32_bf16 v[80:83], v[164:167], a[0:3], 0\nTR 172,201,4096\nTR 174,201,6144\nv_mfma_f32_16x16x32_bf16 v[84:87], v[164:167], a[4:7], 0\n"
"s_add_u32 m0, s5, 0x12000\ns_add_u32 s86, s2, 0\nbuffer_load_dwordx4 v214, s[44:47], s86 offen lds\n"
"v_mfma_f32_16x16x32_bf16 v[88:91], v[164:167], a[8:11], 0\nv_fmaak_f32 v177, v252, v177, 0xc0135761\nv_fmaak_f32 v178, v252, v178, 0xc0135761\n"
"v_mfma_f32_16x16x32_bf16 v[92:95], v[164:167], a[12:15], 0\nv_fmaak_f32 v179, v252, v179, 0xc0135761\nMU 16,0,16\ns_waitcnt lgkmcnt(2)\nMA 64,168,16\n"
"TR 160,200,8192\nTR 162,200,10240\nTR 164,201,8192\nTR 166,201,10240\nMA 68,168,20\nbuffer_load_dwordx4 v215, s[44:47], s86 offen offset:1024 lds\n"
"MU 17,1,17\nMA 72,168,24\nMU 18,2,18\nMU 19,3,19\nMA 76,168,28\nMU 0,176,0\nMU 1,177,1\ns_waitcnt lgkmcnt(4)\nMA 80,172,16\nMU 2,178,2\nMU 3,179,3\n"
"MA 84,172,20\nbuffer_load_dwordx4 v216, s[44:47], s86 offen offset:2048 lds\nEX 0\nMA 88,172,24\nEX 1\nMA 92,172,28\nEX 2\n"
"s_waitcnt vmcnt(15) lgkmcnt(0)\ns_barrier\nMA 64,160,32\nTR 168,200,12288\nTR 170,200,14336\nMA 68,160,36\ns_add_u32 m0, s5, 0\n"
"s_add_u32 s86, s2, 0x1b000\nbuffer_load_dwordx4 v214, s[44:47], s86 offen lds\nMA 72,160,40\nEX 3\nMA 76,160,44\nv_add_f32 v0, 1.0, v0\n"
"v_add_f32 v1, 1.0, v1\nMA 80,164,32\nTR 172,201,12288\nTR 174,201,14336\nMA 84,164,36\nbuffer_load_dwordx4 v215, s[44:47], s86 offen offset:1024 lds\n"
"v_add_f32 v2, 1.0, v2\nMA 88,164,40\nv_add_f32 v3, 1.0, v3\nv_rcp_f32 v0, v0\nMA 92,164,44\nv_rcp_f32 v1, v1\ns_waitcnt lgkmcnt(2)\nMA 64,168,48\n"
"TR 160,200,16384\nTR 162,200,18432\nMA 68,168,52\nbuffer_load_dwordx4 v216, s[44:47], s86 offen offset:2048 lds\nv_rcp_f32 v2, v2\nMA 72,168,56\n"
"v_rcp_f32 v3, v3\nMA 76,168,60\nMU 16,16,0\nMU 17,17,1\ns_waitcnt lgkmcnt(2)\nMA 80,172,48\nTR 164,201,16384\nTR 166,201,18432\nMA 84,172,52\n"
"MU 18,18,2\nMU 19,19,3\nMA 88,172,56\nv_cvt_pk_bf16_f32 v0, v16, v17\nv_cvt_pk_bf16_f32 v1, v18, v19\nMA 92,172,60\nds_write_b64 v207, v[0:1]\n"
"MU 176,4,4\ns_waitcnt lgkmcnt(3)\nMA 64,160,64\nTR 168,200,20480\nTR 170,200,22528\nTR 172,201,20480\nTR 174,201,22528\nMA 68,160,68\nMU 177,5,5\n"
"MU 178,6,6\nMA 72,160,72\nMU 179,7,7\nv_fmaak_f32 v176, v252, v176, 0xc0135761\nMA 76,160,76\nv_fmaak_f32 v177, v252, v177, 0xc0135761\n"
"v_fmaak_f32 v178, v252, v178, 0xc0135761\ns_waitcnt lgkmcnt(5)\nMA 80,164,64\nv_fmaak_f32 v179, v252, v179, 0xc0135761\nMU 20,4,20\nMA 84,164,68\n"
"MU 21,5,21\nMU 22,6,22\nMA 88,164,72\nMU 23,7,23\nMU 4,176,4\nMA 92,164,76\nMU 5,177,5\nMU 6,178,6\ns_waitcnt vmcnt(15) lgkmcnt(0)\ns_barrier\n"
"MA 64,168,80\nTR 160,200,24576\nTR 162,200,26624\nMA 68,168,84\ns_add_u32 m0, s5, 0x3000\ns_add_u32 s86, s2, 0\n"
"buffer_load_dwordx4 v217, s[44:47], s86 offen lds\nMA 72,168,88\nMU 7,179,7\nEX 4\nMA 76,168,92\nEX 5\nMA 80,172,80\nTR 164,201,24576\n"
"TR 166,201,26624\nMA 84,172,84\nbuffer_load_dwordx4 v218, s[44:47], s86 offen offset:1024 lds\nEX 6\nMA 88,172,88\nEX 7\nMA 92,172,92\n"
"v_add_f32 v4, 1.0, v4\nv_add_f32 v5, 1.0, v5\ns_waitcnt lgkmcnt(2)\nv_mfma_f32_16x16x32_bf16 v[96:99], v[160:163], a[0:3], 0\nTR 168,200,28672\n"
"TR 170,200,30720\nv_mfma_f32_16x16x32_bf16 v[100:103], v[160:163], a[4:7], 0\nbuffer_load_dwordx4 v219, s[44:47], s86 offen offset:2048 lds\n"
"v_add_f32 v6, 1.0, v6\nv_mfma_f32_16x16x32_bf16 v[104:107], v[160:163], a[8:11], 0\nv_add_f32 v7, 1.0, v7\nv_rcp_f32 v4, v4\n"
"v_mfma_f32_16x16x32_bf16 v[108:111], v[160:163], a[12:15], 0\nv_rcp_f32 v5, v5\ns_waitcnt lgkmcnt(2)\n"
"v_mfma_f32_16x16x32_bf16 v[112:115], v[164:167], a[0:3], 0\nTR 172,201,28672\nTR 174,201,30720\n"
"v_mfma_f32_16x16x32_bf16 v[116:119], v[164:167], a[4:7], 0\nv_rcp_f32 v6, v6\nv_mfma_f32_16x16x32_bf16 v[120:123], v[164:167], a[8:11], 0\n"
"v_rcp_f32 v7, v7\nv_mfma_f32_16x16x32_bf16 v[124:127], v[164:167], a[12:15], 0\nMU 20,20,4\nMU 21,21,5\ns_waitcnt lgkmcnt(2)\nMA 96,168,16\n"
"TR 160,200,32768\nTR 162,200,34816\nTR 164,201,32768\nTR 166,201,34816\nMA 100,168,20\nMU 22,22,6\nMU 23,23,7\nMA 104,168,24\n"
"v_cvt_pk_bf16_f32 v4, v20, v21\nv_cvt_pk_bf16_f32 v5, v22, v23\nMA 108,168,28\nds_write_b64 v207, v[4:5] offset:1024\nMU 176,8,8\n"
"s_waitcnt lgkmcnt(5)\nMA 112,172,16\nMU 177,9,9\nMU 178,10,10\nMA 116,172,20\nMU 179,11,11\nv_fmaak_f32 v176, v252, v176, 0xc0135761\nMA 120,172,24\n"
"v_fmaak_f32 v177, v252, v177, 0xc0135761\nv_fmaak_f32 v178, v252, v178, 0xc0135761\nMA 124,172,28\nv_fmaak_f32 v179, v252, v179, 0xc0135761\n"
"MU 24,8,24\ns_waitcnt vmcnt(15) lgkmcnt(0)\ns_barrier\nMA 96,160,32\nTR 168,200,36864\nTR 170,200,38912\nMA 100,160,36\ns_add_u32 m0, s5, 0x6000\n"
"s_add_u32 s86, s3, 0\nbuffer_load_dwordx4 v214, s[44:47], s86 offen lds\nMA 104,160,40\nMU 25,9,25\nMU 26,10,26\nMA 108,160,44\nMU 27,11,27\n"
"MU 8,176,8\nMA 112,164,32\nTR 172,201,36864\nTR 174,201,38912\nMA 116,164,36\nbuffer_load_dwordx4 v215, s[44:47], s86 offen offset:1024 lds\n"
"MU 9,177,9\nMA 120,164,40\nMU 10,178,10\nMU 11,179,11\nMA 124,164,44\nEX 8\ns_waitcnt lgkmcnt(2)\nMA 96,168,48\nTR 160,200,40960\nTR 162,200,43008\n"
"MA 100,168,52\nbuffer_load_dwordx4 v216, s[44:47], s86 offen offset:2048 lds\nEX 9\nMA 104,168,56\nEX 10\nMA 108,168,60\nEX 11\ns_waitcnt lgkmcnt(2)\n"
"MA 112,172,48\nTR 164,201,40960\nTR 166,201,43008\nMA 116,172,52\nv_add_f32 v8, 1.0, v8\nv_add_f32 v9, 1.0, v9\nMA 120,172,56\n"
"v_add_f32 v10, 1.0, v10\nv_add_f32 v11, 1.0, v11\nMA 124,172,60\nv_rcp_f32 v8, v8\ns_waitcnt lgkmcnt(2)\nMA 96,160,64\nTR 168,200,45056\n"
"TR 170,200,47104\nTR 172,201,45056\nTR 174,201,47104\nMA 100,160,68\nv_rcp_f32 v9, v9\nMA 104,160,72\nv_rcp_f32 v10, v10\nMA 108,160,76\n"
"v_rcp_f32 v11, v11\ns_waitcnt lgkmcnt(4)\nMA 112,164,64\nMU 24,24,8\nMU 25,25,9\nMA 116,164,68\nMU 26,26,10\nMU 27,27,11\nMA 120,164,72\n"
"v_cvt_pk_bf16_f32 v8, v24, v25\nv_cvt_pk_bf16_f32 v9, v26, v27\nMA 124,164,76\nds_write_b64 v207, v[8:9] offset:2048\nMU 176,12,12\nMU 177,13,13\n"
"MU 178,14,14\nMU 179,15,15\nv_fmaak_f32 v176, v252, v176, 0xc0135761\nv_fmaak_f32 v177, v252, v177, 0xc0135761\n"
"v_fmaak_f32 v178, v252, v178, 0xc0135761\nv_fmaak_f32 v179, v252, v179, 0xc0135761\nMU 28,12,28\nMU 29,13,29\nMU 30,14,30\nMU 31,15,31\nMU 12,176,12\n"
"MU 13,177,13\nMU 14,178,14\nMU 15,179,15\nEX 12\nEX 13\nEX 14\nEX 15\nv_add_f32 v12, 1.0, v12\nv_add_f32 v13, 1.0, v13\nv_add_f32 v14, 1.0, v14\n"
"v_add_f32 v15, 1.0, v15\nv_rcp_f32 v12, v12\nv_rcp_f32 v13, v13\nv_rcp_f32 v14, v14\nv_rcp_f32 v15, v15\nMU 28,28,12\nMU 29,29,13\nMU 30,30,14\n"
"MU 31,31,15\nv_cvt_pk_bf16_f32 v12, v28, v29\nv_cvt_pk_bf16_f32 v13, v30, v31\nds_write_b64 v207, v[12:13] offset:3072\n"
"s_waitcnt vmcnt(15) lgkmcnt(0)\ns_barrier\nMA 96,168,80\nTR 160,200,49152\nTR 162,200,51200\nMA 100,168,84\ns_add_u32 m0, s5, 0x9000\n"
"s_add_u32 s86, s3, 0x1b000\nbuffer_load_dwordx4 v214, s[44:47], s86 offen lds\nMA 104,168,88\nds_read_b128 a[128:131], v208 offset:32768\n"
"ds_read_b128 a[132:135], v208 offset:33792\nMA 108,168,92\nds_read_b128 a[136:139], v208 offset:34816\nds_read_b128 a[140:143], v208 offset:35840\n"
"MA 112,172,80\nTR 164,201,49152\nTR 166,201,51200\nMA 116,172,84\nbuffer_load_dwordx4 v215, s[44:47], s86 offen offset:1024 lds\nMA 120,172,88\n"
"MA 124,172,92\ns_waitcnt lgkmcnt(6)\nMA 64,160,96\nTR 168,200,53248\nTR 170,200,55296\nMA 68,160,100\n"
"buffer_load_dwordx4 v216, s[44:47], s86 offen offset:2048 lds\nMA 72,160,104\nMA 76,160,108\ns_waitcnt lgkmcnt(2)\nMA 80,164,96\nTR 172,201,53248\n"
"TR 174,201,55296\nMA 84,164,100\nMA 88,164,104\nMA 92,164,108\ns_waitcnt lgkmcnt(2)\nMA 64,168,112\nTR 160,200,57344\nTR 162,200,59392\n"
"TR 164,201,57344\nTR 166,201,59392\nMA 68,168,116\nMA 72,168,120\nMA 76,168,124\ns_waitcnt lgkmcnt(4)\nMA 80,172,112\nMA 84,172,116\nMA 88,172,120\n"
"MA 92,172,124\ns_waitcnt vmcnt(15) lgkmcnt(0)\ns_barrier\nMA 64,160,128\nTR 168,200,61440\nTR 170,200,63488\nMA 68,160,132\ns_add_u32 m0, s5, 0xc000\n"
"s_add_u32 s86, s3, 0\nbuffer_load_dwordx4 v217, s[44:47], s86 offen lds\nMA 72,160,136\nMA 76,160,140\nMA 80,164,128\nTR 172,201,61440\n"
"TR 174,201,63488\nMA 84,164,132\nbuffer_load_dwordx4 v218, s[44:47], s86 offen offset:1024 lds\nMA 88,164,136\nMA 92,164,140\ns_waitcnt lgkmcnt(2)\n"
"MA 96,168,96\nds_read_b64_tr_b16 v[160:161], v202\nTR 162,202,2048\nMA 100,168,100\nbuffer_load_dwordx4 v219, s[44:47], s86 offen offset:2048 lds\n"
"v_cvt_pk_bf16_f32 v64, v64, v65\nMA 104,168,104\nv_cvt_pk_bf16_f32 v65, v66, v67\nv_cvt_pk_bf16_f32 v66, v80, v81\nMA 108,168,108\n"
"v_cvt_pk_bf16_f32 v67, v82, v83\nv_accvgpr_write_b32 a144, v64\ns_waitcnt lgkmcnt(2)\nMA 112,172,96\nds_read_b64_tr_b16 v[164:165], v203\n"
"TR 166,203,2048\nMA 116,172,100\nv_accvgpr_write_b32 a145, v65\nv_accvgpr_write_b32 a146, v66\nMA 120,172,104\nv_accvgpr_write_b32 a147, v67\n"
"v_cvt_pk_bf16_f32 v68, v68, v69\nMA 124,172,108\nv_cvt_pk_bf16_f32 v69, v70, v71\nv_cvt_pk_bf16_f32 v70, v84, v85\ns_waitcnt lgkmcnt(2)\n"
"MA 96,160,112\nTR 168,202,4096\nTR 170,202,6144\nTR 172,203,4096\nTR 174,203,6144\nMA 100,160,116\nv_cvt_pk_bf16_f32 v71, v86, v87\n"
"v_accvgpr_write_b32 a148, v68\nMA 104,160,120\nv_accvgpr_write_b32 a149, v69\nv_accvgpr_write_b32 a150, v70\nMA 108,160,124\n"
"v_accvgpr_write_b32 a151, v71\nv_cvt_pk_bf16_f32 v72, v72, v73\ns_waitcnt lgkmcnt(4)\nMA 112,164,112\ns_lshl4_add_u32 s21, s0, 0\n"
"buffer_store_dwordx4 a[144:147], v209, s[48:51], s21 offen sc1\nMA 116,164,116\nv_cvt_pk_bf16_f32 v73, v74, v75\nv_cvt_pk_bf16_f32 v74, v88, v89\n"
"MA 120,164,120\nv_cvt_pk_bf16_f32 v75, v90, v91\nv_accvgpr_write_b32 a152, v72\nMA 124,164,124\nv_accvgpr_write_b32 a153, v73\n"
"v_accvgpr_write_b32 a154, v74\ns_waitcnt vmcnt(16) lgkmcnt(0)\ns_barrier\nMA 96,168,128\nTR 160,202,8192\nTR 162,202,10240\nMA 100,168,132\n"
"s_add_u32 m0, s5, 0xf000\ns_add_u32 s86, s78, 0\nbuffer_load_dwordx4 v214, s[44:47], s86 offen lds\nMA 104,168,136\nv_accvgpr_write_b32 a155, v75\n"
"v_cvt_pk_bf16_f32 v76, v76, v77\nMA 108,168,140\nv_cvt_pk_bf16_f32 v77, v78, v79\nv_cvt_pk_bf16_f32 v78, v92, v93\nMA 112,172,128\nTR 164,203,8192\n"
"TR 166,203,10240\nMA 116,172,132\nbuffer_load_dwordx4 v215, s[44:47], s86 offen offset:1024 lds\nv_cvt_pk_bf16_f32 v79, v94, v95\nMA 120,172,136\n"
"v_accvgpr_write_b32 a156, v76\nv_accvgpr_write_b32 a157, v77\nMA 124,172,140\nv_accvgpr_write_b32 a158, v78\nv_accvgpr_write_b32 a159, v79\n"
"s_waitcnt lgkmcnt(2)\nv_mfma_f32_16x16x32_bf16 v[64:67], v[160:163], a[0:3], 0\nTR 168,202,12288\nTR 170,202,14336\n"
"v_mfma_f32_16x16x32_bf16 v[68:71], v[160:163], a[4:7], 0\nbuffer_load_dwordx4 v216, s[44:47], s86 offen offset:2048 lds\ns_cmp_gt_u32 s7, 5\n"
"s_cbranch_scc1 L_kv_done\ns_cmp_gt_u32 s7, 2\ns_cbranch_scc1 L_kv_v\ns_cmp_eq_u32 s7, 2\ns_cbranch_scc1 L_kv_k2\nv_cvt_pk_bf16_f32 v0, v48, v49\n"
"v_cvt_pk_bf16_f32 v1, v50, v51\nMU 8,48,48\nv_fmac_f32 v8, v49, v49\nv_fmac_f32 v8, v50, v50\nv_fmac_f32 v8, v51, v51\n"
"v_cvt_pk_bf16_f32 v2, v52, v53\nv_cvt_pk_bf16_f32 v3, v54, v55\nMU 9,52,52\nv_fmac_f32 v9, v53, v53\nv_fmac_f32 v9, v54, v54\n"
"v_fmac_f32 v9, v55, v55\nv_cvt_pk_bf16_f32 v4, v56, v57\nv_cvt_pk_bf16_f32 v5, v58, v59\nMU 10,56,56\nv_fmac_f32 v10, v57, v57\n"
"v_fmac_f32 v10, v58, v58\nv_fmac_f32 v10, v59, v59\nv_cvt_pk_bf16_f32 v6, v60, v61\nv_cvt_pk_bf16_f32 v7, v62, v63\nMU 11,60,60\n"
"v_fmac_f32 v11, v61, v61\nv_fmac_f32 v11, v62, v62\nv_fmac_f32 v11, v63, v63\nbuffer_store_dwordx2 v[0:1], v180, s[48:51], 0 offen sc1\n"
"buffer_store_dwordx2 v[2:3], v180, s[48:51], 0 offen offset:256 sc1\nbuffer_store_dwordx2 v[4:5], v180, s[48:51], 0 offen offset:2560 sc1\n"
"buffer_store_dwordx2 v[6:7], v180, s[48:51], 0 offen offset:2816 sc1\nv_permlane16_swap_b32 v8, v9\nv_permlane16_swap_b32 v10, v11\n"
"v_add_f32 v8, v8, v9\nv_add_f32 v10, v10, v11\nv_max_f32 v8, v8, v10\ns_branch L_kv_norm\nL_kv_k2:\nv_cvt_pk_bf16_f32 v0, v50, v51\n"
"v_mov_b32 v1, 0x3f803f80\nMU 8,50,50\nv_fmac_f32 v8, v51, v51\nv_cvt_pk_bf16_f32 v2, v54, v55\nv_mov_b32 v3, 0x3f803f80\nMU 9,54,54\n"
"v_fmac_f32 v9, v55, v55\nv_cvt_pk_bf16_f32 v4, v58, v59\nv_mov_b32 v5, 0x3f803f80\nMU 10,58,58\nv_fmac_f32 v10, v59, v59\n"
"v_cvt_pk_bf16_f32 v6, v62, v63\nv_mov_b32 v7, 0x3f803f80\nMU 11,62,62\nv_fmac_f32 v11, v63, v63\n"
"buffer_store_dwordx2 v[0:1], v180, s[48:51], 0 offen sc1\nbuffer_store_dwordx2 v[2:3], v180, s[48:51], 0 offen offset:128 sc1\n"
"buffer_store_dwordx2 v[4:5], v180, s[48:51], 0 offen offset:2560 sc1\nbuffer_store_dwordx2 v[6:7], v180, s[48:51], 0 offen offset:2688 sc1\n"
"v_max_f32 v8, v8, v9\nv_max3_f32 v8, v8, v10, v11\nv_mov_b32 v9, v8\ns_nop 1\nv_permlane16_swap_b32 v9, v8\nL_kv_norm:\ns_nop 1\n"
"v_max_f32_dpp v8, v8, v8 row_ror:8 row_mask:0xf bank_mask:0xf\ns_nop 1\nv_max_f32_dpp v8, v8, v8 row_ror:4 row_mask:0xf bank_mask:0xf\ns_nop 1\n"
"v_max_f32_dpp v8, v8, v8 row_ror:2 row_mask:0xf bank_mask:0xf\ns_nop 1\nv_max_f32_dpp v8, v8, v8 row_ror:1 row_mask:0xf bank_mask:0xf\ns_nop 0\n"
"v_readlane_b32 s22, v8, 32\nv_readlane_b32 s23, v8, 48\ns_max_u32 s22, s22, s23\nv_mov_b32 v9, s22\n"
"buffer_store_dword v9, v248, s[48:51], s11 offen sc1\ns_branch L_kv_done\nL_kv_v:\nv_cvt_pk_bf16_f32 v0, v48, v49\nv_cvt_pk_bf16_f32 v1, v50, v51\n"
"v_cvt_pk_bf16_f32 v2, v52, v53\nv_cvt_pk_bf16_f32 v3, v54, v55\nv_cvt_pk_bf16_f32 v4, v56, v57\nv_cvt_pk_bf16_f32 v5, v58, v59\n"
"v_cvt_pk_bf16_f32 v6, v60, v61\nv_cvt_pk_bf16_f32 v7, v62, v63\nds_write_b16 v181, v0\nds_write_b16_d16_hi v181, v0 offset:128\n"
"ds_write_b16 v181, v1 offset:256\nds_write_b16_d16_hi v181, v1 offset:384\nds_write_b16 v181, v2 offset:32\nds_write_b16_d16_hi v181, v2 offset:160\n"
"ds_write_b16 v181, v3 offset:288\nds_write_b16_d16_hi v181, v3 offset:416\nds_write_b16 v181, v4 offset:64\nds_write_b16_d16_hi v181, v4 offset:192\n"
"ds_write_b16 v181, v5 offset:320\nds_write_b16_d16_hi v181, v5 offset:448\nds_write_b16 v181, v6 offset:96\nds_write_b16_d16_hi v181, v6 offset:224\n"
"ds_write_b16 v181, v7 offset:352\nds_write_b16_d16_hi v181, v7 offset:480\ns_waitcnt lgkmcnt(0)\nds_read_b128 v[8:11], v182\ns_waitcnt lgkmcnt(0)\n"
"v_or_b32 v8, v183, v8\nv_or_b32 v9, v183, v9\nv_or_b32 v10, v183, v10\nv_or_b32 v11, v183, v11\n"
"buffer_store_dwordx4 v[8:11], v180, s[48:51], 0 offen sc1\ns_nop 1\nL_kv_done:\nv_mfma_f32_16x16x32_bf16 v[72:75], v[160:163], a[8:11], 0\n"
"ds_write_b32 v239, v48\nds_write_b32 v239, v52 offset:64\nv_mfma_f32_16x16x32_bf16 v[76:79], v[160:163], a[12:15], 0\n"
"ds_write_b32 v239, v56 offset:128\nds_write_b32 v239, v60 offset:192\ns_waitcnt lgkmcnt(6)\nv_mfma_f32_16x16x32_bf16 v[80:83], v[164:167], a[0:3], 0\n"
"TR 172,203,12288\nTR 174,203,14336\nv_mfma_f32_16x16x32_bf16 v[84:87], v[164:167], a[4:7], 0\nv_cmp_ne_u32 vcc, 0, v249\n"
"v_mul_f32 v32, 0x3eae1a93, v32\nv_mfma_f32_16x16x32_bf16 v[88:91], v[164:167], a[8:11], 0\nv_mul_f32 v33, 0x3eae1a93, v33\n"
"v_mul_f32 v34, 0x3eae1a93, v34\nv_mfma_f32_16x16x32_bf16 v[92:95], v[164:167], a[12:15], 0\nv_mul_f32 v35, 0x3eae1a93, v35\n"
"v_mul_f32 v36, 0x3eae1a93, v36\ns_waitcnt lgkmcnt(6)\nMA 64,168,16\nTR 160,202,16384\nTR 162,202,18432\nTR 164,203,16384\nTR 166,203,18432\n"
"MA 68,168,20\nv_mul_f32 v37, 0x3eae1a93, v37\nv_mul_f32 v38, 0x3eae1a93, v38\nMA 72,168,24\nv_mul_f32 v39, 0x3eae1a93, v39\n"
"v_mul_f32 v40, 0x3eae1a93, v40\nMA 76,168,28\nv_mul_f32 v41, 0x3eae1a93, v41\nv_mul_f32 v42, 0x3eae1a93, v42\ns_waitcnt lgkmcnt(4)\nMA 80,172,16\n"
"s_lshl4_add_u32 s21, s0, 0x4800\nbuffer_store_dwordx4 a[148:151], v209, s[48:51], s21 offen sc1\nMA 84,172,20\nv_mul_f32 v43, 0x3eae1a93, v43\n"
"v_mul_f32 v44, 0x3eae1a93, v44\nMA 88,172,24\nv_mul_f32 v45, 0x3eae1a93, v45\nv_mul_f32 v46, 0x3eae1a93, v46\nMA 92,172,28\n"
"v_mul_f32 v47, 0x3eae1a93, v47\nv_cndmask_b32 v50, 0, v50, vcc\ns_waitcnt vmcnt(17) lgkmcnt(0)\ns_barrier\nMA 64,160,32\n"
"ds_read_b64_tr_b16 v[168:169], v200\nTR 170,200,2048\nMA 68,160,36\ns_add_u32 m0, s5, 0x12000\ns_add_u32 s86, s78, 0x1b000\n"
"buffer_load_dwordx4 v214, s[44:47], s86 offen lds\nMA 72,160,40\nv_cndmask_b32 v51, 0, v51, vcc\nv_cndmask_b32 v54, 0, v54, vcc\nMA 76,160,44\n"
"v_cndmask_b32 v55, 0, v55, vcc\nv_cndmask_b32 v58, 0, v58, vcc\nMA 80,164,32\nds_read_b64_tr_b16 v[172:173], v201\nTR 174,201,2048\nMA 84,164,36\n"
"buffer_load_dwordx4 v215, s[44:47], s86 offen offset:1024 lds\nv_cndmask_b32 v59, 0, v59, vcc\nMA 88,164,40\nv_cndmask_b32 v62, 0, v62, vcc\n"
"v_cndmask_b32 v63, 0, v63, vcc\nMA 92,164,44\nv_mul_f32 v50, 0x3eae1a93, v50\nv_mul_f32 v51, 0x3eae1a93, v51\ns_waitcnt lgkmcnt(2)\nMA 64,168,48\n"
"TR 160,200,4096\nTR 162,200,6144\nMA 68,168,52\nbuffer_load_dwordx4 v216, s[44:47], s86 offen offset:2048 lds\nv_mul_f32 v54, 0x3eae1a93, v54\n"
"MA 72,168,56\nv_mul_f32 v55, 0x3eae1a93, v55\nv_mul_f32 v58, 0x3eae1a93, v58\nMA 76,168,60\nv_mul_f32 v59, 0x3eae1a93, v59\n"
"v_mul_f32 v62, 0x3eae1a93, v62\ns_waitcnt lgkmcnt(2)\nMA 80,172,48\nTR 164,201,4096\nTR 166,201,6144\nMA 84,172,52\nv_mul_f32 v63, 0x3eae1a93, v63\n"
"MU 0,32,32\nMA 88,172,56\nMU 1,36,36\nMU 2,40,40\nMA 92,172,60\nMU 3,44,44\nv_fmac_f32 v0, v33, v33\ns_waitcnt lgkmcnt(2)\nMA 64,160,64\n"
"TR 168,200,8192\nTR 170,200,10240\nTR 172,201,8192\nTR 174,201,10240\nMA 68,160,68\ns_lshl4_add_u32 s21, s0, 0x9000\n"
"buffer_store_dwordx4 a[152:155], v209, s[48:51], s21 offen sc1\nMA 72,160,72\nv_fmac_f32 v1, v37, v37\nv_fmac_f32 v2, v41, v41\nMA 76,160,76\n"
"v_fmac_f32 v3, v45, v45\nv_fmac_f32 v0, v34, v34\ns_waitcnt lgkmcnt(4)\nMA 80,164,64\nv_fmac_f32 v1, v38, v38\nv_fmac_f32 v2, v42, v42\nMA 84,164,68\n"
"v_fmac_f32 v3, v46, v46\nv_fmac_f32 v0, v35, v35\nMA 88,164,72\nv_fmac_f32 v1, v39, v39\nv_fmac_f32 v2, v43, v43\nMA 92,164,76\n"
"v_fmac_f32 v3, v47, v47\nv_fmac_f32 v0, v50, v50\ns_waitcnt vmcnt(18) lgkmcnt(0)\ns_barrier\nMA 64,168,80\nTR 160,200,12288\nTR 162,200,14336\n"
"MA 68,168,84\ns_add_u32 m0, s5, 0\ns_add_u32 s86, s78, 0\nbuffer_load_dwordx4 v217, s[44:47], s86 offen lds\nMA 72,168,88\nv_fmac_f32 v1, v54, v54\n"
"v_fmac_f32 v2, v58, v58\nMA 76,168,92\nv_fmac_f32 v3, v62, v62\nv_fmac_f32 v0, v51, v51\nMA 80,172,80\nTR 164,201,12288\nTR 166,201,14336\n"
"MA 84,172,84\nbuffer_load_dwordx4 v218, s[44:47], s86 offen offset:1024 lds\nv_fmac_f32 v1, v55, v55\nMA 88,172,88\nv_fmac_f32 v2, v59, v59\n"
"v_fmac_f32 v3, v63, v63\nMA 92,172,92\nv_cvt_pk_bf16_f32 v128, v32, v33\nv_cvt_pk_bf16_f32 v129, v34, v35\ns_waitcnt lgkmcnt(2)\nMA 64,160,96\n"
"TR 168,200,16384\nTR 170,200,18432\nMA 68,160,100\nbuffer_load_dwordx4 v219, s[44:47], s86 offen offset:2048 lds\nv_cvt_pk_bf16_f32 v130, v36, v37\n"
"MA 72,160,104\nv_cvt_pk_bf16_f32 v131, v38, v39\nv_cvt_pk_bf16_f32 v132, v40, v41\nMA 76,160,108\nv_cvt_pk_bf16_f32 v133, v42, v43\n"
"v_cvt_pk_bf16_f32 v134, v44, v45\ns_waitcnt lgkmcnt(2)\nMA 80,164,96\nTR 172,201,16384\nTR 174,201,18432\nMA 84,164,100\n"
"v_cvt_pk_bf16_f32 v135, v46, v47\nv_cvt_pk_bf16_f32 v4, v50, v51\nMA 88,164,104\nv_cvt_pk_bf16_f32 v136, v54, v55\nv_cvt_pk_bf16_f32 v5, v58, v59\n"
"MA 92,164,108\nv_cvt_pk_bf16_f32 v138, v62, v63\nv_permlane16_swap_b32 v0, v1\ns_waitcnt lgkmcnt(2)\nMA 64,168,112\nTR 160,200,20480\n"
"TR 162,200,22528\nTR 164,201,20480\nTR 166,201,22528\nMA 68,168,116\ns_lshl4_add_u32 s21, s0, 0xd800\n"
"buffer_store_dwordx4 a[156:159], v209, s[48:51], s21 offen sc1\nMA 72,168,120\nv_permlane16_swap_b32 v128, v130\nv_permlane16_swap_b32 v129, v131\n"
"MA 76,168,124\nv_permlane16_swap_b32 v4, v136\nv_permlane16_swap_b32 v2, v3\ns_waitcnt lgkmcnt(4)\nMA 80,172,112\nv_permlane16_swap_b32 v132, v134\n"
"v_permlane16_swap_b32 v133, v135\nMA 84,172,116\nv_permlane16_swap_b32 v5, v138\nv_add_f32 v0, v0, v1\nMA 88,172,120\nv_add_f32 v2, v2, v3\n"
"v_mov_b32 v1, v0\nMA 92,172,124\nv_mov_b32 v3, v2\ns_nop 0\ns_waitcnt vmcnt(19) lgkmcnt(0)\ns_barrier\nMA 64,160,128\nTR 168,200,24576\n"
"TR 170,200,26624\nMA 68,160,132\ns_add_u32 m0, s5, 0x3000\ns_add_u32 s86, s79, 0\nbuffer_load_dwordx4 v214, s[44:47], s86 offen lds\nMA 72,160,136\n"
"v_permlane32_swap_b32 v0, v1\nv_permlane32_swap_b32 v2, v3\nMA 76,160,140\nv_add_f32 v140, v0, v1\nv_add_f32 v141, v2, v3\nMA 80,164,128\n"
"TR 172,201,24576\nTR 174,201,26624\nMA 84,164,132\nbuffer_load_dwordx4 v215, s[44:47], s86 offen offset:1024 lds\nv_cvt_pk_bf16_f32 v96, v96, v97\n"
"MA 88,164,136\nv_cvt_pk_bf16_f32 v97, v98, v99\nv_cvt_pk_bf16_f32 v98, v112, v113\nMA 92,164,140\nv_cvt_pk_bf16_f32 v99, v114, v115\n"
"v_accvgpr_write_b32 a160, v96\nv_accvgpr_write_b32 a161, v97\nv_accvgpr_write_b32 a162, v98\nv_accvgpr_write_b32 a163, v99\n"
"v_cvt_pk_bf16_f32 v100, v100, v101\nv_cvt_pk_bf16_f32 v101, v102, v103\nv_cvt_pk_bf16_f32 v102, v116, v117\nv_cvt_pk_bf16_f32 v103, v118, v119\n"
"v_accvgpr_write_b32 a164, v100\nv_accvgpr_write_b32 a165, v101\nv_accvgpr_write_b32 a166, v102\nv_accvgpr_write_b32 a167, v103\n"
"v_cvt_pk_bf16_f32 v104, v104, v105\nv_cvt_pk_bf16_f32 v105, v106, v107\nv_cvt_pk_bf16_f32 v106, v120, v121\nv_cvt_pk_bf16_f32 v107, v122, v123\n"
"v_accvgpr_write_b32 a168, v104\nv_accvgpr_write_b32 a169, v105\nv_accvgpr_write_b32 a170, v106\nv_accvgpr_write_b32 a171, v107\n"
"v_cvt_pk_bf16_f32 v108, v108, v109\nv_cvt_pk_bf16_f32 v109, v110, v111\nv_cvt_pk_bf16_f32 v110, v124, v125\nv_cvt_pk_bf16_f32 v111, v126, v127\n"
"v_accvgpr_write_b32 a172, v108\nv_accvgpr_write_b32 a173, v109\nv_accvgpr_write_b32 a174, v110\nv_accvgpr_write_b32 a175, v111\ns_waitcnt lgkmcnt(2)\n"
"v_mfma_f32_16x16x32_bf16 v[96:99], v[168:171], a[0:3], 0\nTR 160,200,28672\nTR 162,200,30720\n"
"v_mfma_f32_16x16x32_bf16 v[100:103], v[168:171], a[4:7], 0\nbuffer_load_dwordx4 v216, s[44:47], s86 offen offset:2048 lds\n"
"v_cvt_pk_bf16_f32 v64, v64, v65\nv_mfma_f32_16x16x32_bf16 v[104:107], v[168:171], a[8:11], 0\nv_cvt_pk_bf16_f32 v65, v66, v67\n"
"v_cvt_pk_bf16_f32 v66, v80, v81\nv_mfma_f32_16x16x32_bf16 v[108:111], v[168:171], a[12:15], 0\nv_cvt_pk_bf16_f32 v67, v82, v83\n"
"v_accvgpr_write_b32 a176, v64\ns_waitcnt lgkmcnt(2)\nv_mfma_f32_16x16x32_bf16 v[112:115], v[172:175], a[0:3], 0\nTR 164,201,28672\nTR 166,201,30720\n"
"v_mfma_f32_16x16x32_bf16 v[116:119], v[172:175], a[4:7], 0\ns_lshl4_add_u32 s21, s1, 0\n"
"buffer_store_dwordx4 a[160:163], v209, s[48:51], s21 offen sc1\nv_mfma_f32_16x16x32_bf16 v[120:123], v[172:175], a[8:11], 0\n"
"v_accvgpr_write_b32 a177, v65\nv_accvgpr_write_b32 a178, v66\nv_mfma_f32_16x16x32_bf16 v[124:127], v[172:175], a[12:15], 0\n"
"v_accvgpr_write_b32 a179, v67\nv_cvt_pk_bf16_f32 v68, v68, v69\ns_waitcnt lgkmcnt(2)\nMA 96,160,16\nTR 168,200,32768\nTR 170,200,34816\n"
"TR 172,201,32768\nTR 174,201,34816\nMA 100,160,20\nv_cvt_pk_bf16_f32 v69, v70, v71\nv_cvt_pk_bf16_f32 v70, v84, v85\nMA 104,160,24\n"
"v_cvt_pk_bf16_f32 v71, v86, v87\nv_accvgpr_write_b32 a180, v68\nMA 108,160,28\nv_accvgpr_write_b32 a181, v69\nv_accvgpr_write_b32 a182, v70\n"
"s_waitcnt lgkmcnt(4)\nMA 112,164,16\nv_accvgpr_write_b32 a183, v71\nv_cvt_pk_bf16_f32 v72, v72, v73\nMA 116,164,20\nv_cvt_pk_bf16_f32 v73, v74, v75\n"
"v_cvt_pk_bf16_f32 v74, v88, v89\nMA 120,164,24\nv_cvt_pk_bf16_f32 v75, v90, v91\nv_accvgpr_write_b32 a184, v72\nMA 124,164,28\n"
"v_accvgpr_write_b32 a185, v73\nv_accvgpr_write_b32 a186, v74\ns_waitcnt vmcnt(20) lgkmcnt(0)\ns_barrier\nMA 96,168,32\nTR 160,200,36864\n"
"TR 162,200,38912\nMA 100,168,36\ns_add_u32 m0, s5, 0x6000\ns_add_u32 s86, s79, 0x1b000\nbuffer_load_dwordx4 v214, s[44:47], s86 offen lds\n"
"MA 104,168,40\nv_accvgpr_write_b32 a187, v75\nv_cvt_pk_bf16_f32 v76, v76, v77\nMA 108,168,44\nv_cvt_pk_bf16_f32 v77, v78, v79\n"
"v_cvt_pk_bf16_f32 v78, v92, v93\nMA 112,172,32\nTR 164,201,36864\nTR 166,201,38912\nMA 116,172,36\n"
"buffer_load_dwordx4 v215, s[44:47], s86 offen offset:1024 lds\nv_cvt_pk_bf16_f32 v79, v94, v95\nMA 120,172,40\nv_accvgpr_write_b32 a188, v76\n"
"v_accvgpr_write_b32 a189, v77\nMA 124,172,44\nv_accvgpr_write_b32 a190, v78\nv_accvgpr_write_b32 a191, v79\ns_waitcnt lgkmcnt(2)\nMA 96,160,48\n"
"TR 168,200,40960\nTR 170,200,43008\nMA 100,160,52\nbuffer_load_dwordx4 v216, s[44:47], s86 offen offset:2048 lds\ns_lshl4_add_u32 s21, s1, 0x4800\n"
"buffer_store_dwordx4 a[164:167], v209, s[48:51], s21 offen sc1\nMA 104,160,56\nMA 108,160,60\ns_waitcnt lgkmcnt(2)\nMA 112,164,48\nTR 172,201,40960\n"
"TR 174,201,43008\nMA 116,164,52\nMA 120,164,56\nMA 124,164,60\ns_waitcnt lgkmcnt(2)\nMA 96,168,64\nTR 160,200,45056\nTR 162,200,47104\n"
"TR 164,201,45056\nTR 166,201,47104\nMA 100,168,68\nMA 104,168,72\nMA 108,168,76\ns_waitcnt lgkmcnt(4)\nMA 112,172,64\nMA 116,172,68\nMA 120,172,72\n"
"MA 124,172,76\ns_waitcnt vmcnt(17) lgkmcnt(0)\ns_barrier\ns_cmp_eq_u32 s16, 0\ns_cbranch_scc1 L_kvf_skip\nv_mov_b32 v0, s84\nv_mov_b32 v1, s85\n"
"buffer_store_dwordx2 v[0:1], v248, s[48:51], s9 offen sc1\nL_kvf_skip:\nMA 96,160,80\nTR 168,200,49152\nTR 170,200,51200\nMA 100,160,84\n"
"s_add_u32 m0, s5, 0x9000\ns_add_u32 s86, s79, 0\nbuffer_load_dwordx4 v217, s[44:47], s86 offen lds\nMA 104,160,88\nMA 108,160,92\nMA 112,164,80\n"
"TR 172,201,49152\nTR 174,201,51200\nMA 116,164,84\nbuffer_load_dwordx4 v218, s[44:47], s86 offen offset:1024 lds\nMA 120,164,88\nMA 124,164,92\n"
"s_waitcnt lgkmcnt(2)\nMA 96,168,96\nTR 160,200,53248\nTR 162,200,55296\nMA 100,168,100\n"
"buffer_load_dwordx4 v219, s[44:47], s86 offen offset:2048 lds\ns_lshl4_add_u32 s21, s1, 0x9000\n"
"buffer_store_dwordx4 a[168:171], v209, s[48:51], s21 offen sc1\nMA 104,168,104\nds_read_b32 v142, v240\nMA 108,168,108\ns_waitcnt lgkmcnt(3)\n"
"MA 112,172,96\nTR 164,201,53248\nTR 166,201,55296\nMA 116,172,100\nMA 120,172,104\nMA 124,172,108\ns_getpc_b64 s[22:23]\nL_pc3:\n"
"v_add_u32 v143, 0x2000, v184\nv_min_u32 v143, L_end-L_pc3-4, v143\nglobal_load_dword v253, v143, s[22:23]\ns_waitcnt lgkmcnt(3)\nMA 96,160,112\n"
"TR 168,200,57344\nTR 170,200,59392\nTR 172,201,57344\nTR 174,201,59392\nMA 100,160,116\nMA 104,160,120\nMA 108,160,124\ns_waitcnt lgkmcnt(4)\n"
"MA 112,164,112\nMA 116,164,116\nMA 120,164,120\nMA 124,164,124\ns_waitcnt lgkmcnt(0)\ns_barrier\nMA 96,168,128\nTR 160,200,61440\nTR 162,200,63488\n"
"MA 100,168,132\ns_add_u32 m0, s5, 0xc000\ns_add_u32 s86, s80, 0\nbuffer_load_dwordx4 v214, s[44:47], s86 offen lds\nMA 104,168,136\nMA 108,168,140\n"
"MA 112,172,128\nTR 164,201,61440\nTR 166,201,63488\nMA 116,172,132\nbuffer_load_dwordx4 v215, s[44:47], s86 offen offset:1024 lds\n"
"s_lshl4_add_u32 s21, s1, 0xd800\nbuffer_store_dwordx4 a[172:175], v209, s[48:51], s21 offen sc1\nMA 120,172,136\nMA 124,172,140\n"
"s_waitcnt lgkmcnt(2)\nv_mfma_f32_16x16x32_bf16 v[64:67], v[160:163], a[0:3], 0\nds_read_b64_tr_b16 v[168:169], v202\nTR 170,202,2048\n"
"v_mfma_f32_16x16x32_bf16 v[68:71], v[160:163], a[4:7], 0\nbuffer_load_dwordx4 v216, s[44:47], s86 offen offset:2048 lds\n"
"v_cvt_pk_bf16_f32 v96, v96, v97\nv_mfma_f32_16x16x32_bf16 v[72:75], v[160:163], a[8:11], 0\nv_cvt_pk_bf16_f32 v97, v98, v99\n"
"v_cvt_pk_bf16_f32 v98, v112, v113\nv_mfma_f32_16x16x32_bf16 v[76:79], v[160:163], a[12:15], 0\nv_cvt_pk_bf16_f32 v99, v114, v115\n"
"v_accvgpr_write_b32 a192, v96\ns_waitcnt lgkmcnt(2)\nv_mfma_f32_16x16x32_bf16 v[80:83], v[164:167], a[0:3], 0\nds_read_b64_tr_b16 v[172:173], v203\n"
"TR 174,203,2048\nv_mfma_f32_16x16x32_bf16 v[84:87], v[164:167], a[4:7], 0\nv_accvgpr_write_b32 a193, v97\nv_accvgpr_write_b32 a194, v98\n"
"v_mfma_f32_16x16x32_bf16 v[88:91], v[164:167], a[8:11], 0\nv_accvgpr_write_b32 a195, v99\nv_cvt_pk_bf16_f32 v100, v100, v101\n"
"v_mfma_f32_16x16x32_bf16 v[92:95], v[164:167], a[12:15], 0\nv_cvt_pk_bf16_f32 v101, v102, v103\nv_cvt_pk_bf16_f32 v102, v116, v117\n"
"s_waitcnt lgkmcnt(2)\nMA 64,168,16\nTR 160,202,4096\nTR 162,202,6144\nTR 164,203,4096\nTR 166,203,6144\nMA 68,168,20\n"
"v_cvt_pk_bf16_f32 v103, v118, v119\nv_accvgpr_write_b32 a196, v100\nMA 72,168,24\nv_accvgpr_write_b32 a197, v101\nv_accvgpr_write_b32 a198, v102\n"
"MA 76,168,28\nv_accvgpr_write_b32 a199, v103\nv_cvt_pk_bf16_f32 v104, v104, v105\ns_waitcnt lgkmcnt(4)\nMA 80,172,16\n"
"v_cvt_pk_bf16_f32 v105, v106, v107\nv_cvt_pk_bf16_f32 v106, v120, v121\nMA 84,172,20\nv_cvt_pk_bf16_f32 v107, v122, v123\n"
"v_accvgpr_write_b32 a200, v104\nMA 88,172,24\nv_accvgpr_write_b32 a201, v105\nv_accvgpr_write_b32 a202, v106\nMA 92,172,28\n"
"v_accvgpr_write_b32 a203, v107\nv_cvt_pk_bf16_f32 v108, v108, v109\ns_waitcnt vmcnt(22) lgkmcnt(0)\ns_barrier\nMA 64,160,32\nTR 168,202,8192\n"
"TR 170,202,10240\nMA 68,160,36\ns_add_u32 m0, s5, 0xf000\ns_add_u32 s86, s80, 0x1b000\nbuffer_load_dwordx4 v214, s[44:47], s86 offen lds\n"
"MA 72,160,40\nv_cvt_pk_bf16_f32 v109, v110, v111\nv_cvt_pk_bf16_f32 v110, v124, v125\nMA 76,160,44\nv_cvt_pk_bf16_f32 v111, v126, v127\n"
"v_accvgpr_write_b32 a204, v108\nMA 80,164,32\nTR 172,203,8192\nTR 174,203,10240\nMA 84,164,36\n"
"buffer_load_dwordx4 v215, s[44:47], s86 offen offset:1024 lds\ns_lshl4_add_u32 s21, s2, 0\n"
"buffer_store_dwordx4 a[176:179], v209, s[48:51], s21 offen sc1\nMA 88,164,40\nv_accvgpr_write_b32 a205, v109\nv_accvgpr_write_b32 a206, v110\n"
"MA 92,164,44\nv_accvgpr_write_b32 a207, v111\ns_waitcnt lgkmcnt(2)\nMA 64,168,48\nTR 160,202,12288\nTR 162,202,14336\nMA 68,168,52\n"
"buffer_load_dwordx4 v216, s[44:47], s86 offen offset:2048 lds\nv_mul_f32 v142, 0xbfb8aa3b, v142\nMA 72,168,56\nEX 142\nMA 76,168,60\n"
"v_add_f32 v142, 1.0, v142\nv_rcp_f32 v142, v142\ns_waitcnt lgkmcnt(2)\nMA 80,172,48\nTR 164,203,12288\nTR 166,203,14336\nMA 84,172,52\nMA 88,172,56\n"
"MA 92,172,60\ns_waitcnt lgkmcnt(2)\nMA 64,160,64\nTR 168,202,16384\nTR 170,202,18432\nTR 172,203,16384\nTR 174,203,18432\nMA 68,160,68\nMA 72,160,72\n"
"MA 76,160,76\ns_waitcnt lgkmcnt(4)\nMA 80,164,64\nMA 84,164,68\nMA 88,164,72\nMA 92,164,76\ns_waitcnt vmcnt(22) lgkmcnt(0)\ns_barrier\nMA 64,168,80\n"
"ds_read_b64_tr_b16 v[160:161], v200\nTR 162,200,2048\nMA 68,168,84\ns_add_u32 m0, s5, 0x12000\ns_add_u32 s86, s80, 0\n"
"buffer_load_dwordx4 v217, s[44:47], s86 offen lds\nMA 72,168,88\ns_lshl4_add_u32 s21, s2, 0x4800\n"
"buffer_store_dwordx4 a[180:183], v209, s[48:51], s21 offen sc1\nMA 76,168,92\nMA 80,172,80\nds_read_b64_tr_b16 v[164:165], v201\nTR 166,201,2048\n"
"MA 84,172,84\nbuffer_load_dwordx4 v218, s[44:47], s86 offen offset:1024 lds\nMA 88,172,88\nMA 92,172,92\ns_waitcnt lgkmcnt(2)\nMA 64,160,96\n"
"TR 168,200,4096\nTR 170,200,6144\nMA 68,160,100\nbuffer_load_dwordx4 v219, s[44:47], s86 offen offset:2048 lds\nMA 72,160,104\nMA 76,160,108\n"
"s_waitcnt lgkmcnt(2)\nMA 80,164,96\nTR 172,201,4096\nTR 174,201,6144\nMA 84,164,100\nMA 88,164,104\nMA 92,164,108\ns_waitcnt lgkmcnt(2)\n"
"MA 64,168,112\nTR 160,200,8192\nTR 162,200,10240\nTR 164,201,8192\nTR 166,201,10240\nMA 68,168,116\nMA 72,168,120\nMA 76,168,124\n"
"s_waitcnt lgkmcnt(4)\nMA 80,172,112\ns_lshl4_add_u32 s21, s2, 0x9000\nbuffer_store_dwordx4 a[184:187], v209, s[48:51], s21 offen sc1\nMA 84,172,116\n"
"MA 88,172,120\nMA 92,172,124\ns_waitcnt vmcnt(23) lgkmcnt(0)\ns_barrier\nMA 64,160,128\nTR 168,200,12288\nTR 170,200,14336\nMA 68,160,132\n"
"s_add_u32 m0, s5, 0\ns_add_u32 s86, s81, 0\nbuffer_load_dwordx4 v214, s[44:47], s86 offen lds\nMA 72,160,136\nMA 76,160,140\nMA 80,164,128\n"
"TR 172,201,12288\nTR 174,201,14336\nMA 84,164,132\nbuffer_load_dwordx4 v215, s[44:47], s86 offen offset:1024 lds\nMA 88,164,136\nMA 92,164,140\n"
"s_waitcnt lgkmcnt(2)\nv_mfma_f32_16x16x32_bf16 v[96:99], v[168:171], a[0:3], 0\nTR 160,200,16384\nTR 162,200,18432\n"
"v_mfma_f32_16x16x32_bf16 v[100:103], v[168:171], a[4:7], 0\nbuffer_load_dwordx4 v216, s[44:47], s86 offen offset:2048 lds\n"
"buffer_load_dwordx4 v[0:3], v243, s[48:51], 0 offen sc1\nv_mfma_f32_16x16x32_bf16 v[104:107], v[168:171], a[8:11], 0\n"
"v_cvt_pk_bf16_f32 v64, v64, v65\nv_cvt_pk_bf16_f32 v65, v66, v67\nv_mfma_f32_16x16x32_bf16 v[108:111], v[168:171], a[12:15], 0\n"
"v_cvt_pk_bf16_f32 v66, v80, v81\nv_cvt_pk_bf16_f32 v67, v82, v83\ns_waitcnt lgkmcnt(2)\nv_mfma_f32_16x16x32_bf16 v[112:115], v[172:175], a[0:3], 0\n"
"TR 164,201,16384\nTR 166,201,18432\nv_mfma_f32_16x16x32_bf16 v[116:119], v[172:175], a[4:7], 0\nv_accvgpr_write_b32 a208, v64\n"
"v_accvgpr_write_b32 a209, v65\nv_mfma_f32_16x16x32_bf16 v[120:123], v[172:175], a[8:11], 0\nv_accvgpr_write_b32 a210, v66\n"
"v_accvgpr_write_b32 a211, v67\nv_mfma_f32_16x16x32_bf16 v[124:127], v[172:175], a[12:15], 0\nv_cvt_pk_bf16_f32 v68, v68, v69\n"
"v_cvt_pk_bf16_f32 v69, v70, v71\ns_waitcnt lgkmcnt(2)\nMA 96,160,16\nTR 168,200,20480\nTR 170,200,22528\nTR 172,201,20480\nTR 174,201,22528\n"
"MA 100,160,20\nv_cvt_pk_bf16_f32 v70, v84, v85\nv_cvt_pk_bf16_f32 v71, v86, v87\nMA 104,160,24\nv_accvgpr_write_b32 a212, v68\n"
"v_accvgpr_write_b32 a213, v69\nMA 108,160,28\nv_accvgpr_write_b32 a214, v70\nv_accvgpr_write_b32 a215, v71\ns_waitcnt lgkmcnt(4)\nMA 112,164,16\n"
"s_lshl4_add_u32 s21, s2, 0xd800\nbuffer_store_dwordx4 a[188:191], v209, s[48:51], s21 offen sc1\nMA 116,164,20\nv_cvt_pk_bf16_f32 v72, v72, v73\n"
"v_cvt_pk_bf16_f32 v73, v74, v75\nMA 120,164,24\nv_cvt_pk_bf16_f32 v74, v88, v89\nv_cvt_pk_bf16_f32 v75, v90, v91\nMA 124,164,28\n"
"v_accvgpr_write_b32 a216, v72\nv_accvgpr_write_b32 a217, v73\ns_waitcnt vmcnt(24) lgkmcnt(0)\ns_barrier\nMA 96,168,32\nTR 160,200,24576\n"
"TR 162,200,26624\nMA 100,168,36\ns_add_u32 m0, s5, 0x3000\ns_add_u32 s86, s81, 0x1b000\nbuffer_load_dwordx4 v214, s[44:47], s86 offen lds\n"
"MA 104,168,40\nv_accvgpr_write_b32 a218, v74\nv_accvgpr_write_b32 a219, v75\nMA 108,168,44\nv_cvt_pk_bf16_f32 v76, v76, v77\n"
"v_cvt_pk_bf16_f32 v77, v78, v79\nMA 112,172,32\nTR 164,201,24576\nTR 166,201,26624\nMA 116,172,36\n"
"buffer_load_dwordx4 v215, s[44:47], s86 offen offset:1024 lds\nv_cvt_pk_bf16_f32 v78, v92, v93\nMA 120,172,40\nv_cvt_pk_bf16_f32 v79, v94, v95\n"
"v_accvgpr_write_b32 a220, v76\nMA 124,172,44\nv_accvgpr_write_b32 a221, v77\nv_accvgpr_write_b32 a222, v78\ns_waitcnt lgkmcnt(2)\nMA 96,160,48\n"
"TR 168,200,28672\nTR 170,200,30720\nMA 100,160,52\nbuffer_load_dwordx4 v216, s[44:47], s86 offen offset:2048 lds\nv_accvgpr_write_b32 a223, v79\n"
"MA 104,160,56\nMA 108,160,60\ns_waitcnt lgkmcnt(2)\nMA 112,164,48\nTR 172,201,28672\nTR 174,201,30720\nMA 116,164,52\nMA 120,164,56\nMA 124,164,60\n"
"s_waitcnt lgkmcnt(2)\nMA 96,168,64\nTR 160,200,32768\nTR 162,200,34816\nTR 164,201,32768\nTR 166,201,34816\nMA 100,168,68\n"
"s_lshl4_add_u32 s21, s3, 0\nbuffer_store_dwordx4 a[192:195], v209, s[48:51], s21 offen sc1\nMA 104,168,72\nMA 108,168,76\ns_waitcnt lgkmcnt(4)\n"
"MA 112,172,64\nMA 116,172,68\nMA 120,172,72\nMA 124,172,76\ns_waitcnt vmcnt(24) lgkmcnt(0)\ns_barrier\nMA 96,160,80\nTR 168,200,36864\n"
"TR 170,200,38912\nMA 100,160,84\ns_add_u32 m0, s5, 0x6000\ns_add_u32 s86, s81, 0\nbuffer_load_dwordx4 v217, s[44:47], s86 offen lds\nMA 104,160,88\n"
"MA 108,160,92\nMA 112,164,80\nTR 172,201,36864\nTR 174,201,38912\nMA 116,164,84\nbuffer_load_dwordx4 v218, s[44:47], s86 offen offset:1024 lds\n"
"MA 120,164,88\nMA 124,164,92\ns_waitcnt lgkmcnt(2)\nMA 96,168,96\nTR 160,200,40960\nTR 162,200,43008\nMA 100,168,100\n"
"buffer_load_dwordx4 v219, s[44:47], s86 offen offset:2048 lds\nMA 104,168,104\nMA 108,168,108\ns_waitcnt lgkmcnt(2)\nMA 112,172,96\nTR 164,201,40960\n"
"TR 166,201,43008\nMA 116,172,100\ns_lshl4_add_u32 s21, s3, 0x4800\nbuffer_store_dwordx4 a[196:199], v209, s[48:51], s21 offen sc1\nMA 120,172,104\n"
"MA 124,172,108\ns_waitcnt lgkmcnt(2)\nMA 96,160,112\nTR 168,200,45056\nTR 170,200,47104\nTR 172,201,45056\nTR 174,201,47104\nMA 100,160,116\n"
"MA 104,160,120\nMA 108,160,124\ns_waitcnt lgkmcnt(4)\nMA 112,164,112\nMA 116,164,116\nMA 120,164,120\nMA 124,164,124\ns_waitcnt vmcnt(22) lgkmcnt(0)\n"
"s_barrier\nMA 96,168,128\nTR 160,200,49152\nTR 162,200,51200\nMA 100,168,132\ns_add_u32 m0, s5, 0x9000\ns_add_u32 s86, s19, 0\n"
"buffer_load_dwordx4 v214, s[44:47], s86 offen lds\nMA 104,168,136\nMA 108,168,140\nMA 112,172,128\nTR 164,201,49152\nTR 166,201,51200\n"
"MA 116,172,132\nbuffer_load_dwordx4 v215, s[44:47], s86 offen offset:1024 lds\nMA 120,172,136\nMA 124,172,140\ns_waitcnt vmcnt(11)\n"
"v_xor_b32 v0, s84, v0\nv_xor_b32 v1, s85, v1\nv_xor_b32 v2, s84, v2\nv_xor_b32 v3, s85, v3\nv_or_b32 v0, v0, v1\nv_or_b32 v0, v0, v2\n"
"v_or_b32 v0, v0, v3\nv_cmp_ne_u32 vcc, 0, v0\ns_cbranch_vccnz L_kv_spin\nL_kv_ok:\nbuffer_load_dwordx4 v[8:11], v245, s[48:51], 0 offen sc1\n"
"s_waitcnt lgkmcnt(2)\nv_mfma_f32_16x16x32_bf16 v[64:67], v[160:163], a[0:3], 0\nTR 168,200,53248\nTR 170,200,55296\n"
"v_mfma_f32_16x16x32_bf16 v[68:71], v[160:163], a[4:7], 0\nbuffer_load_dwordx4 v216, s[44:47], s86 offen offset:2048 lds\n"
"v_cvt_pk_bf16_f32 v96, v96, v97\nv_mfma_f32_16x16x32_bf16 v[72:75], v[160:163], a[8:11], 0\nv_cvt_pk_bf16_f32 v97, v98, v99\n"
"v_cvt_pk_bf16_f32 v98, v112, v113\nv_mfma_f32_16x16x32_bf16 v[76:79], v[160:163], a[12:15], 0\nv_cvt_pk_bf16_f32 v99, v114, v115\n"
"v_accvgpr_write_b32 a224, v96\ns_waitcnt lgkmcnt(2)\nv_mfma_f32_16x16x32_bf16 v[80:83], v[164:167], a[0:3], 0\nTR 172,201,53248\nTR 174,201,55296\n"
"v_mfma_f32_16x16x32_bf16 v[84:87], v[164:167], a[4:7], 0\ns_lshl4_add_u32 s21, s3, 0x9000\n"
"buffer_store_dwordx4 a[200:203], v209, s[48:51], s21 offen sc1\nv_mfma_f32_16x16x32_bf16 v[88:91], v[164:167], a[8:11], 0\n"
"v_accvgpr_write_b32 a225, v97\nv_accvgpr_write_b32 a226, v98\nv_mfma_f32_16x16x32_bf16 v[92:95], v[164:167], a[12:15], 0\n"
"v_accvgpr_write_b32 a227, v99\nv_cvt_pk_bf16_f32 v100, v100, v101\ns_waitcnt lgkmcnt(2)\nMA 64,168,16\nTR 160,200,57344\nTR 162,200,59392\n"
"TR 164,201,57344\nTR 166,201,59392\nMA 68,168,20\nv_cvt_pk_bf16_f32 v101, v102, v103\nv_cvt_pk_bf16_f32 v102, v116, v117\nMA 72,168,24\n"
"v_cvt_pk_bf16_f32 v103, v118, v119\nv_accvgpr_write_b32 a228, v100\nMA 76,168,28\nv_accvgpr_write_b32 a229, v101\nv_accvgpr_write_b32 a230, v102\n"
"s_waitcnt lgkmcnt(4)\nMA 80,172,16\nv_accvgpr_write_b32 a231, v103\nv_cvt_pk_bf16_f32 v104, v104, v105\nMA 84,172,20\n"
"v_cvt_pk_bf16_f32 v105, v106, v107\nv_cvt_pk_bf16_f32 v106, v120, v121\nMA 88,172,24\nv_cvt_pk_bf16_f32 v107, v122, v123\n"
"v_accvgpr_write_b32 a232, v104\nMA 92,172,28\nv_accvgpr_write_b32 a233, v105\nv_accvgpr_write_b32 a234, v106\ns_waitcnt lgkmcnt(0)\ns_barrier\n"
"MA 64,160,32\nTR 168,200,61440\nTR 170,200,63488\nMA 68,160,36\ns_add_u32 m0, s5, 0xc000\ns_add_u32 s86, s19, 0x1b000\n"
"buffer_load_dwordx4 v214, s[44:47], s86 offen lds\nMA 72,160,40\nv_accvgpr_write_b32 a235, v107\nv_cvt_pk_bf16_f32 v108, v108, v109\nMA 76,160,44\n"
"v_cvt_pk_bf16_f32 v109, v110, v111\nv_cvt_pk_bf16_f32 v110, v124, v125\nMA 80,164,32\nTR 172,201,61440\nTR 174,201,63488\nMA 84,164,36\n"
"buffer_load_dwordx4 v215, s[44:47], s86 offen offset:1024 lds\nv_cvt_pk_bf16_f32 v111, v126, v127\nMA 88,164,40\nv_accvgpr_write_b32 a236, v108\n"
"v_accvgpr_write_b32 a237, v109\nMA 92,164,44\nv_accvgpr_write_b32 a238, v110\nv_accvgpr_write_b32 a239, v111\ns_waitcnt lgkmcnt(2)\nMA 64,168,48\n"
"ds_read_b64_tr_b16 v[160:161], v202\nTR 162,202,2048\nMA 68,168,52\nbuffer_load_dwordx4 v216, s[44:47], s86 offen offset:2048 lds\n"
"s_lshl4_add_u32 s21, s3, 0xd800\nbuffer_store_dwordx4 a[204:207], v209, s[48:51], s21 offen sc1\nMA 72,168,56\nMA 76,168,60\ns_waitcnt lgkmcnt(2)\n"
"MA 80,172,48\nds_read_b64_tr_b16 v[164:165], v203\nTR 166,203,2048\nMA 84,172,52\nMA 88,172,56\nMA 92,172,60\ns_waitcnt lgkmcnt(2)\nMA 64,160,64\n"
"TR 168,202,4096\nTR 170,202,6144\nTR 172,203,4096\nTR 174,203,6144\nMA 68,160,68\nMA 72,160,72\nMA 76,160,76\ns_waitcnt lgkmcnt(4)\nMA 80,164,64\n"
"MA 84,164,68\nMA 88,164,72\nMA 92,164,76\ns_waitcnt lgkmcnt(0)\ns_barrier\nMA 64,168,80\nTR 160,202,8192\nTR 162,202,10240\nMA 68,168,84\n"
"s_add_u32 m0, s5, 0xf000\ns_add_u32 s86, s19, 0\nbuffer_load_dwordx4 v217, s[44:47], s86 offen lds\nMA 72,168,88\nMA 76,168,92\nMA 80,172,80\n"
"TR 164,203,8192\nTR 166,203,10240\nMA 84,172,84\nbuffer_load_dwordx4 v218, s[44:47], s86 offen offset:1024 lds\nMA 88,172,88\nMA 92,172,92\n"
"s_waitcnt lgkmcnt(2)\nMA 64,160,96\nTR 168,202,12288\nTR 170,202,14336\nMA 68,160,100\nbuffer_load_dwordx4 v219, s[44:47], s86 offen offset:2048 lds\n"
"s_lshl4_add_u32 s21, s78, 0\nbuffer_store_dwordx4 a[208:211], v209, s[48:51], s21 offen sc1\nMA 72,160,104\nMA 76,160,108\ns_waitcnt lgkmcnt(2)\n"
"MA 80,164,96\nTR 172,203,12288\nTR 174,203,14336\nMA 84,164,100\ns_add_u32 m0, s6, 0x15000\ns_add_u32 s86, s6, 0x19000\n"
"buffer_load_dwordx4 v220, s[48:51], s86 offen sc1 lds\nMA 88,164,104\nMA 92,164,108\ns_waitcnt lgkmcnt(2)\nMA 64,168,112\nTR 160,202,16384\n"
"TR 162,202,18432\nTR 164,203,16384\nTR 166,203,18432\nMA 68,168,116\ns_add_u32 m0, s6, 0x16000\ns_add_u32 s86, s6, 0x1a000\n"
"buffer_load_dwordx4 v220, s[48:51], s86 offen sc1 lds\nMA 72,168,120\nMA 76,168,124\ns_waitcnt lgkmcnt(4)\nMA 80,172,112\nMA 84,172,116\n"
"s_add_u32 m0, s6, 0x17000\ns_add_u32 s86, s6, 0x1b000\nbuffer_load_dwordx4 v220, s[48:51], s86 offen sc1 lds\nMA 88,172,120\nMA 92,172,124\n"
"s_waitcnt lgkmcnt(0)\ns_barrier\nMA 64,160,128\nds_read_b64_tr_b16 v[168:169], v200\nTR 170,200,2048\nMA 68,160,132\ns_add_u32 m0, s6, 0x18000\n"
"s_add_u32 s86, s6, 0x1c000\nbuffer_load_dwordx4 v220, s[48:51], s86 offen sc1 lds\nMA 72,160,136\nMA 76,160,140\nMA 80,164,128\n"
"ds_read_b64_tr_b16 v[172:173], v201\nTR 174,201,2048\nMA 84,164,132\ns_add_u32 m0, s6, 0x19000\ns_add_u32 s86, s6, 0x1d000\n"
"buffer_load_dwordx4 v220, s[48:51], s86 offen sc1 lds\nMA 88,164,136\ns_lshl4_add_u32 s21, s78, 0x4800\n"
"buffer_store_dwordx4 a[212:215], v209, s[48:51], s21 offen sc1\nMA 92,164,140\ns_waitcnt lgkmcnt(2)\n"
"v_mfma_f32_16x16x32_bf16 v[96:99], v[168:171], a[0:3], 0\nTR 160,200,4096\nTR 162,200,6144\n"
"v_mfma_f32_16x16x32_bf16 v[100:103], v[168:171], a[4:7], 0\ns_add_u32 m0, s6, 0x1a000\ns_add_u32 s86, s6, 0x1e000\n"
"buffer_load_dwordx4 v220, s[48:51], s86 offen sc1 lds\nv_mfma_f32_16x16x32_bf16 v[104:107], v[168:171], a[8:11], 0\nv_cvt_pk_bf16_f32 v64, v64, v65\n"
"v_cvt_pk_bf16_f32 v65, v66, v67\nv_mfma_f32_16x16x32_bf16 v[108:111], v[168:171], a[12:15], 0\nv_cvt_pk_bf16_f32 v66, v80, v81\n"
"v_cvt_pk_bf16_f32 v67, v82, v83\ns_waitcnt lgkmcnt(2)\nv_mfma_f32_16x16x32_bf16 v[112:115], v[172:175], a[0:3], 0\nTR 164,201,4096\nTR 166,201,6144\n"
"v_mfma_f32_16x16x32_bf16 v[116:119], v[172:175], a[4:7], 0\ns_add_u32 m0, s6, 0x1b000\ns_add_u32 s86, s6, 0x1f000\n"
"buffer_load_dwordx4 v220, s[48:51], s86 offen sc1 lds\nv_mfma_f32_16x16x32_bf16 v[120:123], v[172:175], a[8:11], 0\nv_accvgpr_write_b32 a240, v64\n"
"v_accvgpr_write_b32 a241, v65\nv_mfma_f32_16x16x32_bf16 v[124:127], v[172:175], a[12:15], 0\nv_accvgpr_write_b32 a242, v66\n"
"v_accvgpr_write_b32 a243, v67\ns_waitcnt lgkmcnt(2)\nMA 96,160,16\nTR 168,200,8192\nTR 170,200,10240\nTR 172,201,8192\nTR 174,201,10240\n"
"MA 100,160,20\ns_add_u32 m0, s6, 0x1c000\ns_add_u32 s86, s6, 0x20000\nbuffer_load_dwordx4 v220, s[48:51], s86 offen sc1 lds\nMA 104,160,24\n"
"v_cvt_pk_bf16_f32 v68, v68, v69\nv_cvt_pk_bf16_f32 v69, v70, v71\nMA 108,160,28\nv_cvt_pk_bf16_f32 v70, v84, v85\nv_cvt_pk_bf16_f32 v71, v86, v87\n"
"s_waitcnt lgkmcnt(4)\nMA 112,164,16\nv_accvgpr_write_b32 a244, v68\nv_accvgpr_write_b32 a245, v69\nMA 116,164,20\ns_add_u32 m0, s6, 0x1d000\n"
"s_add_u32 s86, s6, 0x21000\nbuffer_load_dwordx4 v220, s[48:51], s86 offen sc1 lds\nMA 120,164,24\nv_accvgpr_write_b32 a246, v70\n"
"v_accvgpr_write_b32 a247, v71\nMA 124,164,28\nv_cvt_pk_bf16_f32 v72, v72, v73\nv_cvt_pk_bf16_f32 v73, v74, v75\ns_waitcnt vmcnt(28) lgkmcnt(0)\n"
"s_barrier\nMA 96,168,32\nTR 160,200,12288\nTR 162,200,14336\nMA 100,168,36\ns_add_u32 m0, s6, 0x1e000\ns_add_u32 s86, s6, 0x22000\n"
"buffer_load_dwordx4 v220, s[48:51], s86 offen sc1 lds\nMA 104,168,40\ns_lshl4_add_u32 s21, s78, 0x9000\n"
"buffer_store_dwordx4 a[216:219], v209, s[48:51], s21 offen sc1\nMA 108,168,44\nv_cvt_pk_bf16_f32 v74, v88, v89\nv_cvt_pk_bf16_f32 v75, v90, v91\n"
"MA 112,172,32\nTR 164,201,12288\nTR 166,201,14336\nMA 116,172,36\ns_add_u32 m0, s6, 0x1f000\ns_add_u32 s86, s6, 0x23000\n"
"buffer_load_dwordx4 v220, s[48:51], s86 offen sc1 lds\nMA 120,172,40\nv_accvgpr_write_b32 a248, v72\nv_accvgpr_write_b32 a249, v73\nMA 124,172,44\n"
"v_accvgpr_write_b32 a250, v74\nv_accvgpr_write_b32 a251, v75\ns_waitcnt lgkmcnt(2)\nMA 96,160,48\nTR 168,200,16384\nTR 170,200,18432\nMA 100,160,52\n"
"s_add_u32 m0, s6, 0x20000\ns_add_u32 s86, s6, 0x24000\nbuffer_load_dwordx4 v220, s[48:51], s86 offen sc1 lds\nMA 104,160,56\ns_waitcnt vmcnt(24)\n"
"v_add_f32 v8, v8, v9\nv_add_f32 v8, v8, v10\ns_nop 1\nv_max_f32_dpp v8, v8, v8 row_ror:8 row_mask:0xf bank_mask:0xf\ns_nop 1\n"
"v_max_f32_dpp v8, v8, v8 row_ror:4 row_mask:0xf bank_mask:0xf\ns_nop 1\nv_max_f32_dpp v8, v8, v8 row_ror:2 row_mask:0xf bank_mask:0xf\ns_nop 1\n"
"v_max_f32_dpp v8, v8, v8 row_ror:1 row_mask:0xf bank_mask:0xf\ns_nop 0\nv_readlane_b32 s22, v8, 0\nv_readlane_b32 s23, v8, 16\n"
"s_max_u32 s22, s22, s23\nv_mul_f32 v140, s22, v140\nv_mul_f32 v141, s22, v141\nv_sqrt_f32 v140, v140\nv_sqrt_f32 v141, v141\n"
"v_mul_f32 v140, 0x3f828f5c, v140\nv_mul_f32 v141, 0x3f828f5c, v141\nv_max_f32 v0, v140, v141\nv_cmp_nge_f32 vcc, 0x42200000, v0\n"
"s_cbranch_vccnz L_truemax\nL_truemax_ret:\nv_cmp_gt_u32 vcc, 0x200, v220\nv_xor_b32 v140, 0x80000000, v140\nv_and_b32 v0, 0xffff0000, v140\n"
"v_sub_f32 v1, v140, v0\nv_and_b32 v2, 0xffff0000, v1\nv_sub_f32 v3, v1, v2\nv_lshrrev_b32 v0, 16, v0\nv_or_b32 v0, v0, v2\nv_lshrrev_b32 v3, 16, v3\n"
"v_cndmask_b32 v137, v3, v0, vcc\nv_xor_b32 v141, 0x80000000, v141\nv_and_b32 v0, 0xffff0000, v141\nv_sub_f32 v1, v141, v0\n"
"v_and_b32 v2, 0xffff0000, v1\nv_sub_f32 v3, v1, v2\nv_lshrrev_b32 v0, 16, v0\nv_or_b32 v0, v0, v2\nv_lshrrev_b32 v3, 16, v3\n"
"v_cndmask_b32 v139, v3, v0, vcc\nMA 108,160,60\nv_mov_b32 v48, 0\nv_mov_b32 v49, 0\ns_waitcnt lgkmcnt(2)\nMA 112,164,48\nTR 172,201,16384\n"
"TR 174,201,18432\nMA 116,164,52\ns_add_u32 m0, s6, 0x21000\ns_add_u32 s86, s6, 0x25000\nbuffer_load_dwordx4 v220, s[48:51], s86 offen sc1 lds\n"
"MA 120,164,56\nv_mov_b32 v50, 0\nv_mov_b32 v51, 0\nMA 124,164,60\nv_mov_b32 v52, 0\nv_mov_b32 v53, 0\ns_waitcnt lgkmcnt(2)\nMA 96,168,64\n"
"TR 160,200,20480\nTR 162,200,22528\nTR 164,201,20480\nTR 166,201,22528\nMA 100,168,68\ns_add_u32 m0, s6, 0x22000\ns_add_u32 s86, s6, 0x26000\n"
"buffer_load_dwordx4 v220, s[48:51], s86 offen sc1 lds\nMA 104,168,72\nv_mov_b32 v54, 0\nv_mov_b32 v55, 0\nMA 108,168,76\nv_mov_b32 v56, 0\n"
"v_mov_b32 v57, 0\ns_waitcnt lgkmcnt(4)\nMA 112,172,64\nv_mov_b32 v58, 0\nv_mov_b32 v59, 0\nMA 116,172,68\ns_add_u32 m0, s6, 0x23000\n"
"s_add_u32 s86, s6, 0x27000\nbuffer_load_dwordx4 v220, s[48:51], s86 offen sc1 lds\nMA 120,172,72\nv_mov_b32 v60, 0\nv_mov_b32 v61, 0\nMA 124,172,76\n"
"v_mov_b32 v62, 0\nv_mov_b32 v63, 0\ns_waitcnt lgkmcnt(0)\ns_barrier\nMA 96,160,80\nTR 168,200,24576\nTR 170,200,26624\nMA 100,160,84\n"
"s_add_u32 m0, s6, 0x24000\ns_add_u32 s86, s6, 0x28000\nbuffer_load_dwordx4 v220, s[48:51], s86 offen sc1 lds\nMA 104,160,88\n"
"s_lshl4_add_u32 s21, s78, 0xd800\nbuffer_store_dwordx4 a[220:223], v209, s[48:51], s21 offen sc1\nMA 108,160,92\nv_mov_b32 v144, 0\n"
"v_mov_b32 v145, 0\nMA 112,164,80\nTR 172,201,24576\nTR 174,201,26624\nMA 116,164,84\ns_add_u32 m0, s6, 0x25000\ns_add_u32 s86, s6, 0x29000\n"
"buffer_load_dwordx4 v220, s[48:51], s86 offen sc1 lds\nMA 120,164,88\nv_mov_b32 v146, 0\nv_mov_b32 v147, 0\nMA 124,164,92\nv_mov_b32 v148, 0\n"
"v_mov_b32 v149, 0\ns_waitcnt lgkmcnt(2)\nMA 96,168,96\nTR 160,200,28672\nTR 162,200,30720\nMA 100,168,100\ns_add_u32 m0, s6, 0x26000\n"
"s_add_u32 s86, s6, 0x2a000\nbuffer_load_dwordx4 v220, s[48:51], s86 offen sc1 lds\nMA 104,168,104\nv_mov_b32 v150, 0\nv_mov_b32 v151, 0\n"
"MA 108,168,108\nv_mov_b32 v152, 0\nv_mov_b32 v153, 0\ns_waitcnt lgkmcnt(2)\nMA 112,172,96\nTR 164,201,28672\nTR 166,201,30720\nMA 116,172,100\n"
"s_add_u32 m0, s6, 0x27000\ns_add_u32 s86, s6, 0x2b000\nbuffer_load_dwordx4 v220, s[48:51], s86 offen sc1 lds\nMA 120,172,104\nv_mov_b32 v154, 0\n"
"v_mov_b32 v155, 0\nMA 124,172,108\nv_mov_b32 v156, 0\nv_mov_b32 v157, 0\ns_waitcnt lgkmcnt(2)\nMA 96,160,112\nTR 168,200,32768\nTR 170,200,34816\n"
"TR 172,201,32768\nTR 174,201,34816\nMA 100,160,116\ns_add_u32 m0, s6, 0x12000\ns_add_u32 s86, s6, 0x16000\n"
"buffer_load_dwordx4 v220, s[48:51], s86 offen sc1 lds\nMA 104,160,120\nv_mov_b32 v158, 0\nv_mov_b32 v159, 0\nMA 108,160,124\n"
"v_cvt_pk_bf16_f32 v76, v76, v77\nv_cvt_pk_bf16_f32 v77, v78, v79\ns_waitcnt lgkmcnt(4)\nMA 112,164,112\ns_lshl4_add_u32 s21, s79, 0\n"
"buffer_store_dwordx4 a[224:227], v209, s[48:51], s21 offen sc1\nMA 116,164,116\ns_add_u32 m0, s6, 0x13000\ns_add_u32 s86, s6, 0x17000\n"
"buffer_load_dwordx4 v220, s[48:51], s86 offen sc1 lds\nMA 120,164,120\nv_cvt_pk_bf16_f32 v78, v92, v93\nv_cvt_pk_bf16_f32 v79, v94, v95\n"
"MA 124,164,124\nv_accvgpr_write_b32 a252, v76\nv_accvgpr_write_b32 a253, v77\ns_waitcnt vmcnt(34) lgkmcnt(0)\ns_barrier\nMA 96,168,128\n"
"TR 160,200,36864\nTR 162,200,38912\nMA 100,168,132\ns_add_u32 m0, s6, 0x14000\ns_add_u32 s86, s6, 0x18000\n"
"buffer_load_dwordx4 v220, s[48:51], s86 offen sc1 lds\nMA 104,168,136\nv_accvgpr_write_b32 a254, v78\nv_accvgpr_write_b32 a255, v79\nMA 108,168,140\n"
"MA 112,172,128\nTR 164,201,36864\nTR 166,201,38912\nMA 116,172,132\ns_add_u32 m0, s6, 0\ns_add_u32 s86, s6, 0x4000\n"
"buffer_load_dwordx4 v220, s[48:51], s86 offen sc1 lds\nMA 120,172,136\nMA 124,172,140\ns_waitcnt lgkmcnt(2)\n"
"v_mfma_f32_16x16x32_bf16 v[64:67], v[160:163], a[0:3], 0\nTR 168,200,40960\nTR 170,200,43008\n"
"v_mfma_f32_16x16x32_bf16 v[68:71], v[160:163], a[4:7], 0\ns_add_u32 m0, s6, 0x1000\ns_add_u32 s86, s6, 0x5000\n"
"buffer_load_dwordx4 v220, s[48:51], s86 offen sc1 lds\nv_mfma_f32_16x16x32_bf16 v[72:75], v[160:163], a[8:11], 0\nv_cvt_pk_bf16_f32 v96, v96, v97\n"
"v_cvt_pk_bf16_f32 v97, v98, v99\nv_mfma_f32_16x16x32_bf16 v[76:79], v[160:163], a[12:15], 0\nv_cvt_pk_bf16_f32 v98, v112, v113\n"
"v_cvt_pk_bf16_f32 v99, v114, v115\ns_waitcnt lgkmcnt(2)\nv_mfma_f32_16x16x32_bf16 v[80:83], v[164:167], a[0:3], 0\nTR 172,201,40960\n"
"TR 174,201,43008\nv_mfma_f32_16x16x32_bf16 v[84:87], v[164:167], a[4:7], 0\ns_add_u32 m0, s6, 0x2000\ns_add_u32 s86, s6, 0x6000\n"
"buffer_load_dwordx4 v220, s[48:51], s86 offen sc1 lds\nv_mfma_f32_16x16x32_bf16 v[88:91], v[164:167], a[8:11], 0\nv_accvgpr_write_b32 a144, v96\n"
"v_accvgpr_write_b32 a145, v97\nv_mfma_f32_16x16x32_bf16 v[92:95], v[164:167], a[12:15], 0\nv_accvgpr_write_b32 a146, v98\n"
"v_accvgpr_write_b32 a147, v99\ns_waitcnt lgkmcnt(2)\nMA 64,168,16\nTR 160,200,45056\nTR 162,200,47104\nTR 164,201,45056\nTR 166,201,47104\n"
"MA 68,168,20\ns_add_u32 m0, s6, 0x3000\ns_add_u32 s86, s6, 0x7000\nbuffer_load_dwordx4 v220, s[48:51], s86 offen sc1 lds\nMA 72,168,24\n"
"s_lshl4_add_u32 s21, s79, 0x4800\nbuffer_store_dwordx4 a[228:231], v209, s[48:51], s21 offen sc1\nMA 76,168,28\nv_cvt_pk_bf16_f32 v100, v100, v101\n"
"v_cvt_pk_bf16_f32 v101, v102, v103\ns_waitcnt lgkmcnt(4)\nMA 80,172,16\nv_cvt_pk_bf16_f32 v102, v116, v117\nv_cvt_pk_bf16_f32 v103, v118, v119\n"
"MA 84,172,20\ns_add_u32 m0, s6, 0x4000\ns_add_u32 s86, s6, 0x8000\nbuffer_load_dwordx4 v220, s[48:51], s86 offen sc1 lds\nMA 88,172,24\n"
"v_accvgpr_write_b32 a148, v100\nv_accvgpr_write_b32 a149, v101\nMA 92,172,28\nv_accvgpr_write_b32 a150, v102\nv_accvgpr_write_b32 a151, v103\n"
"s_waitcnt vmcnt(37) lgkmcnt(0)\ns_barrier\nMA 64,160,32\nTR 168,200,49152\nTR 170,200,51200\nMA 68,160,36\ns_add_u32 m0, s6, 0x5000\n"
"s_add_u32 s86, s6, 0x9000\nbuffer_load_dwordx4 v220, s[48:51], s86 offen sc1 lds\nMA 72,160,40\nv_cvt_pk_bf16_f32 v104, v104, v105\n"
"v_cvt_pk_bf16_f32 v105, v106, v107\nMA 76,160,44\nv_cvt_pk_bf16_f32 v106, v120, v121\nv_cvt_pk_bf16_f32 v107, v122, v123\nMA 80,164,32\n"
"TR 172,201,49152\nTR 174,201,51200\nMA 84,164,36\ns_add_u32 m0, s6, 0x6000\ns_add_u32 s86, s6, 0xa000\n"
"buffer_load_dwordx4 v220, s[48:51], s86 offen sc1 lds\nMA 88,164,40\nv_accvgpr_write_b32 a152, v104\nv_accvgpr_write_b32 a153, v105\nMA 92,164,44\n"
"v_accvgpr_write_b32 a154, v106\nv_accvgpr_write_b32 a155, v107\ns_waitcnt lgkmcnt(2)\nMA 64,168,48\nTR 160,200,53248\nTR 162,200,55296\nMA 68,168,52\n"
"s_add_u32 m0, s6, 0x7000\ns_add_u32 s86, s6, 0xb000\nbuffer_load_dwordx4 v220, s[48:51], s86 offen sc1 lds\nMA 72,168,56\n"
"v_cvt_pk_bf16_f32 v108, v108, v109\nv_cvt_pk_bf16_f32 v109, v110, v111\nMA 76,168,60\nv_cvt_pk_bf16_f32 v110, v124, v125\n"
"v_cvt_pk_bf16_f32 v111, v126, v127\ns_waitcnt lgkmcnt(2)\nMA 80,172,48\nTR 164,201,53248\nTR 166,201,55296\nMA 84,172,52\ns_add_u32 m0, s6, 0x8000\n"
"s_add_u32 s86, s6, 0xc000\nbuffer_load_dwordx4 v220, s[48:51], s86 offen sc1 lds\nMA 88,172,56\nv_accvgpr_write_b32 a156, v108\n"
"v_accvgpr_write_b32 a157, v109\nMA 92,172,60\nv_accvgpr_write_b32 a158, v110\nv_accvgpr_write_b32 a159, v111\ns_waitcnt lgkmcnt(2)\nMA 64,160,64\n"
"TR 168,200,57344\nTR 170,200,59392\nTR 172,201,57344\nTR 174,201,59392\nMA 68,160,68\ns_add_u32 m0, s6, 0x9000\ns_add_u32 s86, s6, 0xd000\n"
"buffer_load_dwordx4 v220, s[48:51], s86 offen sc1 lds\nMA 72,160,72\ns_lshl4_add_u32 s21, s79, 0x9000\n"
"buffer_store_dwordx4 a[232:235], v209, s[48:51], s21 offen sc1\nMA 76,160,76\ns_waitcnt lgkmcnt(4)\nMA 80,164,64\nMA 84,164,68\n"
"s_add_u32 m0, s6, 0xa000\ns_add_u32 s86, s6, 0xe000\nbuffer_load_dwordx4 v220, s[48:51], s86 offen sc1 lds\nMA 88,164,72\nMA 92,164,76\n"
"s_waitcnt vmcnt(40) lgkmcnt(0)\ns_barrier\nMA 64,168,80\nTR 160,200,61440\nTR 162,200,63488\nMA 68,168,84\ns_add_u32 m0, s6, 0xb000\n"
"s_add_u32 s86, s6, 0xf000\nbuffer_load_dwordx4 v220, s[48:51], s86 offen sc1 lds\nMA 72,168,88\nMA 76,168,92\nMA 80,172,80\nTR 164,201,61440\n"
"TR 166,201,63488\nMA 84,172,84\nMA 88,172,88\nMA 92,172,92\ns_waitcnt lgkmcnt(2)\nMA 64,160,96\nds_read_b64_tr_b16 v[168:169], v202\nTR 170,202,2048\n"
"MA 68,160,100\ns_add_u32 m0, s6, 0xc000\ns_add_u32 s86, s6, 0x10000\nbuffer_load_dwordx4 v220, s[48:51], s86 offen sc1 lds\nMA 72,160,104\n"
"MA 76,160,108\ns_waitcnt lgkmcnt(2)\nMA 80,164,96\nds_read_b64_tr_b16 v[172:173], v203\nTR 174,203,2048\nMA 84,164,100\ns_add_u32 m0, s6, 0xd000\n"
"s_add_u32 s86, s6, 0x11000\nbuffer_load_dwordx4 v220, s[48:51], s86 offen sc1 lds\nMA 88,164,104\ns_lshl4_add_u32 s21, s79, 0xd800\n"
"buffer_store_dwordx4 a[236:239], v209, s[48:51], s21 offen sc1\nMA 92,164,108\ns_waitcnt lgkmcnt(2)\nMA 64,168,112\nTR 160,202,4096\nTR 162,202,6144\n"
"TR 164,203,4096\nTR 166,203,6144\nMA 68,168,116\ns_add_u32 m0, s6, 0xe000\ns_add_u32 s86, s6, 0x12000\n"
"buffer_load_dwordx4 v220, s[48:51], s86 offen sc1 lds\nMA 72,168,120\nMA 76,168,124\ns_waitcnt lgkmcnt(4)\nMA 80,172,112\nMA 84,172,116\n"
"MA 88,172,120\nMA 92,172,124\ns_waitcnt vmcnt(22) lgkmcnt(0)\ns_barrier\nMA 64,160,128\nMA 68,160,132\nMA 72,160,136\nMA 76,160,140\nMA 80,164,128\n"
"MA 84,164,132\nMA 88,164,136\nMA 92,164,140\nv_mov_b32 v111, v186\nv_mov_b32 v108, 0xdddddddd\nv_mov_b32 v110, v187\nv_mov_b32 v109, v184\n"
"v_add_u32 v250, 0x15400, v250\nv_add_u32 v221, 0x15400, v221\nv_add_u32 v222, 0x15400, v222\nv_add_u32 v111, 0x15400, v111\n"
"ds_read_b128 v[96:99], v250\nds_read_b64 v[104:105], v221\nds_read_b128 v[176:179], v222\nds_read_b128 v[180:183], v111\ns_waitcnt lgkmcnt(2)\n"
"v_mfma_f32_32x32x16_bf16 v[0:15], v[96:99], v[128:131], 0\nv_mfma_f32_32x32x8bf16_1k v[0:15], v[104:105], v[136:137], v[0:15]\n"
"v_mfma_f32_32x32x16_bf16 v[16:31], v[96:99], v[132:135], 0\nds_read_b128 v[100:103], v250 offset:2560\nds_read_b64 v[106:107], v221 offset:2560\n"
"ds_read_b128 v[160:163], v222 offset:2560\nds_read_b128 v[164:167], v111 offset:2560\ns_nop 7\nEX 0\nEX 1\nEX 2\nEX 3\n"
"v_mfma_f32_32x32x8bf16_1k v[16:31], v[104:105], v[138:139], v[16:31]\nEX 4\nEX 5\nEX 6\nEX 7\ns_lshl4_add_u32 s21, s80, 0\n"
"buffer_store_dwordx4 a[240:243], v209, s[48:51], s21 offen sc1\ns_add_u32 m0, s6, 0xf000\ns_add_u32 s86, s6, 0x13000\n"
"buffer_load_dwordx4 v220, s[48:51], s86 offen sc1 lds\nEX 8\nEX 9\nEX 10\nEX 11\ns_waitcnt lgkmcnt(4)\n"
"v_smfmac_f32_32x32x32_bf16 v[48:63], v[176:179], v[0:7], v108\nEX 12\nEX 13\nEX 14\nEX 15\ns_waitcnt lgkmcnt(0)\n"
"v_mfma_f32_32x32x16_bf16 v[32:47], v[100:103], v[128:131], 0\nEX 16\nEX 17\nEX 18\nEX 19\n"
"v_mfma_f32_32x32x8bf16_1k v[32:47], v[106:107], v[136:137], v[32:47]\ns_add_u32 m0, s6, 0x10000\ns_add_u32 s86, s6, 0x14000\n"
"buffer_load_dwordx4 v220, s[48:51], s86 offen sc1 lds\nEX 20\nEX 21\nEX 22\nEX 23\nv_smfmac_f32_32x32x32_bf16 v[48:63], v[180:183], v[8:15], v108\n"
"s_add_u32 m0, s6, 0x11000\ns_add_u32 s86, s6, 0x15000\nbuffer_load_dwordx4 v220, s[48:51], s86 offen sc1 lds\nEX 24\nEX 25\nEX 26\nEX 27\n"
"v_smfmac_f32_32x32x32_bf16 v[144:159], v[176:179], v[16:23], v108\nEX 28\nEX 29\nEX 30\nEX 31\n"
"v_mfma_f32_32x32x16_bf16 v[0:15], v[100:103], v[132:135], 0\nds_read_b128 v[96:99], v250 offset:5120\nds_read_b64 v[104:105], v221 offset:5120\n"
"ds_read_b128 v[168:171], v222 offset:5120\nds_read_b128 v[172:175], v111 offset:5120\nEX 32\nEX 33\nEX 34\nEX 35\n"
"v_mfma_f32_32x32x8bf16_1k v[0:15], v[106:107], v[138:139], v[0:15]\nEX 36\nEX 37\nEX 38\nEX 39\n"
"v_smfmac_f32_32x32x32_bf16 v[144:159], v[180:183], v[24:31], v108\ns_lshl4_add_u32 s21, s80, 0x4800\n"
"buffer_store_dwordx4 a[244:247], v209, s[48:51], s21 offen sc1\nv_cvt_pk_bf16_f32 v64, v64, v65\nEX 40\nEX 41\nEX 42\nEX 43\n"
"v_smfmac_f32_32x32x32_bf16 v[48:63], v[160:163], v[32:39], v108\nEX 44\nEX 45\nEX 46\nEX 47\ns_waitcnt lgkmcnt(0)\n"
"v_mfma_f32_32x32x16_bf16 v[16:31], v[96:99], v[128:131], 0\nEX 0\nEX 1\nEX 2\nEX 3\n"
"v_mfma_f32_32x32x8bf16_1k v[16:31], v[104:105], v[136:137], v[16:31]\nv_cvt_pk_bf16_f32 v65, v66, v67\nv_cvt_pk_bf16_f32 v66, v80, v81\n"
"v_cvt_pk_bf16_f32 v67, v82, v83\nEX 4\nEX 5\nEX 6\nEX 7\nv_smfmac_f32_32x32x32_bf16 v[48:63], v[164:167], v[40:47], v108\n"
"v_accvgpr_write_b32 a160, v64\nv_accvgpr_write_b32 a161, v65\nv_accvgpr_write_b32 a162, v66\nEX 8\nEX 9\nEX 10\nEX 11\n"
"v_smfmac_f32_32x32x32_bf16 v[144:159], v[160:163], v[0:7], v108\nEX 12\nEX 13\nEX 14\nEX 15\n"
"v_mfma_f32_32x32x16_bf16 v[32:47], v[96:99], v[132:135], 0\nds_read_b128 v[100:103], v250 offset:7680\nds_read_b64 v[106:107], v221 offset:7680\n"
"ds_read_b128 v[176:179], v222 offset:7680\nds_read_b128 v[180:183], v111 offset:7680\nEX 16\nEX 17\nEX 18\nEX 19\n"
"v_mfma_f32_32x32x8bf16_1k v[32:47], v[104:105], v[138:139], v[32:47]\nEX 20\nEX 21\nEX 22\nEX 23\n"
"v_smfmac_f32_32x32x32_bf16 v[144:159], v[164:167], v[8:15], v108\ns_lshl4_add_u32 s21, s80, 0x9000\n"
"buffer_store_dwordx4 a[248:251], v209, s[48:51], s21 offen sc1\nv_accvgpr_write_b32 a163, v67\nEX 24\nEX 25\nEX 26\nEX 27\n"
"v_smfmac_f32_32x32x32_bf16 v[48:63], v[168:171], v[16:23], v108\nEX 28\nEX 29\nEX 30\nEX 31\ns_waitcnt lgkmcnt(0)\n"
"v_mfma_f32_32x32x16_bf16 v[0:15], v[100:103], v[128:131], 0\nEX 32\nEX 33\nEX 34\nEX 35\n"
"v_mfma_f32_32x32x8bf16_1k v[0:15], v[106:107], v[136:137], v[0:15]\ns_lshl4_add_u32 s21, s80, 0xd800\n"
"buffer_store_dwordx4 a[252:255], v209, s[48:51], s21 offen sc1\nv_cvt_pk_bf16_f32 v68, v68, v69\nEX 36\nEX 37\nEX 38\nEX 39\n"
"v_smfmac_f32_32x32x32_bf16 v[48:63], v[172:175], v[24:31], v108\nv_cvt_pk_bf16_f32 v69, v70, v71\nv_cvt_pk_bf16_f32 v70, v84, v85\n"
"v_cvt_pk_bf16_f32 v71, v86, v87\nEX 40\nEX 41\nEX 42\nEX 43\nv_smfmac_f32_32x32x32_bf16 v[144:159], v[168:171], v[32:39], v108\nEX 44\nEX 45\nEX 46\n"
"EX 47\nv_mfma_f32_32x32x16_bf16 v[16:31], v[100:103], v[132:135], 0\nds_read_b128 v[96:99], v250 offset:10240\n"
"ds_read_b64 v[104:105], v221 offset:10240\nds_read_b128 v[160:163], v222 offset:10240\nds_read_b128 v[164:167], v111 offset:10240\nEX 0\nEX 1\nEX 2\n"
"EX 3\nv_mfma_f32_32x32x8bf16_1k v[16:31], v[106:107], v[138:139], v[16:31]\nEX 4\nEX 5\nEX 6\nEX 7\n"
"v_smfmac_f32_32x32x32_bf16 v[144:159], v[172:175], v[40:47], v108\nv_accvgpr_write_b32 a164, v68\nv_accvgpr_write_b32 a165, v69\n"
"v_accvgpr_write_b32 a166, v70\nEX 8\nEX 9\nEX 10\nEX 11\nv_smfmac_f32_32x32x32_bf16 v[48:63], v[176:179], v[0:7], v108\nEX 12\nEX 13\nEX 14\nEX 15\n"
"s_waitcnt lgkmcnt(0)\nv_mfma_f32_32x32x16_bf16 v[32:47], v[96:99], v[128:131], 0\nEX 16\nEX 17\nEX 18\nEX 19\n"
"v_mfma_f32_32x32x8bf16_1k v[32:47], v[104:105], v[136:137], v[32:47]\ns_lshl4_add_u32 s21, s81, 0\n"
"buffer_store_dwordx4 a[144:147], v209, s[48:51], s21 offen sc1\nv_accvgpr_write_b32 a167, v71\nEX 20\nEX 21\nEX 22\nEX 23\n"
"v_smfmac_f32_32x32x32_bf16 v[48:63], v[180:183], v[8:15], v108\nv_cvt_pk_bf16_f32 v72, v72, v73\nv_cvt_pk_bf16_f32 v73, v74, v75\n"
"v_cvt_pk_bf16_f32 v74, v88, v89\nEX 24\nEX 25\nEX 26\nEX 27\nv_smfmac_f32_32x32x32_bf16 v[144:159], v[176:179], v[16:23], v108\nEX 28\nEX 29\nEX 30\n"
"EX 31\nv_mfma_f32_32x32x16_bf16 v[0:15], v[96:99], v[132:135], 0\nds_read_b128 v[100:103], v250 offset:12800\n"
"ds_read_b64 v[106:107], v221 offset:12800\nds_read_b128 v[168:171], v222 offset:12800\nds_read_b128 v[172:175], v111 offset:12800\nEX 32\nEX 33\n"
"EX 34\nEX 35\nv_mfma_f32_32x32x8bf16_1k v[0:15], v[104:105], v[138:139], v[0:15]\nEX 36\nEX 37\nEX 38\nEX 39\n"
"v_smfmac_f32_32x32x32_bf16 v[144:159], v[180:183], v[24:31], v108\nv_cvt_pk_bf16_f32 v75, v90, v91\nv_accvgpr_write_b32 a168, v72\n"
"v_accvgpr_write_b32 a169, v73\nEX 40\nEX 41\nEX 42\nEX 43\nv_smfmac_f32_32x32x32_bf16 v[48:63], v[160:163], v[32:39], v108\nEX 44\nEX 45\nEX 46\n"
"EX 47\ns_waitcnt lgkmcnt(0)\nv_mfma_f32_32x32x16_bf16 v[16:31], v[100:103], v[128:131], 0\nEX 0\nEX 1\nEX 2\nEX 3\n"
"v_mfma_f32_32x32x8bf16_1k v[16:31], v[106:107], v[136:137], v[16:31]\ns_lshl4_add_u32 s21, s81, 0x4800\n"
"buffer_store_dwordx4 a[148:151], v209, s[48:51], s21 offen sc1\nv_accvgpr_write_b32 a170, v74\nEX 4\nEX 5\nEX 6\nEX 7\n"
"v_smfmac_f32_32x32x32_bf16 v[48:63], v[164:167], v[40:47], v108\nv_accvgpr_write_b32 a171, v75\nv_cvt_pk_bf16_f32 v76, v76, v77\n"
"v_cvt_pk_bf16_f32 v77, v78, v79\nEX 8\nEX 9\nEX 10\nEX 11\nv_smfmac_f32_32x32x32_bf16 v[144:159], v[160:163], v[0:7], v108\nEX 12\nEX 13\nEX 14\n"
"EX 15\nv_mfma_f32_32x32x16_bf16 v[32:47], v[100:103], v[132:135], 0\nds_read_b128 v[96:99], v250 offset:15360\n"
"ds_read_b64 v[104:105], v221 offset:15360\nds_read_b128 v[176:179], v222 offset:15360\nds_read_b128 v[180:183], v111 offset:15360\nEX 16\nEX 17\n"
"EX 18\nEX 19\nv_mfma_f32_32x32x8bf16_1k v[32:47], v[106:107], v[138:139], v[32:47]\nEX 20\nEX 21\nEX 22\nEX 23\n"
"v_smfmac_f32_32x32x32_bf16 v[144:159], v[164:167], v[8:15], v108\ns_lshl4_add_u32 s21, s81, 0x9000\n"
"buffer_store_dwordx4 a[152:155], v209, s[48:51], s21 offen sc1\nv_cvt_pk_bf16_f32 v78, v92, v93\nEX 24\nEX 25\nEX 26\nEX 27\n"
"v_smfmac_f32_32x32x32_bf16 v[48:63], v[168:171], v[16:23], v108\nEX 28\nEX 29\nEX 30\nEX 31\ns_waitcnt lgkmcnt(0)\n"
"v_mfma_f32_32x32x16_bf16 v[0:15], v[96:99], v[128:131], 0\nEX 32\nEX 33\nEX 34\nEX 35\n"
"v_mfma_f32_32x32x8bf16_1k v[0:15], v[104:105], v[136:137], v[0:15]\nv_cvt_pk_bf16_f32 v79, v94, v95\nv_accvgpr_write_b32 a172, v76\n"
"v_accvgpr_write_b32 a173, v77\nEX 36\nEX 37\nEX 38\nEX 39\nv_smfmac_f32_32x32x32_bf16 v[48:63], v[172:175], v[24:31], v108\n"
"v_accvgpr_write_b32 a174, v78\nv_accvgpr_write_b32 a175, v79\ns_cmp_eq_u32 s14, 0\ns_cbranch_scc1 L_arr_skip\nv_mov_b32 v64, s84\nv_mov_b32 v65, s85\n"
"buffer_store_dwordx2 v[64:65], v248, s[48:51], s8 offen sc1\nL_arr_skip:\nEX 40\nEX 41\nEX 42\nEX 43\n"
"v_smfmac_f32_32x32x32_bf16 v[144:159], v[168:171], v[32:39], v108\nEX 44\nEX 45\nEX 46\nEX 47\n"
"v_mfma_f32_32x32x16_bf16 v[16:31], v[96:99], v[132:135], 0\nds_read_b128 v[100:103], v250 offset:17920\nds_read_b64 v[106:107], v221 offset:17920\n"
"ds_read_b128 v[160:163], v222 offset:17920\nds_read_b128 v[164:167], v111 offset:17920\nEX 0\nEX 1\nEX 2\nEX 3\n"
"v_mfma_f32_32x32x8bf16_1k v[16:31], v[104:105], v[138:139], v[16:31]\nEX 4\nEX 5\nEX 6\nEX 7\n"
"v_smfmac_f32_32x32x32_bf16 v[144:159], v[172:175], v[40:47], v108\ns_lshl4_add_u32 s21, s81, 0xd800\n"
"buffer_store_dwordx4 a[156:159], v209, s[48:51], s21 offen sc1\nv_mov_b32 v68, -1\nEX 8\nEX 9\nEX 10\nEX 11\n"
"v_smfmac_f32_32x32x32_bf16 v[48:63], v[176:179], v[0:7], v108\nEX 12\nEX 13\nEX 14\nEX 15\ns_waitcnt lgkmcnt(0)\n"
"v_mfma_f32_32x32x16_bf16 v[32:47], v[100:103], v[128:131], 0\nEX 16\nEX 17\nEX 18\nEX 19\n"
"v_mfma_f32_32x32x8bf16_1k v[32:47], v[106:107], v[136:137], v[32:47]\nv_mov_b32 v69, -1\nv_mov_b32 v70, -1\nv_mov_b32 v71, -1\nEX 20\nEX 21\nEX 22\n"
"EX 23\nv_smfmac_f32_32x32x32_bf16 v[48:63], v[180:183], v[8:15], v108\nbuffer_load_dword v253, v185, s[52:55], 0 offen\nEX 24\nEX 25\nEX 26\nEX 27\n"
"v_smfmac_f32_32x32x32_bf16 v[144:159], v[176:179], v[16:23], v108\nEX 28\nEX 29\nEX 30\nEX 31\n"
"v_mfma_f32_32x32x16_bf16 v[0:15], v[100:103], v[132:135], 0\nds_read_b128 v[96:99], v250 offset:20480\nds_read_b64 v[104:105], v221 offset:20480\n"
"ds_read_b128 v[168:171], v222 offset:20480\nds_read_b128 v[172:175], v111 offset:20480\nEX 32\nEX 33\nEX 34\nEX 35\n"
"v_mfma_f32_32x32x8bf16_1k v[0:15], v[106:107], v[138:139], v[0:15]\nEX 36\nEX 37\nEX 38\nEX 39\n"
"v_smfmac_f32_32x32x32_bf16 v[144:159], v[180:183], v[24:31], v108\ns_lshl4_add_u32 s21, s19, 0\n"
"buffer_store_dwordx4 a[160:163], v209, s[48:51], s21 offen sc1\nEX 40\nEX 41\nEX 42\nEX 43\n"
"v_smfmac_f32_32x32x32_bf16 v[48:63], v[160:163], v[32:39], v108\nEX 44\nEX 45\nEX 46\nEX 47\ns_waitcnt lgkmcnt(0)\n"
"v_mfma_f32_32x32x16_bf16 v[16:31], v[96:99], v[128:131], 0\nEX 0\nEX 1\nEX 2\nEX 3\n"
"v_mfma_f32_32x32x8bf16_1k v[16:31], v[104:105], v[136:137], v[16:31]\ns_lshl4_add_u32 s21, s19, 0x4800\n"
"buffer_store_dwordx4 a[164:167], v209, s[48:51], s21 offen sc1\nEX 4\nEX 5\nEX 6\nEX 7\n"
"v_smfmac_f32_32x32x32_bf16 v[48:63], v[164:167], v[40:47], v108\nEX 8\nEX 9\nEX 10\nEX 11\n"
"v_smfmac_f32_32x32x32_bf16 v[144:159], v[160:163], v[0:7], v108\nEX 12\nEX 13\nEX 14\nEX 15\n"
"v_mfma_f32_32x32x16_bf16 v[32:47], v[96:99], v[132:135], 0\nds_read_b128 v[100:103], v250 offset:23040\nds_read_b64 v[106:107], v221 offset:23040\n"
"ds_read_b128 v[176:179], v222 offset:23040\nds_read_b128 v[180:183], v111 offset:23040\nEX 16\nEX 17\nEX 18\nEX 19\n"
"v_mfma_f32_32x32x8bf16_1k v[32:47], v[104:105], v[138:139], v[32:47]\nEX 20\nEX 21\nEX 22\nEX 23\n"
"v_smfmac_f32_32x32x32_bf16 v[144:159], v[164:167], v[8:15], v108\nEX 24\nEX 25\nEX 26\nEX 27\n"
"v_smfmac_f32_32x32x32_bf16 v[48:63], v[168:171], v[16:23], v108\nEX 28\nEX 29\nEX 30\nEX 31\ns_waitcnt lgkmcnt(0)\n"
"v_mfma_f32_32x32x16_bf16 v[0:15], v[100:103], v[128:131], 0\nEX 32\nEX 33\nEX 34\nEX 35\n"
"v_mfma_f32_32x32x8bf16_1k v[0:15], v[106:107], v[136:137], v[0:15]\ns_lshl4_add_u32 s21, s19, 0x9000\n"
"buffer_store_dwordx4 a[168:171], v209, s[48:51], s21 offen sc1\nEX 36\nEX 37\nEX 38\nEX 39\n"
"v_smfmac_f32_32x32x32_bf16 v[48:63], v[172:175], v[24:31], v108\nEX 40\nEX 41\nEX 42\nEX 43\n"
"v_smfmac_f32_32x32x32_bf16 v[144:159], v[168:171], v[32:39], v108\nEX 44\nEX 45\nEX 46\nEX 47\n"
"v_mfma_f32_32x32x16_bf16 v[16:31], v[100:103], v[132:135], 0\nds_read_b128 v[96:99], v250 offset:25600\nds_read_b64 v[104:105], v221 offset:25600\n"
"ds_read_b128 v[160:163], v222 offset:25600\nds_read_b128 v[164:167], v111 offset:25600\nEX 0\nEX 1\nEX 2\nEX 3\n"
"v_mfma_f32_32x32x8bf16_1k v[16:31], v[106:107], v[138:139], v[16:31]\nEX 4\nEX 5\nEX 6\nEX 7\n"
"v_smfmac_f32_32x32x32_bf16 v[144:159], v[172:175], v[40:47], v108\nEX 8\nEX 9\nEX 10\nEX 11\n"
"v_smfmac_f32_32x32x32_bf16 v[48:63], v[176:179], v[0:7], v108\nEX 12\nEX 13\nEX 14\nEX 15\ns_waitcnt lgkmcnt(0)\n"
"v_mfma_f32_32x32x16_bf16 v[32:47], v[96:99], v[128:131], 0\nEX 16\nEX 17\nEX 18\nEX 19\n"
"v_mfma_f32_32x32x8bf16_1k v[32:47], v[104:105], v[136:137], v[32:47]\ns_lshl4_add_u32 s21, s19, 0xd800\n"
"buffer_store_dwordx4 a[172:175], v209, s[48:51], s21 offen sc1\nEX 20\nEX 21\nEX 22\nEX 23\n"
"v_smfmac_f32_32x32x32_bf16 v[48:63], v[180:183], v[8:15], v108\nEX 24\nEX 25\nEX 26\nEX 27\n"
"v_smfmac_f32_32x32x32_bf16 v[144:159], v[176:179], v[16:23], v108\nEX 28\nEX 29\nEX 30\nEX 31\n"
"v_mfma_f32_32x32x16_bf16 v[0:15], v[96:99], v[132:135], 0\nds_read_b128 v[100:103], v250 offset:28160\nds_read_b64 v[106:107], v221 offset:28160\n"
"ds_read_b128 v[168:171], v222 offset:28160\nds_read_b128 v[172:175], v111 offset:28160\nEX 32\nEX 33\nEX 34\nEX 35\n"
"v_mfma_f32_32x32x8bf16_1k v[0:15], v[104:105], v[138:139], v[0:15]\nEX 36\nEX 37\nEX 38\nEX 39\n"
"v_smfmac_f32_32x32x32_bf16 v[144:159], v[180:183], v[24:31], v108\ns_mov_b32 s21, 0\nbuffer_store_dwordx4 v[68:71], v223, s[48:51], s21 offen sc1\n"
"EX 40\nEX 41\nEX 42\nEX 43\nv_smfmac_f32_32x32x32_bf16 v[48:63], v[160:163], v[32:39], v108\nEX 44\nEX 45\nEX 46\nEX 47\ns_waitcnt lgkmcnt(0)\n"
"v_mfma_f32_32x32x16_bf16 v[16:31], v[100:103], v[128:131], 0\nEX 0\nEX 1\nEX 2\nEX 3\n"
"v_mfma_f32_32x32x8bf16_1k v[16:31], v[106:107], v[136:137], v[16:31]\nEX 4\nEX 5\nEX 6\nEX 7\n"
"v_smfmac_f32_32x32x32_bf16 v[48:63], v[164:167], v[40:47], v108\nEX 8\nEX 9\nEX 10\nEX 11\n"
"v_smfmac_f32_32x32x32_bf16 v[144:159], v[160:163], v[0:7], v108\nEX 12\nEX 13\nEX 14\nEX 15\n"
"v_mfma_f32_32x32x16_bf16 v[32:47], v[100:103], v[132:135], 0\nds_read_b128 v[96:99], v250 offset:30720\nds_read_b64 v[104:105], v221 offset:30720\n"
"ds_read_b128 v[176:179], v222 offset:30720\nds_read_b128 v[180:183], v111 offset:30720\nEX 16\nEX 17\nEX 18\nEX 19\n"
"v_mfma_f32_32x32x8bf16_1k v[32:47], v[106:107], v[138:139], v[32:47]\nEX 20\nEX 21\nEX 22\nEX 23\n"
"v_smfmac_f32_32x32x32_bf16 v[144:159], v[164:167], v[8:15], v108\ns_mov_b32 s21, 0x8000\n"
"buffer_store_dwordx4 v[68:71], v223, s[48:51], s21 offen sc1\nbuffer_store_dword v68, v224, s[48:51], 0 offen sc1\nEX 24\nEX 25\nEX 26\nEX 27\n"
"v_smfmac_f32_32x32x32_bf16 v[48:63], v[168:171], v[16:23], v108\nEX 28\nEX 29\nEX 30\nEX 31\ns_waitcnt lgkmcnt(0)\n"
"v_mfma_f32_32x32x16_bf16 v[0:15], v[96:99], v[128:131], 0\nEX 32\nEX 33\nEX 34\nEX 35\n"
"v_mfma_f32_32x32x8bf16_1k v[0:15], v[104:105], v[136:137], v[0:15]\nEX 36\nEX 37\nEX 38\nEX 39\n"
"v_smfmac_f32_32x32x32_bf16 v[48:63], v[172:175], v[24:31], v108\nEX 40\nEX 41\nEX 42\nEX 43\n"
"v_smfmac_f32_32x32x32_bf16 v[144:159], v[168:171], v[32:39], v108\nEX 44\nEX 45\nEX 46\nEX 47\n"
"v_mfma_f32_32x32x16_bf16 v[16:31], v[96:99], v[132:135], 0\nds_read_b128 v[100:103], v250 offset:33280\nds_read_b64 v[106:107], v221 offset:33280\n"
"ds_read_b128 v[160:163], v222 offset:33280\nds_read_b128 v[164:167], v111 offset:33280\nEX 0\nEX 1\nEX 2\nEX 3\n"
"v_mfma_f32_32x32x8bf16_1k v[16:31], v[104:105], v[138:139], v[16:31]\nEX 4\nEX 5\nEX 6\nEX 7\n"
"v_smfmac_f32_32x32x32_bf16 v[144:159], v[172:175], v[40:47], v108\nEX 8\nEX 9\nEX 10\nEX 11\n"
"v_smfmac_f32_32x32x32_bf16 v[48:63], v[176:179], v[0:7], v108\nEX 12\nEX 13\nEX 14\nEX 15\ns_waitcnt lgkmcnt(0)\n"
"v_mfma_f32_32x32x16_bf16 v[32:47], v[100:103], v[128:131], 0\nEX 16\nEX 17\nEX 18\nEX 19\n"
"v_mfma_f32_32x32x8bf16_1k v[32:47], v[106:107], v[136:137], v[32:47]\nEX 20\nEX 21\nEX 22\nEX 23\n"
"v_smfmac_f32_32x32x32_bf16 v[48:63], v[180:183], v[8:15], v108\nEX 24\nEX 25\nEX 26\nEX 27\n"
"v_smfmac_f32_32x32x32_bf16 v[144:159], v[176:179], v[16:23], v108\nEX 28\nEX 29\nEX 30\nEX 31\n"
"v_mfma_f32_32x32x16_bf16 v[0:15], v[100:103], v[132:135], 0\nds_read_b128 v[96:99], v250 offset:35840\nds_read_b64 v[104:105], v221 offset:35840\n"
"ds_read_b128 v[168:171], v222 offset:35840\nds_read_b128 v[172:175], v111 offset:35840\nEX 32\nEX 33\nEX 34\nEX 35\n"
"v_mfma_f32_32x32x8bf16_1k v[0:15], v[106:107], v[138:139], v[0:15]\nEX 36\nEX 37\nEX 38\nEX 39\n"
"v_smfmac_f32_32x32x32_bf16 v[144:159], v[180:183], v[24:31], v108\nEX 40\nEX 41\nEX 42\nEX 43\n"
"v_smfmac_f32_32x32x32_bf16 v[48:63], v[160:163], v[32:39], v108\nEX 44\nEX 45\nEX 46\nEX 47\ns_waitcnt lgkmcnt(0)\n"
"v_mfma_f32_32x32x16_bf16 v[16:31], v[96:99], v[128:131], 0\nEX 0\nEX 1\nEX 2\nEX 3\n"
"v_mfma_f32_32x32x8bf16_1k v[16:31], v[104:105], v[136:137], v[16:31]\nEX 4\nEX 5\nEX 6\nEX 7\n"
"v_smfmac_f32_32x32x32_bf16 v[48:63], v[164:167], v[40:47], v108\nEX 8\nEX 9\nEX 10\nEX 11\n"
"v_smfmac_f32_32x32x32_bf16 v[144:159], v[160:163], v[0:7], v108\nEX 12\nEX 13\nEX 14\nEX 15\ns_waitcnt vmcnt(0)\ns_barrier\ns_cmp_eq_u32 s14, 0\n"
"s_cbranch_scc1 L_pf_skip\nv_mov_b32 v64, s84\nv_mov_b32 v65, s85\nbuffer_store_dwordx2 v[64:65], v248, s[48:51], s10 offen sc1\nL_pf_skip:\n"
"v_mfma_f32_32x32x16_bf16 v[32:47], v[96:99], v[132:135], 0\nds_read_b128 v[100:103], v250 offset:38400\nds_read_b64 v[106:107], v221 offset:38400\n"
"ds_read_b128 v[176:179], v222 offset:38400\nds_read_b128 v[180:183], v111 offset:38400\nEX 16\nEX 17\nEX 18\nEX 19\n"
"v_mfma_f32_32x32x8bf16_1k v[32:47], v[104:105], v[138:139], v[32:47]\nEX 20\nEX 21\nEX 22\nEX 23\n"
"v_smfmac_f32_32x32x32_bf16 v[144:159], v[164:167], v[8:15], v108\nv_mov_b32 v88, v246\nv_mov_b32 v89, v247\nv_mov_b32 v90, 0\nEX 24\nEX 25\nEX 26\n"
"EX 27\nv_smfmac_f32_32x32x32_bf16 v[48:63], v[168:171], v[16:23], v108\nEX 28\nEX 29\nEX 30\nEX 31\ns_waitcnt lgkmcnt(0)\n"
"v_mfma_f32_32x32x16_bf16 v[0:15], v[100:103], v[128:131], 0\nEX 32\nEX 33\nEX 34\nEX 35\n"
"v_mfma_f32_32x32x8bf16_1k v[0:15], v[106:107], v[136:137], v[0:15]\nv_mov_b32 v91, 0\nv_mov_b32 v92, v246\nv_mov_b32 v93, v247\nEX 36\nEX 37\nEX 38\n"
"EX 39\nv_smfmac_f32_32x32x32_bf16 v[48:63], v[172:175], v[24:31], v108\nv_mov_b32 v94, v246\nv_mov_b32 v95, v247\nEX 40\nEX 41\nEX 42\nEX 43\n"
"v_smfmac_f32_32x32x32_bf16 v[144:159], v[168:171], v[32:39], v108\nEX 44\nEX 45\nEX 46\nEX 47\n"
"v_mfma_f32_32x32x16_bf16 v[16:31], v[100:103], v[132:135], 0\nds_read_b128 v[96:99], v250 offset:40960\nds_read_b64 v[104:105], v221 offset:40960\n"
"ds_read_b128 v[160:163], v222 offset:40960\nds_read_b128 v[164:167], v111 offset:40960\nEX 0\nEX 1\nEX 2\nEX 3\n"
"v_mfma_f32_32x32x8bf16_1k v[16:31], v[106:107], v[138:139], v[16:31]\nEX 4\nEX 5\nEX 6\nEX 7\n"
"v_smfmac_f32_32x32x32_bf16 v[144:159], v[172:175], v[40:47], v108\nEX 8\nEX 9\nEX 10\nEX 11\n"
"v_smfmac_f32_32x32x32_bf16 v[48:63], v[176:179], v[0:7], v108\nEX 12\nEX 13\nEX 14\nEX 15\ns_waitcnt lgkmcnt(0)\n"
"v_mfma_f32_32x32x16_bf16 v[32:47], v[96:99], v[128:131], 0\nEX 16\nEX 17\nEX 18\nEX 19\n"
"v_mfma_f32_32x32x8bf16_1k v[32:47], v[104:105], v[136:137], v[32:47]\nEX 20\nEX 21\nEX 22\nEX 23\n"
"v_smfmac_f32_32x32x32_bf16 v[48:63], v[180:183], v[8:15], v108\nEX 24\nEX 25\nEX 26\nEX 27\n"
"v_smfmac_f32_32x32x32_bf16 v[144:159], v[176:179], v[16:23], v108\nEX 28\nEX 29\nEX 30\nEX 31\n"
"v_mfma_f32_32x32x16_bf16 v[0:15], v[96:99], v[132:135], 0\nds_read_b128 v[100:103], v250 offset:43520\nds_read_b64 v[106:107], v221 offset:43520\n"
"ds_read_b128 v[168:171], v222 offset:43520\nds_read_b128 v[172:175], v111 offset:43520\nEX 32\nEX 33\nEX 34\nEX 35\n"
"v_mfma_f32_32x32x8bf16_1k v[0:15], v[104:105], v[138:139], v[0:15]\nEX 36\nEX 37\nEX 38\nEX 39\n"
"v_smfmac_f32_32x32x32_bf16 v[144:159], v[180:183], v[24:31], v108\nEX 40\nEX 41\nEX 42\nEX 43\n"
"v_smfmac_f32_32x32x32_bf16 v[48:63], v[160:163], v[32:39], v108\nEX 44\nEX 45\nEX 46\nEX 47\ns_waitcnt lgkmcnt(0)\n"
"v_mfma_f32_32x32x16_bf16 v[16:31], v[100:103], v[128:131], 0\nEX 0\nEX 1\nEX 2\nEX 3\n"
"v_mfma_f32_32x32x8bf16_1k v[16:31], v[106:107], v[136:137], v[16:31]\nEX 4\nEX 5\nEX 6\nEX 7\n"
"v_smfmac_f32_32x32x32_bf16 v[48:63], v[164:167], v[40:47], v108\nEX 8\nEX 9\nEX 10\nEX 11\n"
"v_smfmac_f32_32x32x32_bf16 v[144:159], v[160:163], v[0:7], v108\nEX 12\nEX 13\nEX 14\nEX 15\n"
"v_mfma_f32_32x32x16_bf16 v[32:47], v[100:103], v[132:135], 0\nds_read_b128 v[96:99], v250 offset:46080\nds_read_b64 v[104:105], v221 offset:46080\n"
"ds_read_b128 v[176:179], v222 offset:46080\nds_read_b128 v[180:183], v111 offset:46080\nEX 16\nEX 17\nEX 18\nEX 19\n"
"v_mfma_f32_32x32x8bf16_1k v[32:47], v[106:107], v[138:139], v[32:47]\nEX 20\nEX 21\nEX 22\nEX 23\n"
"v_smfmac_f32_32x32x32_bf16 v[144:159], v[164:167], v[8:15], v108\nEX 24\nEX 25\nEX 26\nEX 27\n"
"v_smfmac_f32_32x32x32_bf16 v[48:63], v[168:171], v[16:23], v108\nEX 28\nEX 29\nEX 30\nEX 31\ns_waitcnt lgkmcnt(0)\n"
"v_mfma_f32_32x32x16_bf16 v[0:15], v[96:99], v[128:131], 0\nEX 32\nEX 33\nEX 34\nEX 35\n"
"v_mfma_f32_32x32x8bf16_1k v[0:15], v[104:105], v[136:137], v[0:15]\nEX 36\nEX 37\nEX 38\nEX 39\n"
"v_smfmac_f32_32x32x32_bf16 v[48:63], v[172:175], v[24:31], v108\nEX 40\nEX 41\nEX 42\nEX 43\n"
"v_smfmac_f32_32x32x32_bf16 v[144:159], v[168:171], v[32:39], v108\nEX 44\nEX 45\nEX 46\nEX 47\n"
"v_mfma_f32_32x32x16_bf16 v[16:31], v[96:99], v[132:135], 0\nds_read_b128 v[100:103], v250 offset:48640\nds_read_b64 v[106:107], v221 offset:48640\n"
"ds_read_b128 v[160:163], v222 offset:48640\nds_read_b128 v[164:167], v111 offset:48640\nEX 0\nEX 1\nEX 2\nEX 3\n"
"v_mfma_f32_32x32x8bf16_1k v[16:31], v[104:105], v[138:139], v[16:31]\nEX 4\nEX 5\nEX 6\nEX 7\n"
"v_smfmac_f32_32x32x32_bf16 v[144:159], v[172:175], v[40:47], v108\nbuffer_load_dwordx4 v[64:67], v244, s[48:51], 0 offen sc1\nEX 8\nEX 9\nEX 10\n"
"EX 11\nv_smfmac_f32_32x32x32_bf16 v[48:63], v[176:179], v[0:7], v108\nEX 12\nEX 13\nEX 14\nEX 15\ns_waitcnt lgkmcnt(0)\n"
"v_mfma_f32_32x32x16_bf16 v[32:47], v[100:103], v[128:131], 0\nEX 16\nEX 17\nEX 18\nEX 19\n"
"v_mfma_f32_32x32x8bf16_1k v[32:47], v[106:107], v[136:137], v[32:47]\nEX 20\nEX 21\nEX 22\nEX 23\n"
"v_smfmac_f32_32x32x32_bf16 v[48:63], v[180:183], v[8:15], v108\nEX 24\nEX 25\nEX 26\nEX 27\n"
"v_smfmac_f32_32x32x32_bf16 v[144:159], v[176:179], v[16:23], v108\nEX 28\nEX 29\nEX 30\nEX 31\n"
"v_mfma_f32_32x32x16_bf16 v[0:15], v[100:103], v[132:135], 0\nds_read_b128 v[96:99], v250 offset:51200\nds_read_b64 v[104:105], v221 offset:51200\n"
"ds_read_b128 v[168:171], v222 offset:51200\nds_read_b128 v[172:175], v111 offset:51200\nEX 32\nEX 33\nEX 34\nEX 35\n"
"v_mfma_f32_32x32x8bf16_1k v[0:15], v[106:107], v[138:139], v[0:15]\nEX 36\nEX 37\nEX 38\nEX 39\n"
"v_smfmac_f32_32x32x32_bf16 v[144:159], v[180:183], v[24:31], v108\nEX 40\nEX 41\nEX 42\nEX 43\n"
"v_smfmac_f32_32x32x32_bf16 v[48:63], v[160:163], v[32:39], v108\nEX 44\nEX 45\nEX 46\nEX 47\ns_waitcnt lgkmcnt(0)\n"
"v_mfma_f32_32x32x16_bf16 v[16:31], v[96:99], v[128:131], 0\nEX 0\nEX 1\nEX 2\nEX 3\n"
"v_mfma_f32_32x32x8bf16_1k v[16:31], v[104:105], v[136:137], v[16:31]\nEX 4\nEX 5\nEX 6\nEX 7\n"
"v_smfmac_f32_32x32x32_bf16 v[48:63], v[164:167], v[40:47], v108\nEX 8\nEX 9\nEX 10\nEX 11\n"
"v_smfmac_f32_32x32x32_bf16 v[144:159], v[160:163], v[0:7], v108\nEX 12\nEX 13\nEX 14\nEX 15\n"
"v_mfma_f32_32x32x16_bf16 v[32:47], v[96:99], v[132:135], 0\nds_read_b128 v[100:103], v250 offset:53760\nds_read_b64 v[106:107], v221 offset:53760\n"
"ds_read_b128 v[176:179], v222 offset:53760\nds_read_b128 v[180:183], v111 offset:53760\nEX 16\nEX 17\nEX 18\nEX 19\n"
"v_mfma_f32_32x32x8bf16_1k v[32:47], v[104:105], v[138:139], v[32:47]\nEX 20\nEX 21\nEX 22\nEX 23\n"
"v_smfmac_f32_32x32x32_bf16 v[144:159], v[164:167], v[8:15], v108\nEX 24\nEX 25\nEX 26\nEX 27\n"
"v_smfmac_f32_32x32x32_bf16 v[48:63], v[168:171], v[16:23], v108\nEX 28\nEX 29\nEX 30\nEX 31\ns_waitcnt lgkmcnt(0)\n"
"v_mfma_f32_32x32x16_bf16 v[0:15], v[100:103], v[128:131], 0\nEX 32\nEX 33\nEX 34\nEX 35\n"
"v_mfma_f32_32x32x8bf16_1k v[0:15], v[106:107], v[136:137], v[0:15]\nEX 36\nEX 37\nEX 38\nEX 39\n"
"v_smfmac_f32_32x32x32_bf16 v[48:63], v[172:175], v[24:31], v108\nEX 40\nEX 41\nEX 42\nEX 43\n"
"v_smfmac_f32_32x32x32_bf16 v[144:159], v[168:171], v[32:39], v108\nEX 44\nEX 45\nEX 46\nEX 47\n"
"v_mfma_f32_32x32x16_bf16 v[16:31], v[100:103], v[132:135], 0\nds_read_b128 v[96:99], v250 offset:56320\nds_read_b64 v[104:105], v221 offset:56320\n"
"ds_read_b128 v[160:163], v222 offset:56320\nds_read_b128 v[164:167], v111 offset:56320\nEX 0\nEX 1\nEX 2\nEX 3\n"
"v_mfma_f32_32x32x8bf16_1k v[16:31], v[106:107], v[138:139], v[16:31]\nEX 4\nEX 5\nEX 6\nEX 7\n"
"v_smfmac_f32_32x32x32_bf16 v[144:159], v[172:175], v[40:47], v108\ns_waitcnt vmcnt(0)\nv_xor_b32 v64, s84, v64\nv_xor_b32 v65, s85, v65\n"
"v_xor_b32 v66, s84, v66\nv_xor_b32 v67, s85, v67\nv_or_b32 v64, v64, v65\nv_or_b32 v64, v64, v66\nv_or_b32 v64, v64, v67\nv_cmp_ne_u32 vcc, 0, v64\n"
"s_cbranch_vccnz L_pfl_spin\nL_pfl_ok:\nEX 8\nEX 9\nEX 10\nEX 11\nv_smfmac_f32_32x32x32_bf16 v[48:63], v[176:179], v[0:7], v108\nEX 12\nEX 13\nEX 14\n"
"EX 15\ns_waitcnt lgkmcnt(0)\nv_mfma_f32_32x32x16_bf16 v[32:47], v[96:99], v[128:131], 0\nEX 16\nEX 17\nEX 18\nEX 19\n"
"v_mfma_f32_32x32x8bf16_1k v[32:47], v[104:105], v[136:137], v[32:47]\ns_mov_b32 s21, 0\nbuffer_load_dwordx4 a[0:3], v226, s[48:51], s21 offen sc1\n"
"buffer_load_dwordx4 a[4:7], v226, s[48:51], s21 offen offset:1024 sc1\nbuffer_load_dwordx2 a[128:129], v227, s[48:51], s21 offen sc1\nEX 20\nEX 21\n"
"EX 22\nEX 23\nv_smfmac_f32_32x32x32_bf16 v[48:63], v[180:183], v[8:15], v108\nEX 24\nEX 25\nEX 26\nEX 27\n"
"v_smfmac_f32_32x32x32_bf16 v[144:159], v[176:179], v[16:23], v108\nEX 28\nEX 29\nEX 30\nEX 31\n"
"v_mfma_f32_32x32x16_bf16 v[0:15], v[96:99], v[132:135], 0\nds_read_b128 v[100:103], v250 offset:58880\nds_read_b64 v[106:107], v221 offset:58880\n"
"ds_read_b128 v[168:171], v222 offset:58880\nds_read_b128 v[172:175], v111 offset:58880\nEX 32\nEX 33\nEX 34\nEX 35\n"
"v_mfma_f32_32x32x8bf16_1k v[0:15], v[104:105], v[138:139], v[0:15]\nEX 36\nEX 37\nEX 38\nEX 39\n"
"v_smfmac_f32_32x32x32_bf16 v[144:159], v[180:183], v[24:31], v108\nEX 40\nEX 41\nEX 42\nEX 43\n"
"v_smfmac_f32_32x32x32_bf16 v[48:63], v[160:163], v[32:39], v108\nEX 44\nEX 45\nEX 46\nEX 47\ns_waitcnt lgkmcnt(0)\n"
"v_mfma_f32_32x32x16_bf16 v[16:31], v[100:103], v[128:131], 0\nEX 0\nEX 1\nEX 2\nEX 3\n"
"v_mfma_f32_32x32x8bf16_1k v[16:31], v[106:107], v[136:137], v[16:31]\nEX 4\nEX 5\nEX 6\nEX 7\n"
"v_smfmac_f32_32x32x32_bf16 v[48:63], v[164:167], v[40:47], v108\nEX 8\nEX 9\nEX 10\nEX 11\n"
"v_smfmac_f32_32x32x32_bf16 v[144:159], v[160:163], v[0:7], v108\nEX 12\nEX 13\nEX 14\nEX 15\n"
"v_mfma_f32_32x32x16_bf16 v[32:47], v[100:103], v[132:135], 0\nds_read_b128 v[96:99], v250 offset:61440\nds_read_b64 v[104:105], v221 offset:61440\n"
"ds_read_b128 v[176:179], v222 offset:61440\nds_read_b128 v[180:183], v111 offset:61440\nEX 16\nEX 17\nEX 18\nEX 19\n"
"v_mfma_f32_32x32x8bf16_1k v[32:47], v[106:107], v[138:139], v[32:47]\nEX 20\nEX 21\nEX 22\nEX 23\n"
"v_smfmac_f32_32x32x32_bf16 v[144:159], v[164:167], v[8:15], v108\ns_mov_b32 s21, 0x24000\nbuffer_load_dwordx4 a[8:11], v226, s[48:51], s21 offen sc1\n"
"buffer_load_dwordx4 a[12:15], v226, s[48:51], s21 offen offset:1024 sc1\nbuffer_load_dwordx2 a[130:131], v227, s[48:51], s21 offen sc1\nEX 24\nEX 25\n"
"EX 26\nEX 27\nv_smfmac_f32_32x32x32_bf16 v[48:63], v[168:171], v[16:23], v108\nEX 28\nEX 29\nEX 30\nEX 31\ns_waitcnt lgkmcnt(0)\n"
"v_mfma_f32_32x32x16_bf16 v[0:15], v[96:99], v[128:131], 0\nEX 32\nEX 33\nEX 34\nEX 35\n"
"v_mfma_f32_32x32x8bf16_1k v[0:15], v[104:105], v[136:137], v[0:15]\nEX 36\nEX 37\nEX 38\nEX 39\n"
"v_smfmac_f32_32x32x32_bf16 v[48:63], v[172:175], v[24:31], v108\nEX 40\nEX 41\nEX 42\nEX 43\n"
"v_smfmac_f32_32x32x32_bf16 v[144:159], v[168:171], v[32:39], v108\nEX 44\nEX 45\nEX 46\nEX 47\n"
"v_mfma_f32_32x32x16_bf16 v[16:31], v[96:99], v[132:135], 0\nv_add_u32 v250, 0xfa00, v250\nv_add_u32 v221, 0xfa00, v221\nv_add_u32 v222, 0xfa00, v222\n"
"v_add_u32 v111, 0xfa00, v111\nds_read_b128 v[100:103], v250\nds_read_b64 v[106:107], v221\nds_read_b128 v[160:163], v222\n"
"ds_read_b128 v[164:167], v111\nEX 0\nEX 1\nEX 2\nEX 3\nv_mfma_f32_32x32x8bf16_1k v[16:31], v[104:105], v[138:139], v[16:31]\nEX 4\nEX 5\nEX 6\nEX 7\n"
"v_smfmac_f32_32x32x32_bf16 v[144:159], v[172:175], v[40:47], v108\nEX 8\nEX 9\nEX 10\nEX 11\n"
"v_smfmac_f32_32x32x32_bf16 v[48:63], v[176:179], v[0:7], v108\nEX 12\nEX 13\nEX 14\nEX 15\ns_waitcnt lgkmcnt(0)\n"
"v_mfma_f32_32x32x16_bf16 v[32:47], v[100:103], v[128:131], 0\nEX 16\nEX 17\nEX 18\nEX 19\n"
"v_mfma_f32_32x32x8bf16_1k v[32:47], v[106:107], v[136:137], v[32:47]\nEX 20\nEX 21\nEX 22\nEX 23\n"
"v_smfmac_f32_32x32x32_bf16 v[48:63], v[180:183], v[8:15], v108\nEX 24\nEX 25\nEX 26\nEX 27\n"
"v_smfmac_f32_32x32x32_bf16 v[144:159], v[176:179], v[16:23], v108\nEX 28\nEX 29\nEX 30\nEX 31\n"
"v_mfma_f32_32x32x16_bf16 v[0:15], v[100:103], v[132:135], 0\nds_read_b128 v[96:99], v250 offset:2560\nds_read_b64 v[104:105], v221 offset:2560\n"
"ds_read_b128 v[168:171], v222 offset:2560\nds_read_b128 v[172:175], v111 offset:2560\nEX 32\nEX 33\nEX 34\nEX 35\n"
"v_mfma_f32_32x32x8bf16_1k v[0:15], v[106:107], v[138:139], v[0:15]\nEX 36\nEX 37\nEX 38\nEX 39\n"
"v_smfmac_f32_32x32x32_bf16 v[144:159], v[180:183], v[24:31], v108\ns_mov_b32 s21, 0x48000\n"
"buffer_load_dwordx4 a[16:19], v226, s[48:51], s21 offen sc1\nbuffer_load_dwordx4 a[20:23], v226, s[48:51], s21 offen offset:1024 sc1\n"
"buffer_load_dwordx2 a[132:133], v227, s[48:51], s21 offen sc1\nEX 40\nEX 41\nEX 42\nEX 43\n"
"v_smfmac_f32_32x32x32_bf16 v[48:63], v[160:163], v[32:39], v108\nEX 44\nEX 45\nEX 46\nEX 47\ns_waitcnt lgkmcnt(0)\n"
"v_mfma_f32_32x32x16_bf16 v[16:31], v[96:99], v[128:131], 0\nEX 0\nEX 1\nEX 2\nEX 3\n"
"v_mfma_f32_32x32x8bf16_1k v[16:31], v[104:105], v[136:137], v[16:31]\nEX 4\nEX 5\nEX 6\nEX 7\n"
"v_smfmac_f32_32x32x32_bf16 v[48:63], v[164:167], v[40:47], v108\nEX 8\nEX 9\nEX 10\nEX 11\n"
"v_smfmac_f32_32x32x32_bf16 v[144:159], v[160:163], v[0:7], v108\nEX 12\nEX 13\nEX 14\nEX 15\n"
"v_mfma_f32_32x32x16_bf16 v[32:47], v[96:99], v[132:135], 0\nds_read_b128 v[100:103], v250 offset:5120\nds_read_b64 v[106:107], v221 offset:5120\n"
"ds_read_b128 v[176:179], v222 offset:5120\nds_read_b128 v[180:183], v111 offset:5120\nEX 16\nEX 17\nEX 18\nEX 19\n"
"v_mfma_f32_32x32x8bf16_1k v[32:47], v[104:105], v[138:139], v[32:47]\nEX 20\nEX 21\nEX 22\nEX 23\n"
"v_smfmac_f32_32x32x32_bf16 v[144:159], v[164:167], v[8:15], v108\nEX 24\nEX 25\nEX 26\nEX 27\n"
"v_smfmac_f32_32x32x32_bf16 v[48:63], v[168:171], v[16:23], v108\nEX 28\nEX 29\nEX 30\nEX 31\ns_waitcnt lgkmcnt(0)\n"
"v_mfma_f32_32x32x16_bf16 v[0:15], v[100:103], v[128:131], 0\nEX 32\nEX 33\nEX 34\nEX 35\n"
"v_mfma_f32_32x32x8bf16_1k v[0:15], v[106:107], v[136:137], v[0:15]\nEX 36\nEX 37\nEX 38\nEX 39\n"
"v_smfmac_f32_32x32x32_bf16 v[48:63], v[172:175], v[24:31], v108\nEX 40\nEX 41\nEX 42\nEX 43\n"
"v_smfmac_f32_32x32x32_bf16 v[144:159], v[168:171], v[32:39], v108\nEX 44\nEX 45\nEX 46\nEX 47\n"
"v_mfma_f32_32x32x16_bf16 v[16:31], v[100:103], v[132:135], 0\nds_read_b128 v[96:99], v250 offset:7680\nds_read_b64 v[104:105], v221 offset:7680\n"
"ds_read_b128 v[160:163], v222 offset:7680\nds_read_b128 v[164:167], v111 offset:7680\nEX 0\nEX 1\nEX 2\nEX 3\n"
"v_mfma_f32_32x32x8bf16_1k v[16:31], v[106:107], v[138:139], v[16:31]\nEX 4\nEX 5\nEX 6\nEX 7\n"
"v_smfmac_f32_32x32x32_bf16 v[144:159], v[172:175], v[40:47], v108\ns_mov_b32 s21, 0x6c000\n"
"buffer_load_dwordx4 a[24:27], v226, s[48:51], s21 offen sc1\nbuffer_load_dwordx4 a[28:31], v226, s[48:51], s21 offen offset:1024 sc1\n"
"buffer_load_dwordx2 a[134:135], v227, s[48:51], s21 offen sc1\nEX 8\nEX 9\nEX 10\nEX 11\n"
"v_smfmac_f32_32x32x32_bf16 v[48:63], v[176:179], v[0:7], v108\nEX 12\nEX 13\nEX 14\nEX 15\ns_waitcnt lgkmcnt(0)\n"
"v_mfma_f32_32x32x16_bf16 v[32:47], v[96:99], v[128:131], 0\nEX 16\nEX 17\nEX 18\nEX 19\n"
"v_mfma_f32_32x32x8bf16_1k v[32:47], v[104:105], v[136:137], v[32:47]\nEX 20\nEX 21\nEX 22\nEX 23\n"
"v_smfmac_f32_32x32x32_bf16 v[48:63], v[180:183], v[8:15], v108\nEX 24\nEX 25\nEX 26\nEX 27\n"
"v_smfmac_f32_32x32x32_bf16 v[144:159], v[176:179], v[16:23], v108\nEX 28\nEX 29\nEX 30\nEX 31\n"
"v_mfma_f32_32x32x16_bf16 v[0:15], v[96:99], v[132:135], 0\nds_read_b128 v[100:103], v250 offset:10240\nds_read_b64 v[106:107], v221 offset:10240\n"
"ds_read_b128 v[168:171], v222 offset:10240\nds_read_b128 v[172:175], v111 offset:10240\nEX 32\nEX 33\nEX 34\nEX 35\n"
"v_mfma_f32_32x32x8bf16_1k v[0:15], v[104:105], v[138:139], v[0:15]\nEX 36\nEX 37\nEX 38\nEX 39\n"
"v_smfmac_f32_32x32x32_bf16 v[144:159], v[180:183], v[24:31], v108\ns_cmp_eq_u32 s15, 0\ns_cbranch_scc1 L_bump1\n"
"buffer_load_dwordx4 v[68:71], v251, s[48:51], 0 offen sc1\nbuffer_load_dwordx4 v[72:75], v251, s[48:51], 0 offen offset:16 sc1\nL_bump1:\nEX 40\n"
"EX 41\nEX 42\nEX 43\nv_smfmac_f32_32x32x32_bf16 v[48:63], v[160:163], v[32:39], v108\nEX 44\nEX 45\nEX 46\nEX 47\ns_waitcnt lgkmcnt(0)\n"
"v_mfma_f32_32x32x16_bf16 v[16:31], v[100:103], v[128:131], 0\nEX 0\nEX 1\nEX 2\nEX 3\n"
"v_mfma_f32_32x32x8bf16_1k v[16:31], v[106:107], v[136:137], v[16:31]\nEX 4\nEX 5\nEX 6\nEX 7\n"
"v_smfmac_f32_32x32x32_bf16 v[48:63], v[164:167], v[40:47], v108\nEX 8\nEX 9\nEX 10\nEX 11\n"
"v_smfmac_f32_32x32x32_bf16 v[144:159], v[160:163], v[0:7], v108\nEX 12\nEX 13\nEX 14\nEX 15\ns_barrier\n"
"v_mfma_f32_32x32x16_bf16 v[32:47], v[100:103], v[132:135], 0\nv_add_u32 v250, 0xfffe0818, v250\nv_add_u32 v221, 0xfffe0818, v221\n"
"v_add_u32 v222, 0xfffe0818, v222\nv_add_u32 v111, 0xfffe0818, v111\nds_read_b128 v[96:99], v250 offset:62440\n"
"ds_read_b64 v[104:105], v221 offset:62440\nds_read_b128 v[176:179], v222 offset:62440\nds_read_b128 v[180:183], v111 offset:62440\nEX 16\nEX 17\n"
"EX 18\nEX 19\nv_mfma_f32_32x32x8bf16_1k v[32:47], v[106:107], v[138:139], v[32:47]\nEX 20\nEX 21\nEX 22\nEX 23\n"
"v_smfmac_f32_32x32x32_bf16 v[144:159], v[164:167], v[8:15], v108\ns_mov_b32 s21, 0x90000\n"
"buffer_load_dwordx4 a[32:35], v226, s[48:51], s21 offen sc1\nbuffer_load_dwordx4 a[36:39], v226, s[48:51], s21 offen offset:1024 sc1\n"
"buffer_load_dwordx2 a[136:137], v227, s[48:51], s21 offen sc1\nEX 24\nEX 25\nEX 26\nEX 27\n"
"v_smfmac_f32_32x32x32_bf16 v[48:63], v[168:171], v[16:23], v108\nEX 28\nEX 29\nEX 30\nEX 31\ns_waitcnt lgkmcnt(0)\n"
"v_mfma_f32_32x32x16_bf16 v[0:15], v[96:99], v[128:131], 0\nEX 32\nEX 33\nEX 34\nEX 35\n"
"v_mfma_f32_32x32x8bf16_1k v[0:15], v[104:105], v[136:137], v[0:15]\nEX 36\nEX 37\nEX 38\nEX 39\n"
"v_smfmac_f32_32x32x32_bf16 v[48:63], v[172:175], v[24:31], v108\nEX 40\nEX 41\nEX 42\nEX 43\n"
"v_smfmac_f32_32x32x32_bf16 v[144:159], v[168:171], v[32:39], v108\nEX 44\nEX 45\nEX 46\nEX 47\n"
"v_mfma_f32_32x32x16_bf16 v[16:31], v[96:99], v[132:135], 0\nds_read_b128 v[100:103], v250 offset:59880\nds_read_b64 v[106:107], v221 offset:59880\n"
"ds_read_b128 v[160:163], v222 offset:59880\nds_read_b128 v[164:167], v111 offset:59880\nEX 0\nEX 1\nEX 2\nEX 3\n"
"v_mfma_f32_32x32x8bf16_1k v[16:31], v[104:105], v[138:139], v[16:31]\nEX 4\nEX 5\nEX 6\nEX 7\n"
"v_smfmac_f32_32x32x32_bf16 v[144:159], v[172:175], v[40:47], v108\ns_waitcnt vmcnt(12)\nv_mfma_f32_16x16x32_bf16 v[112:115], v[88:91], a[0:3], 0\n"
"EX 8\nEX 9\nEX 10\nEX 11\nv_smfmac_f32_32x32x32_bf16 v[48:63], v[176:179], v[0:7], v108\nEX 12\nEX 13\nEX 14\nEX 15\ns_waitcnt lgkmcnt(0)\n"
"v_mfma_f32_32x32x16_bf16 v[32:47], v[100:103], v[128:131], 0\nEX 16\nEX 17\nEX 18\nEX 19\n"
"v_mfma_f32_32x32x8bf16_1k v[32:47], v[106:107], v[136:137], v[32:47]\nv_mfma_f32_16x16x32_bf16 v[116:119], v[90:93], a[0:3], 0\n"
"v_mfma_f32_16x16x32_bf16 v[120:123], v[88:91], a[4:7], 0\nEX 20\nEX 21\nEX 22\nEX 23\nv_smfmac_f32_32x32x32_bf16 v[48:63], v[180:183], v[8:15], v108\n"
"v_mfma_f32_16x16x32_bf16 v[124:127], v[90:93], a[4:7], 0\nEX 24\nEX 25\nEX 26\nEX 27\n"
"v_smfmac_f32_32x32x32_bf16 v[144:159], v[176:179], v[16:23], v108\nEX 28\nEX 29\nEX 30\nEX 31\n"
"v_mfma_f32_32x32x16_bf16 v[0:15], v[100:103], v[132:135], 0\nds_read_b128 v[96:99], v250 offset:57320\nds_read_b64 v[104:105], v221 offset:57320\n"
"ds_read_b128 v[168:171], v222 offset:57320\nds_read_b128 v[172:175], v111 offset:57320\nEX 32\nEX 33\nEX 34\nEX 35\n"
"v_mfma_f32_32x32x8bf16_1k v[0:15], v[106:107], v[138:139], v[0:15]\nEX 36\nEX 37\nEX 38\nEX 39\n"
"v_smfmac_f32_32x32x32_bf16 v[144:159], v[180:183], v[24:31], v108\ns_mov_b32 s21, 0xb4000\n"
"buffer_load_dwordx4 a[40:43], v226, s[48:51], s21 offen sc1\nbuffer_load_dwordx4 a[44:47], v226, s[48:51], s21 offen offset:1024 sc1\n"
"buffer_load_dwordx2 a[138:139], v227, s[48:51], s21 offen sc1\nEX 40\nEX 41\nEX 42\nEX 43\n"
"v_smfmac_f32_32x32x32_bf16 v[48:63], v[160:163], v[32:39], v108\nEX 44\nEX 45\nEX 46\nEX 47\ns_waitcnt lgkmcnt(0)\ns_getpc_b64 s[22:23]\nL_pc4:\n"
"v_add_u32 v143, 0x2000, v109\nv_min_u32 v143, L_end-L_pc4-4, v143\nglobal_load_dword v253, v143, s[22:23]\n"
"v_mfma_f32_32x32x16_bf16 v[16:31], v[96:99], v[128:131], 0\nEX 0\nEX 1\nEX 2\nEX 3\n"
"v_mfma_f32_32x32x8bf16_1k v[16:31], v[104:105], v[136:137], v[16:31]\nEX 4\nEX 5\nEX 6\nEX 7\n"
"v_smfmac_f32_32x32x32_bf16 v[48:63], v[164:167], v[40:47], v108\nEX 8\nEX 9\nEX 10\nEX 11\n"
"v_smfmac_f32_32x32x32_bf16 v[144:159], v[160:163], v[0:7], v108\nEX 12\nEX 13\nEX 14\nEX 15\n"
"v_mfma_f32_32x32x16_bf16 v[32:47], v[96:99], v[132:135], 0\nds_read_b128 v[100:103], v250 offset:54760\nds_read_b64 v[106:107], v221 offset:54760\n"
"ds_read_b128 v[176:179], v222 offset:54760\nds_read_b128 v[180:183], v111 offset:54760\nEX 16\nEX 17\nEX 18\nEX 19\n"
"v_mfma_f32_32x32x8bf16_1k v[32:47], v[104:105], v[138:139], v[32:47]\nEX 20\nEX 21\nEX 22\nEX 23\n"
"v_smfmac_f32_32x32x32_bf16 v[144:159], v[164:167], v[8:15], v108\ns_waitcnt vmcnt(13)\nMA 112,88,8\nEX 24\nEX 25\nEX 26\nEX 27\n"
"v_smfmac_f32_32x32x32_bf16 v[48:63], v[168:171], v[16:23], v108\nEX 28\nEX 29\nEX 30\nEX 31\ns_waitcnt lgkmcnt(0)\n"
"v_mfma_f32_32x32x16_bf16 v[0:15], v[100:103], v[128:131], 0\nEX 32\nEX 33\nEX 34\nEX 35\n"
"v_mfma_f32_32x32x8bf16_1k v[0:15], v[106:107], v[136:137], v[0:15]\nMA 116,90,8\nMA 120,88,12\nEX 36\nEX 37\nEX 38\nEX 39\n"
"v_smfmac_f32_32x32x32_bf16 v[48:63], v[172:175], v[24:31], v108\nMA 124,90,12\nv_mfma_f32_16x16x32_bf16 v[184:187], v[92:95], a[128:131], 0\nEX 40\n"
"EX 41\nEX 42\nEX 43\nv_smfmac_f32_32x32x32_bf16 v[144:159], v[168:171], v[32:39], v108\nEX 44\nEX 45\nEX 46\nEX 47\n"
"v_mfma_f32_32x32x16_bf16 v[16:31], v[100:103], v[132:135], 0\nds_read_b128 v[96:99], v250 offset:52200\nds_read_b64 v[104:105], v221 offset:52200\n"
"ds_read_b128 v[160:163], v222 offset:52200\nds_read_b128 v[164:167], v111 offset:52200\nEX 0\nEX 1\nEX 2\nEX 3\n"
"v_mfma_f32_32x32x8bf16_1k v[16:31], v[106:107], v[138:139], v[16:31]\nEX 4\nEX 5\nEX 6\nEX 7\n"
"v_smfmac_f32_32x32x32_bf16 v[144:159], v[172:175], v[40:47], v108\ns_mov_b32 s21, 0xd8000\n"
"buffer_load_dwordx4 a[48:51], v226, s[48:51], s21 offen sc1\nbuffer_load_dwordx4 a[52:55], v226, s[48:51], s21 offen offset:1024 sc1\n"
"buffer_load_dwordx2 a[140:141], v227, s[48:51], s21 offen sc1\nEX 8\nEX 9\nEX 10\nEX 11\n"
"v_smfmac_f32_32x32x32_bf16 v[48:63], v[176:179], v[0:7], v108\nEX 12\nEX 13\nEX 14\nEX 15\ns_waitcnt lgkmcnt(0)\n"
"v_mfma_f32_32x32x16_bf16 v[32:47], v[96:99], v[128:131], 0\nEX 16\nEX 17\nEX 18\nEX 19\n"
"v_mfma_f32_32x32x8bf16_1k v[32:47], v[104:105], v[136:137], v[32:47]\nEX 20\nEX 21\nEX 22\nEX 23\n"
"v_smfmac_f32_32x32x32_bf16 v[48:63], v[180:183], v[8:15], v108\nEX 24\nEX 25\nEX 26\nEX 27\n"
"v_smfmac_f32_32x32x32_bf16 v[144:159], v[176:179], v[16:23], v108\nEX 28\nEX 29\nEX 30\nEX 31\n"
"v_mfma_f32_32x32x16_bf16 v[0:15], v[96:99], v[132:135], 0\nds_read_b128 v[100:103], v250 offset:49640\nds_read_b64 v[106:107], v221 offset:49640\n"
"ds_read_b128 v[168:171], v222 offset:49640\nds_read_b128 v[172:175], v111 offset:49640\nEX 32\nEX 33\nEX 34\nEX 35\n"
"v_mfma_f32_32x32x8bf16_1k v[0:15], v[104:105], v[138:139], v[0:15]\nEX 36\nEX 37\nEX 38\nEX 39\n"
"v_smfmac_f32_32x32x32_bf16 v[144:159], v[180:183], v[24:31], v108\ns_waitcnt vmcnt(13)\nMA 112,88,16\nEX 40\nEX 41\nEX 42\nEX 43\n"
"v_smfmac_f32_32x32x32_bf16 v[48:63], v[160:163], v[32:39], v108\nEX 44\nEX 45\nEX 46\nEX 47\ns_waitcnt lgkmcnt(0)\n"
"v_mfma_f32_32x32x16_bf16 v[16:31], v[100:103], v[128:131], 0\nEX 0\nEX 1\nEX 2\nEX 3\n"
"v_mfma_f32_32x32x8bf16_1k v[16:31], v[106:107], v[136:137], v[16:31]\nMA 116,90,16\nMA 120,88,20\nEX 4\nEX 5\nEX 6\nEX 7\n"
"v_smfmac_f32_32x32x32_bf16 v[48:63], v[164:167], v[40:47], v108\nMA 124,90,20\nEX 8\nEX 9\nEX 10\nEX 11\n"
"v_smfmac_f32_32x32x32_bf16 v[144:159], v[160:163], v[0:7], v108\nEX 12\nEX 13\nEX 14\nEX 15\n"
"v_mfma_f32_32x32x16_bf16 v[32:47], v[100:103], v[132:135], 0\nds_read_b128 v[96:99], v250 offset:47080\nds_read_b64 v[104:105], v221 offset:47080\n"
"ds_read_b128 v[176:179], v222 offset:47080\nds_read_b128 v[180:183], v111 offset:47080\nEX 16\nEX 17\nEX 18\nEX 19\n"
"v_mfma_f32_32x32x8bf16_1k v[32:47], v[106:107], v[138:139], v[32:47]\nEX 20\nEX 21\nEX 22\nEX 23\n"
"v_smfmac_f32_32x32x32_bf16 v[144:159], v[164:167], v[8:15], v108\ns_mov_b32 s21, 0xfc000\n"
"buffer_load_dwordx4 a[56:59], v226, s[48:51], s21 offen sc1\nbuffer_load_dwordx4 a[60:63], v226, s[48:51], s21 offen offset:1024 sc1\n"
"buffer_load_dwordx2 a[142:143], v227, s[48:51], s21 offen sc1\nEX 24\nEX 25\nEX 26\nEX 27\n"
"v_smfmac_f32_32x32x32_bf16 v[48:63], v[168:171], v[16:23], v108\nEX 28\nEX 29\nEX 30\nEX 31\ns_waitcnt lgkmcnt(0)\n"
"v_mfma_f32_32x32x16_bf16 v[0:15], v[96:99], v[128:131], 0\nEX 32\nEX 33\nEX 34\nEX 35\n"
"v_mfma_f32_32x32x8bf16_1k v[0:15], v[104:105], v[136:137], v[0:15]\nEX 36\nEX 37\nEX 38\nEX 39\n"
"v_smfmac_f32_32x32x32_bf16 v[48:63], v[172:175], v[24:31], v108\nEX 40\nEX 41\nEX 42\nEX 43\n"
"v_smfmac_f32_32x32x32_bf16 v[144:159], v[168:171], v[32:39], v108\nEX 44\nEX 45\nEX 46\nEX 47\n"
"v_mfma_f32_32x32x16_bf16 v[16:31], v[96:99], v[132:135], 0\nds_read_b128 v[100:103], v250 offset:44520\nds_read_b64 v[106:107], v221 offset:44520\n"
"ds_read_b128 v[160:163], v222 offset:44520\nds_read_b128 v[164:167], v111 offset:44520\nEX 0\nEX 1\nEX 2\nEX 3\n"
"v_mfma_f32_32x32x8bf16_1k v[16:31], v[104:105], v[138:139], v[16:31]\nEX 4\nEX 5\nEX 6\nEX 7\n"
"v_smfmac_f32_32x32x32_bf16 v[144:159], v[172:175], v[40:47], v108\ns_cmp_eq_u32 s15, 0\ns_cbranch_scc1 L_bump2\ns_waitcnt vmcnt(13)\n"
"v_xor_b32 v68, s84, v68\nv_xor_b32 v69, s85, v69\nv_xor_b32 v70, s84, v70\nv_xor_b32 v71, s85, v71\nv_xor_b32 v72, s84, v72\nv_xor_b32 v73, s85, v73\n"
"v_xor_b32 v74, s84, v74\nv_xor_b32 v75, s85, v75\nv_or_b32 v68, v68, v69\nv_or_b32 v68, v68, v70\nv_or_b32 v68, v68, v71\nv_or_b32 v68, v68, v72\n"
"v_or_b32 v68, v68, v73\nv_or_b32 v68, v68, v74\nv_or_b32 v68, v68, v75\nv_cmp_ne_u32 vcc, 0, v68\ns_cbranch_vccnz L_arrv_spin\nL_arrv_ok:\n"
"v_mov_b32 v68, s84\nv_mov_b32 v69, s85\nbuffer_store_dwordx2 v[68:69], v248, s[48:51], 0 offen sc1\nL_bump2:\ns_waitcnt vmcnt(13)\nMA 112,88,24\n"
"EX 8\nEX 9\nEX 10\nEX 11\nv_smfmac_f32_32x32x32_bf16 v[48:63], v[176:179], v[0:7], v108\nEX 12\nEX 13\nEX 14\nEX 15\ns_waitcnt lgkmcnt(0)\n"
"v_mfma_f32_32x32x16_bf16 v[32:47], v[100:103], v[128:131], 0\nEX 16\nEX 17\nEX 18\nEX 19\n"
"v_mfma_f32_32x32x8bf16_1k v[32:47], v[106:107], v[136:137], v[32:47]\nMA 116,90,24\nMA 120,88,28\nEX 20\nEX 21\nEX 22\nEX 23\n"
"v_smfmac_f32_32x32x32_bf16 v[48:63], v[180:183], v[8:15], v108\nMA 124,90,28\nMA 184,92,132\nEX 24\nEX 25\nEX 26\nEX 27\n"
"v_smfmac_f32_32x32x32_bf16 v[144:159], v[176:179], v[16:23], v108\nEX 28\nEX 29\nEX 30\nEX 31\n"
"v_mfma_f32_32x32x16_bf16 v[0:15], v[100:103], v[132:135], 0\nds_read_b128 v[96:99], v250 offset:41960\nds_read_b64 v[104:105], v221 offset:41960\n"
"ds_read_b128 v[168:171], v222 offset:41960\nds_read_b128 v[172:175], v111 offset:41960\nEX 32\nEX 33\nEX 34\nEX 35\n"
"v_mfma_f32_32x32x8bf16_1k v[0:15], v[106:107], v[138:139], v[0:15]\nEX 36\nEX 37\nEX 38\nEX 39\n"
"v_smfmac_f32_32x32x32_bf16 v[144:159], v[180:183], v[24:31], v108\ns_mov_b32 s21, 0x120000\n"
"buffer_load_dwordx4 a[64:67], v226, s[48:51], s21 offen sc1\nbuffer_load_dwordx4 a[68:71], v226, s[48:51], s21 offen offset:1024 sc1\n"
"buffer_load_dwordx2 a[144:145], v227, s[48:51], s21 offen sc1\nEX 40\nEX 41\nEX 42\nEX 43\n"
"v_smfmac_f32_32x32x32_bf16 v[48:63], v[160:163], v[32:39], v108\nEX 44\nEX 45\nEX 46\nEX 47\ns_waitcnt lgkmcnt(0)\n"
"v_mfma_f32_32x32x16_bf16 v[16:31], v[96:99], v[128:131], 0\nEX 0\nEX 1\nEX 2\nEX 3\n"
"v_mfma_f32_32x32x8bf16_1k v[16:31], v[104:105], v[136:137], v[16:31]\nEX 4\nEX 5\nEX 6\nEX 7\n"
"v_smfmac_f32_32x32x32_bf16 v[48:63], v[164:167], v[40:47], v108\nEX 8\nEX 9\nEX 10\nEX 11\n"
"v_smfmac_f32_32x32x32_bf16 v[144:159], v[160:163], v[0:7], v108\nEX 12\nEX 13\nEX 14\nEX 15\n"
"v_mfma_f32_32x32x16_bf16 v[32:47], v[96:99], v[132:135], 0\nds_read_b128 v[100:103], v250 offset:39400\nds_read_b64 v[106:107], v221 offset:39400\n"
"ds_read_b128 v[176:179], v222 offset:39400\nds_read_b128 v[180:183], v111 offset:39400\nEX 16\nEX 17\nEX 18\nEX 19\n"
"v_mfma_f32_32x32x8bf16_1k v[32:47], v[104:105], v[138:139], v[32:47]\nEX 20\nEX 21\nEX 22\nEX 23\n"
"v_smfmac_f32_32x32x32_bf16 v[144:159], v[164:167], v[8:15], v108\ns_waitcnt vmcnt(13)\nMA 112,88,32\nEX 24\nEX 25\nEX 26\nEX 27\n"
"v_smfmac_f32_32x32x32_bf16 v[48:63], v[168:171], v[16:23], v108\nEX 28\nEX 29\nEX 30\nEX 31\ns_waitcnt lgkmcnt(0)\n"
"v_mfma_f32_32x32x16_bf16 v[0:15], v[100:103], v[128:131], 0\nEX 32\nEX 33\nEX 34\nEX 35\n"
"v_mfma_f32_32x32x8bf16_1k v[0:15], v[106:107], v[136:137], v[0:15]\nMA 116,90,32\nMA 120,88,36\nEX 36\nEX 37\nEX 38\nEX 39\n"
"v_smfmac_f32_32x32x32_bf16 v[48:63], v[172:175], v[24:31], v108\nMA 124,90,36\nEX 40\nEX 41\nEX 42\nEX 43\n"
"v_smfmac_f32_32x32x32_bf16 v[144:159], v[168:171], v[32:39], v108\nEX 44\nEX 45\nEX 46\nEX 47\ns_barrier\n"
"v_mfma_f32_32x32x16_bf16 v[16:31], v[100:103], v[132:135], 0\nds_read_b128 v[96:99], v250 offset:36840\nds_read_b64 v[104:105], v221 offset:36840\n"
"ds_read_b128 v[160:163], v222 offset:36840\nds_read_b128 v[164:167], v111 offset:36840\nEX 0\nEX 1\nEX 2\nEX 3\n"
"v_mfma_f32_32x32x8bf16_1k v[16:31], v[106:107], v[138:139], v[16:31]\nEX 4\nEX 5\nEX 6\nEX 7\n"
"v_smfmac_f32_32x32x32_bf16 v[144:159], v[172:175], v[40:47], v108\ns_mov_b32 s21, 0x144000\n"
"buffer_load_dwordx4 a[72:75], v226, s[48:51], s21 offen sc1\nbuffer_load_dwordx4 a[76:79], v226, s[48:51], s21 offen offset:1024 sc1\n"
"buffer_load_dwordx2 a[146:147], v227, s[48:51], s21 offen sc1\nEX 8\nEX 9\nEX 10\nEX 11\n"
"v_smfmac_f32_32x32x32_bf16 v[48:63], v[176:179], v[0:7], v108\nEX 12\nEX 13\nEX 14\nEX 15\ns_waitcnt lgkmcnt(0)\n"
"v_mfma_f32_32x32x16_bf16 v[32:47], v[96:99], v[128:131], 0\nEX 16\nEX 17\nEX 18\nEX 19\n"
"v_mfma_f32_32x32x8bf16_1k v[32:47], v[104:105], v[136:137], v[32:47]\ns_add_u32 m0, s6, 0x10000\ns_mov_b32 s86, 0\n"
"buffer_load_dwordx4 v228, s[52:55], s86 offen lds\nEX 20\nEX 21\nEX 22\nEX 23\nv_smfmac_f32_32x32x32_bf16 v[48:63], v[180:183], v[8:15], v108\n"
"s_add_u32 m0, s6, 0x11000\ns_mov_b32 s86, 0xa200\nbuffer_load_dwordx4 v228, s[52:55], s86 offen lds\nEX 24\nEX 25\nEX 26\nEX 27\n"
"v_smfmac_f32_32x32x32_bf16 v[144:159], v[176:179], v[16:23], v108\nEX 28\nEX 29\nEX 30\nEX 31\n"
"v_mfma_f32_32x32x16_bf16 v[0:15], v[96:99], v[132:135], 0\nds_read_b128 v[100:103], v250 offset:34280\nds_read_b64 v[106:107], v221 offset:34280\n"
"ds_read_b128 v[168:171], v222 offset:34280\nds_read_b128 v[172:175], v111 offset:34280\nEX 32\nEX 33\nEX 34\nEX 35\n"
"v_mfma_f32_32x32x8bf16_1k v[0:15], v[104:105], v[138:139], v[0:15]\nEX 36\nEX 37\nEX 38\nEX 39\n"
"v_smfmac_f32_32x32x32_bf16 v[144:159], v[180:183], v[24:31], v108\ns_add_u32 m0, s6, 0x12000\ns_mov_b32 s86, 0x14400\n"
"buffer_load_dwordx4 v228, s[52:55], s86 offen lds\nEX 40\nEX 41\nEX 42\nEX 43\nv_smfmac_f32_32x32x32_bf16 v[48:63], v[160:163], v[32:39], v108\n"
"EX 44\nEX 45\nEX 46\nEX 47\ns_waitcnt lgkmcnt(0)\nv_mfma_f32_32x32x16_bf16 v[16:31], v[100:103], v[128:131], 0\nEX 0\nEX 1\nEX 2\nEX 3\n"
"v_mfma_f32_32x32x8bf16_1k v[16:31], v[106:107], v[136:137], v[16:31]\ns_add_u32 m0, s6, 0x13000\ns_mov_b32 s86, 0x1e600\n"
"buffer_load_dwordx4 v228, s[52:55], s86 offen lds\nEX 4\nEX 5\nEX 6\nEX 7\nv_smfmac_f32_32x32x32_bf16 v[48:63], v[164:167], v[40:47], v108\n"
"s_waitcnt vmcnt(17)\nMA 112,88,40\nEX 8\nEX 9\nEX 10\nEX 11\nv_smfmac_f32_32x32x32_bf16 v[144:159], v[160:163], v[0:7], v108\nEX 12\nEX 13\nEX 14\n"
"EX 15\nv_mfma_f32_32x32x16_bf16 v[32:47], v[100:103], v[132:135], 0\nds_read_b128 v[96:99], v250 offset:31720\n"
"ds_read_b64 v[104:105], v221 offset:31720\nds_read_b128 v[176:179], v222 offset:31720\nds_read_b128 v[180:183], v111 offset:31720\nEX 16\nEX 17\n"
"EX 18\nEX 19\nv_mfma_f32_32x32x8bf16_1k v[32:47], v[106:107], v[138:139], v[32:47]\nEX 20\nEX 21\nEX 22\nEX 23\n"
"v_smfmac_f32_32x32x32_bf16 v[144:159], v[164:167], v[8:15], v108\ns_mov_b32 s21, 0x168000\n"
"buffer_load_dwordx4 a[80:83], v226, s[48:51], s21 offen sc1\nbuffer_load_dwordx4 a[84:87], v226, s[48:51], s21 offen offset:1024 sc1\n"
"buffer_load_dwordx2 a[148:149], v227, s[48:51], s21 offen sc1\nEX 24\nEX 25\nEX 26\nEX 27\n"
"v_smfmac_f32_32x32x32_bf16 v[48:63], v[168:171], v[16:23], v108\nEX 28\nEX 29\nEX 30\nEX 31\ns_waitcnt lgkmcnt(0)\n"
"v_mfma_f32_32x32x16_bf16 v[0:15], v[96:99], v[128:131], 0\nEX 32\nEX 33\nEX 34\nEX 35\n"
"v_mfma_f32_32x32x8bf16_1k v[0:15], v[104:105], v[136:137], v[0:15]\ns_add_u32 m0, s6, 0x14000\ns_mov_b32 s86, 0x28800\n"
"buffer_load_dwordx4 v228, s[52:55], s86 offen lds\nEX 36\nEX 37\nEX 38\nEX 39\nv_smfmac_f32_32x32x32_bf16 v[48:63], v[172:175], v[24:31], v108\n"
"s_add_u32 m0, s6, 0x15000\ns_mov_b32 s86, 0x32a00\nbuffer_load_dwordx4 v228, s[52:55], s86 offen lds\nEX 40\nEX 41\nEX 42\nEX 43\n"
"v_smfmac_f32_32x32x32_bf16 v[144:159], v[168:171], v[32:39], v108\nEX 44\nEX 45\nEX 46\nEX 47\n"
"v_mfma_f32_32x32x16_bf16 v[16:31], v[96:99], v[132:135], 0\nds_read_b128 v[100:103], v250 offset:29160\nds_read_b64 v[106:107], v221 offset:29160\n"
"ds_read_b128 v[160:163], v222 offset:29160\nds_read_b128 v[164:167], v111 offset:29160\nEX 0\nEX 1\nEX 2\nEX 3\n"
"v_mfma_f32_32x32x8bf16_1k v[16:31], v[104:105], v[138:139], v[16:31]\nEX 4\nEX 5\nEX 6\nEX 7\n"
"v_smfmac_f32_32x32x32_bf16 v[144:159], v[172:175], v[40:47], v108\ns_add_u32 m0, s6, 0x16000\ns_mov_b32 s86, 0x3cc00\n"
"buffer_load_dwordx4 v228, s[52:55], s86 offen lds\nEX 8\nEX 9\nEX 10\nEX 11\nv_smfmac_f32_32x32x32_bf16 v[48:63], v[176:179], v[0:7], v108\nEX 12\n"
"EX 13\nEX 14\nEX 15\ns_waitcnt lgkmcnt(0)\nv_mfma_f32_32x32x16_bf16 v[32:47], v[100:103], v[128:131], 0\nEX 16\nEX 17\nEX 18\nEX 19\n"
"v_mfma_f32_32x32x8bf16_1k v[32:47], v[106:107], v[136:137], v[32:47]\ns_add_u32 m0, s6, 0x17000\ns_mov_b32 s86, 0x46e00\n"
"buffer_load_dwordx4 v228, s[52:55], s86 offen lds\nEX 20\nEX 21\nEX 22\nEX 23\nv_smfmac_f32_32x32x32_bf16 v[48:63], v[180:183], v[8:15], v108\n"
"MA 116,90,40\nMA 120,88,44\nEX 24\nEX 25\nEX 26\nEX 27\nv_smfmac_f32_32x32x32_bf16 v[144:159], v[176:179], v[16:23], v108\nEX 28\nEX 29\nEX 30\n"
"EX 31\nv_mfma_f32_32x32x16_bf16 v[0:15], v[100:103], v[132:135], 0\nds_read_b128 v[96:99], v250 offset:26600\n"
"ds_read_b64 v[104:105], v221 offset:26600\nds_read_b128 v[168:171], v222 offset:26600\nds_read_b128 v[172:175], v111 offset:26600\nEX 32\nEX 33\n"
"EX 34\nEX 35\nv_mfma_f32_32x32x8bf16_1k v[0:15], v[106:107], v[138:139], v[0:15]\nEX 36\nEX 37\nEX 38\nEX 39\n"
"v_smfmac_f32_32x32x32_bf16 v[144:159], v[180:183], v[24:31], v108\ns_mov_b32 s21, 0x18c000\n"
"buffer_load_dwordx4 a[88:91], v226, s[48:51], s21 offen sc1\nbuffer_load_dwordx4 a[92:95], v226, s[48:51], s21 offen offset:1024 sc1\n"
"buffer_load_dwordx2 a[150:151], v227, s[48:51], s21 offen sc1\nEX 40\nEX 41\nEX 42\nEX 43\n"
"v_smfmac_f32_32x32x32_bf16 v[48:63], v[160:163], v[32:39], v108\nEX 44\nEX 45\nEX 46\nEX 47\ns_waitcnt lgkmcnt(0)\n"
"v_mfma_f32_32x32x16_bf16 v[16:31], v[96:99], v[128:131], 0\nEX 0\nEX 1\nEX 2\nEX 3\n"
"v_mfma_f32_32x32x8bf16_1k v[16:31], v[104:105], v[136:137], v[16:31]\ns_add_u32 m0, s6, 0x18000\ns_mov_b32 s86, 0x51000\n"
"buffer_load_dwordx4 v228, s[52:55], s86 offen lds\nEX 4\nEX 5\nEX 6\nEX 7\nv_smfmac_f32_32x32x32_bf16 v[48:63], v[164:167], v[40:47], v108\n"
"s_add_u32 m0, s6, 0x19000\ns_mov_b32 s86, 0x5b200\nbuffer_load_dwordx4 v228, s[52:55], s86 offen lds\nEX 8\nEX 9\nEX 10\nEX 11\n"
"v_smfmac_f32_32x32x32_bf16 v[144:159], v[160:163], v[0:7], v108\nEX 12\nEX 13\nEX 14\nEX 15\n"
"v_mfma_f32_32x32x16_bf16 v[32:47], v[96:99], v[132:135], 0\nds_read_b128 v[100:103], v250 offset:24040\nds_read_b64 v[106:107], v221 offset:24040\n"
"ds_read_b128 v[176:179], v222 offset:24040\nds_read_b128 v[180:183], v111 offset:24040\nEX 16\nEX 17\nEX 18\nEX 19\n"
"v_mfma_f32_32x32x8bf16_1k v[32:47], v[104:105], v[138:139], v[32:47]\nEX 20\nEX 21\nEX 22\nEX 23\n"
"v_smfmac_f32_32x32x32_bf16 v[144:159], v[164:167], v[8:15], v108\ns_add_u32 m0, s6, 0x1a000\ns_mov_b32 s86, 0x65400\n"
"buffer_load_dwordx4 v228, s[52:55], s86 offen lds\nEX 24\nEX 25\nEX 26\nEX 27\nv_smfmac_f32_32x32x32_bf16 v[48:63], v[168:171], v[16:23], v108\n"
"EX 28\nEX 29\nEX 30\nEX 31\ns_waitcnt lgkmcnt(0)\nv_mfma_f32_32x32x16_bf16 v[0:15], v[100:103], v[128:131], 0\nEX 32\nEX 33\nEX 34\nEX 35\n"
"v_mfma_f32_32x32x8bf16_1k v[0:15], v[106:107], v[136:137], v[0:15]\ns_add_u32 m0, s6, 0x1b000\ns_mov_b32 s86, 0x6f600\n"
"buffer_load_dwordx4 v228, s[52:55], s86 offen lds\nEX 36\nEX 37\nEX 38\nEX 39\nv_smfmac_f32_32x32x32_bf16 v[48:63], v[172:175], v[24:31], v108\n"
"MA 124,90,44\nMA 184,92,136\nEX 40\nEX 41\nEX 42\nEX 43\nv_smfmac_f32_32x32x32_bf16 v[144:159], v[168:171], v[32:39], v108\nEX 44\nEX 45\nEX 46\n"
"EX 47\nv_mfma_f32_32x32x16_bf16 v[16:31], v[100:103], v[132:135], 0\nds_read_b128 v[96:99], v250 offset:21480\n"
"ds_read_b64 v[104:105], v221 offset:21480\nds_read_b128 v[160:163], v222 offset:21480\nds_read_b128 v[164:167], v111 offset:21480\nEX 0\nEX 1\nEX 2\n"
"EX 3\nv_mfma_f32_32x32x8bf16_1k v[16:31], v[106:107], v[138:139], v[16:31]\nEX 4\nEX 5\nEX 6\nEX 7\n"
"v_smfmac_f32_32x32x32_bf16 v[144:159], v[172:175], v[40:47], v108\ns_mov_b32 s21, 0x1b0000\n"
"buffer_load_dwordx4 a[96:99], v226, s[48:51], s21 offen sc1\nbuffer_load_dwordx4 a[100:103], v226, s[48:51], s21 offen offset:1024 sc1\n"
"buffer_load_dwordx2 a[152:153], v227, s[48:51], s21 offen sc1\nEX 8\nEX 9\nEX 10\nEX 11\n"
"v_smfmac_f32_32x32x32_bf16 v[48:63], v[176:179], v[0:7], v108\nEX 12\nEX 13\nEX 14\nEX 15\ns_waitcnt lgkmcnt(0)\n"
"v_mfma_f32_32x32x16_bf16 v[32:47], v[96:99], v[128:131], 0\nEX 16\nEX 17\nEX 18\nEX 19\n"
"v_mfma_f32_32x32x8bf16_1k v[32:47], v[104:105], v[136:137], v[32:47]\ns_add_u32 m0, s6, 0x1c000\ns_mov_b32 s86, 0x79800\n"
"buffer_load_dwordx4 v228, s[52:55], s86 offen lds\nEX 20\nEX 21\nEX 22\nEX 23\nv_smfmac_f32_32x32x32_bf16 v[48:63], v[180:183], v[8:15], v108\n"
"s_add_u32 m0, s6, 0x1d000\ns_mov_b32 s86, 0x83a00\nbuffer_load_dwordx4 v228, s[52:55], s86 offen lds\nEX 24\nEX 25\nEX 26\nEX 27\n"
"v_smfmac_f32_32x32x32_bf16 v[144:159], v[176:179], v[16:23], v108\nEX 28\nEX 29\nEX 30\nEX 31\n"
"v_mfma_f32_32x32x16_bf16 v[0:15], v[96:99], v[132:135], 0\nds_read_b128 v[100:103], v250 offset:18920\nds_read_b64 v[106:107], v221 offset:18920\n"
"ds_read_b128 v[168:171], v222 offset:18920\nds_read_b128 v[172:175], v111 offset:18920\nEX 32\nEX 33\nEX 34\nEX 35\n"
"v_mfma_f32_32x32x8bf16_1k v[0:15], v[104:105], v[138:139], v[0:15]\nEX 36\nEX 37\nEX 38\nEX 39\n"
"v_smfmac_f32_32x32x32_bf16 v[144:159], v[180:183], v[24:31], v108\ns_add_u32 m0, s6, 0x1e000\ns_mov_b32 s86, 0x8dc00\n"
"buffer_load_dwordx4 v228, s[52:55], s86 offen lds\nEX 40\nEX 41\nEX 42\nEX 43\nv_smfmac_f32_32x32x32_bf16 v[48:63], v[160:163], v[32:39], v108\n"
"EX 44\nEX 45\nEX 46\nEX 47\ns_waitcnt lgkmcnt(0)\nv_mfma_f32_32x32x16_bf16 v[16:31], v[100:103], v[128:131], 0\nEX 0\nEX 1\nEX 2\nEX 3\n"
"v_mfma_f32_32x32x8bf16_1k v[16:31], v[106:107], v[136:137], v[16:31]\ns_add_u32 m0, s6, 0x1f000\ns_mov_b32 s86, 0x97e00\n"
"buffer_load_dwordx4 v228, s[52:55], s86 offen lds\nEX 4\nEX 5\nEX 6\nEX 7\nv_smfmac_f32_32x32x32_bf16 v[48:63], v[164:167], v[40:47], v108\n"
"s_waitcnt vmcnt(34)\nMA 112,88,48\nEX 8\nEX 9\nEX 10\nEX 11\nv_smfmac_f32_32x32x32_bf16 v[144:159], v[160:163], v[0:7], v108\nEX 12\nEX 13\nEX 14\n"
"EX 15\nv_mfma_f32_32x32x16_bf16 v[32:47], v[100:103], v[132:135], 0\nds_read_b128 v[96:99], v250 offset:16360\n"
"ds_read_b64 v[104:105], v221 offset:16360\nds_read_b128 v[176:179], v222 offset:16360\nds_read_b128 v[180:183], v111 offset:16360\nEX 16\nEX 17\n"
"EX 18\nEX 19\nv_mfma_f32_32x32x8bf16_1k v[32:47], v[106:107], v[138:139], v[32:47]\nEX 20\nEX 21\nEX 22\nEX 23\n"
"v_smfmac_f32_32x32x32_bf16 v[144:159], v[164:167], v[8:15], v108\ns_mov_b32 s21, 0x1d4000\n"
"buffer_load_dwordx4 a[104:107], v226, s[48:51], s21 offen sc1\nbuffer_load_dwordx4 a[108:111], v226, s[48:51], s21 offen offset:1024 sc1\n"
"buffer_load_dwordx2 a[154:155], v227, s[48:51], s21 offen sc1\nEX 24\nEX 25\nEX 26\nEX 27\n"
"v_smfmac_f32_32x32x32_bf16 v[48:63], v[168:171], v[16:23], v108\nEX 28\nEX 29\nEX 30\nEX 31\ns_waitcnt lgkmcnt(0)\n"
"v_mfma_f32_32x32x16_bf16 v[0:15], v[96:99], v[128:131], 0\nEX 32\nEX 33\nEX 34\nEX 35\n"
"v_mfma_f32_32x32x8bf16_1k v[0:15], v[104:105], v[136:137], v[0:15]\ns_add_u32 m0, s6, 0x20000\ns_mov_b32 s86, 0\n"
"buffer_load_dwordx4 v229, s[52:55], s86 offen lds\nEX 36\nEX 37\nEX 38\nEX 39\nv_smfmac_f32_32x32x32_bf16 v[48:63], v[172:175], v[24:31], v108\n"
"s_add_u32 m0, s6, 0x21000\ns_mov_b32 s86, 0x51000\nbuffer_load_dwordx4 v229, s[52:55], s86 offen lds\nEX 40\nEX 41\nEX 42\nEX 43\n"
"v_smfmac_f32_32x32x32_bf16 v[144:159], v[168:171], v[32:39], v108\nEX 44\nEX 45\nEX 46\nEX 47\n"
"v_mfma_f32_32x32x16_bf16 v[16:31], v[96:99], v[132:135], 0\nds_read_b128 v[100:103], v250 offset:13800\nds_read_b64 v[106:107], v221 offset:13800\n"
"ds_read_b128 v[160:163], v222 offset:13800\nds_read_b128 v[164:167], v111 offset:13800\nEX 0\nEX 1\nEX 2\nEX 3\n"
"v_mfma_f32_32x32x8bf16_1k v[16:31], v[104:105], v[138:139], v[16:31]\nEX 4\nEX 5\nEX 6\nEX 7\n"
"v_smfmac_f32_32x32x32_bf16 v[144:159], v[172:175], v[40:47], v108\ns_add_u32 m0, s6, 0x22000\ns_add_u32 s86, s12, 0\n"
"buffer_load_dwordx4 v230, s[52:55], s86 offen lds\nEX 8\nEX 9\nEX 10\nEX 11\nv_smfmac_f32_32x32x32_bf16 v[48:63], v[176:179], v[0:7], v108\nEX 12\n"
"EX 13\nEX 14\nEX 15\ns_waitcnt lgkmcnt(0)\nv_mfma_f32_32x32x16_bf16 v[32:47], v[100:103], v[128:131], 0\nEX 16\nEX 17\nEX 18\nEX 19\n"
"v_mfma_f32_32x32x8bf16_1k v[32:47], v[106:107], v[136:137], v[32:47]\ns_add_u32 m0, s6, 0x23000\ns_add_u32 s86, s12, 0x28800\n"
"buffer_load_dwordx4 v230, s[52:55], s86 offen lds\nEX 20\nEX 21\nEX 22\nEX 23\nv_smfmac_f32_32x32x32_bf16 v[48:63], v[180:183], v[8:15], v108\n"
"MA 116,90,48\nMA 120,88,52\nEX 24\nEX 25\nEX 26\nEX 27\nv_smfmac_f32_32x32x32_bf16 v[144:159], v[176:179], v[16:23], v108\nEX 28\nEX 29\nEX 30\n"
"EX 31\nv_mfma_f32_32x32x16_bf16 v[0:15], v[100:103], v[132:135], 0\nds_read_b128 v[96:99], v250 offset:11240\n"
"ds_read_b64 v[104:105], v221 offset:11240\nds_read_b128 v[168:171], v222 offset:11240\nds_read_b128 v[172:175], v111 offset:11240\nEX 32\nEX 33\n"
"EX 34\nEX 35\nv_mfma_f32_32x32x8bf16_1k v[0:15], v[106:107], v[138:139], v[0:15]\nEX 36\nEX 37\nEX 38\nEX 39\n"
"v_smfmac_f32_32x32x32_bf16 v[144:159], v[180:183], v[24:31], v108\ns_mov_b32 s21, 0x1f8000\n"
"buffer_load_dwordx4 a[112:115], v226, s[48:51], s21 offen sc1\nbuffer_load_dwordx4 a[116:119], v226, s[48:51], s21 offen offset:1024 sc1\n"
"buffer_load_dwordx2 a[156:157], v227, s[48:51], s21 offen sc1\nEX 40\nEX 41\nEX 42\nEX 43\n"
"v_smfmac_f32_32x32x32_bf16 v[48:63], v[160:163], v[32:39], v108\nEX 44\nEX 45\nEX 46\nEX 47\ns_waitcnt lgkmcnt(0)\n"
"v_mfma_f32_32x32x16_bf16 v[16:31], v[96:99], v[128:131], 0\nEX 0\nEX 1\nEX 2\nEX 3\n"
"v_mfma_f32_32x32x8bf16_1k v[16:31], v[104:105], v[136:137], v[16:31]\ns_add_u32 m0, s6, 0x24000\ns_add_u32 s86, s12, 0x51000\n"
"buffer_load_dwordx4 v230, s[52:55], s86 offen lds\nEX 4\nEX 5\nEX 6\nEX 7\nv_smfmac_f32_32x32x32_bf16 v[48:63], v[164:167], v[40:47], v108\n"
"s_add_u32 m0, s6, 0x25000\ns_add_u32 s86, s12, 0x79800\nbuffer_load_dwordx4 v230, s[52:55], s86 offen lds\nEX 8\nEX 9\nEX 10\nEX 11\n"
"v_smfmac_f32_32x32x32_bf16 v[144:159], v[160:163], v[0:7], v108\nEX 12\nEX 13\nEX 14\nEX 15\n"
"v_mfma_f32_32x32x16_bf16 v[32:47], v[96:99], v[132:135], 0\nds_read_b128 v[100:103], v250 offset:8680\nds_read_b64 v[106:107], v221 offset:8680\n"
"ds_read_b128 v[176:179], v222 offset:8680\nds_read_b128 v[180:183], v111 offset:8680\nEX 16\nEX 17\nEX 18\nEX 19\n"
"v_mfma_f32_32x32x8bf16_1k v[32:47], v[104:105], v[138:139], v[32:47]\nEX 20\nEX 21\nEX 22\nEX 23\n"
"v_smfmac_f32_32x32x32_bf16 v[144:159], v[164:167], v[8:15], v108\ns_mov_b32 m0, s17\ns_nop 0\nbuffer_load_dwordx4 v231, s[52:55], s13 offen lds\n"
"EX 24\nEX 25\nEX 26\nEX 27\nv_smfmac_f32_32x32x32_bf16 v[48:63], v[168:171], v[16:23], v108\nEX 28\nEX 29\nEX 30\nEX 31\ns_waitcnt lgkmcnt(0)\n"
"v_mfma_f32_32x32x16_bf16 v[0:15], v[100:103], v[128:131], 0\nEX 32\nEX 33\nEX 34\nEX 35\n"
"v_mfma_f32_32x32x8bf16_1k v[0:15], v[106:107], v[136:137], v[0:15]\nMA 124,90,52\ns_waitcnt vmcnt(44)\nMA 112,88,56\nEX 36\nEX 37\nEX 38\nEX 39\n"
"v_smfmac_f32_32x32x32_bf16 v[48:63], v[172:175], v[24:31], v108\nMA 116,90,56\nMA 120,88,60\nEX 40\nEX 41\nEX 42\nEX 43\n"
"v_smfmac_f32_32x32x32_bf16 v[144:159], v[168:171], v[32:39], v108\nEX 44\nEX 45\nEX 46\nEX 47\n"
"v_mfma_f32_32x32x16_bf16 v[16:31], v[100:103], v[132:135], 0\nds_read_b128 v[96:99], v250 offset:6120\nds_read_b64 v[104:105], v221 offset:6120\n"
"ds_read_b128 v[160:163], v222 offset:6120\nds_read_b128 v[164:167], v111 offset:6120\nEX 0\nEX 1\nEX 2\nEX 3\n"
"v_mfma_f32_32x32x8bf16_1k v[16:31], v[106:107], v[138:139], v[16:31]\nEX 4\nEX 5\nEX 6\nEX 7\n"
"v_smfmac_f32_32x32x32_bf16 v[144:159], v[172:175], v[40:47], v108\ns_mov_b32 s21, 0x21c000\n"
"buffer_load_dwordx4 a[120:123], v226, s[48:51], s21 offen sc1\nbuffer_load_dwordx4 a[124:127], v226, s[48:51], s21 offen offset:1024 sc1\n"
"buffer_load_dwordx2 a[158:159], v227, s[48:51], s21 offen sc1\nEX 8\nEX 9\nEX 10\nEX 11\n"
"v_smfmac_f32_32x32x32_bf16 v[48:63], v[176:179], v[0:7], v108\nEX 12\nEX 13\nEX 14\nEX 15\ns_waitcnt lgkmcnt(0)\n"
"v_mfma_f32_32x32x16_bf16 v[32:47], v[96:99], v[128:131], 0\nEX 16\nEX 17\nEX 18\nEX 19\n"
"v_mfma_f32_32x32x8bf16_1k v[32:47], v[104:105], v[136:137], v[32:47]\nMA 124,90,60\nMA 184,92,140\nEX 20\nEX 21\nEX 22\nEX 23\n"
"v_smfmac_f32_32x32x32_bf16 v[48:63], v[180:183], v[8:15], v108\ns_waitcnt vmcnt(44)\nMA 112,88,64\nEX 24\nEX 25\nEX 26\nEX 27\n"
"v_smfmac_f32_32x32x32_bf16 v[144:159], v[176:179], v[16:23], v108\nEX 28\nEX 29\nEX 30\nEX 31\n"
"v_mfma_f32_32x32x16_bf16 v[0:15], v[96:99], v[132:135], 0\nds_read_b128 v[100:103], v250 offset:3560\nds_read_b64 v[106:107], v221 offset:3560\n"
"ds_read_b128 v[168:171], v222 offset:3560\nds_read_b128 v[172:175], v111 offset:3560\nEX 32\nEX 33\nEX 34\nEX 35\n"
"v_mfma_f32_32x32x8bf16_1k v[0:15], v[104:105], v[138:139], v[0:15]\nEX 36\nEX 37\nEX 38\nEX 39\n"
"v_smfmac_f32_32x32x32_bf16 v[144:159], v[180:183], v[24:31], v108\nMA 116,90,64\nMA 120,88,68\nEX 40\nEX 41\nEX 42\nEX 43\n"
"v_smfmac_f32_32x32x32_bf16 v[48:63], v[160:163], v[32:39], v108\nEX 44\nEX 45\nEX 46\nEX 47\ns_waitcnt lgkmcnt(0)\n"
"v_mfma_f32_32x32x16_bf16 v[16:31], v[100:103], v[128:131], 0\nEX 0\nEX 1\nEX 2\nEX 3\n"
"v_mfma_f32_32x32x8bf16_1k v[16:31], v[106:107], v[136:137], v[16:31]\nMA 124,90,68\ns_waitcnt vmcnt(41)\nMA 112,88,72\nEX 4\nEX 5\nEX 6\nEX 7\n"
"v_smfmac_f32_32x32x32_bf16 v[48:63], v[164:167], v[40:47], v108\nMA 116,90,72\nMA 120,88,76\nEX 8\nEX 9\nEX 10\nEX 11\n"
"v_smfmac_f32_32x32x32_bf16 v[144:159], v[160:163], v[0:7], v108\nEX 12\nEX 13\nEX 14\nEX 15\n"
"v_mfma_f32_32x32x16_bf16 v[32:47], v[100:103], v[132:135], 0\nds_read_b128 v[96:99], v250 offset:1000\nds_read_b64 v[104:105], v221 offset:1000\n"
"ds_read_b128 v[176:179], v222 offset:1000\nds_read_b128 v[180:183], v111 offset:1000\nEX 16\nEX 17\nEX 18\nEX 19\n"
"v_mfma_f32_32x32x8bf16_1k v[32:47], v[106:107], v[138:139], v[32:47]\nEX 20\nEX 21\nEX 22\nEX 23\n"
"v_smfmac_f32_32x32x32_bf16 v[144:159], v[164:167], v[8:15], v108\nMA 124,90,76\nMA 184,92,144\nEX 24\nEX 25\nEX 26\nEX 27\n"
"v_smfmac_f32_32x32x32_bf16 v[48:63], v[168:171], v[16:23], v108\nEX 28\nEX 29\nEX 30\nEX 31\ns_waitcnt lgkmcnt(0)\n"
"v_mfma_f32_32x32x16_bf16 v[0:15], v[96:99], v[128:131], 0\nEX 32\nEX 33\nEX 34\nEX 35\n"
"v_mfma_f32_32x32x8bf16_1k v[0:15], v[104:105], v[136:137], v[0:15]\ns_waitcnt vmcnt(34)\nMA 112,88,80\nEX 36\nEX 37\nEX 38\nEX 39\n"
"v_smfmac_f32_32x32x32_bf16 v[48:63], v[172:175], v[24:31], v108\nMA 116,90,80\nMA 120,88,84\nEX 40\nEX 41\nEX 42\nEX 43\n"
"v_smfmac_f32_32x32x32_bf16 v[144:159], v[168:171], v[32:39], v108\nEX 44\nEX 45\nEX 46\nEX 47\n"
"v_mfma_f32_32x32x16_bf16 v[16:31], v[96:99], v[132:135], 0\nv_add_u32 v250, 0xffffa9e8, v250\nv_add_u32 v221, 0xffffa9e8, v221\n"
"v_add_u32 v222, 0xffffa9e8, v222\nv_add_u32 v111, 0xffffa9e8, v111\nds_read_b128 v[100:103], v250 offset:20480\n"
"ds_read_b64 v[106:107], v221 offset:20480\nds_read_b128 v[160:163], v222 offset:20480\nds_read_b128 v[164:167], v111 offset:20480\nEX 0\nEX 1\nEX 2\n"
"EX 3\nv_mfma_f32_32x32x8bf16_1k v[16:31], v[104:105], v[138:139], v[16:31]\nEX 4\nEX 5\nEX 6\nEX 7\n"
"v_smfmac_f32_32x32x32_bf16 v[144:159], v[172:175], v[40:47], v108\nMA 124,90,84\ns_waitcnt vmcnt(27)\nMA 112,88,88\nEX 8\nEX 9\nEX 10\nEX 11\n"
"v_smfmac_f32_32x32x32_bf16 v[48:63], v[176:179], v[0:7], v108\nEX 12\nEX 13\nEX 14\nEX 15\ns_waitcnt lgkmcnt(0)\n"
"v_mfma_f32_32x32x16_bf16 v[32:47], v[100:103], v[128:131], 0\nEX 16\nEX 17\nEX 18\nEX 19\n"
"v_mfma_f32_32x32x8bf16_1k v[32:47], v[106:107], v[136:137], v[32:47]\nMA 116,90,88\nMA 120,88,92\nEX 20\nEX 21\nEX 22\nEX 23\n"
"v_smfmac_f32_32x32x32_bf16 v[48:63], v[180:183], v[8:15], v108\nMA 124,90,92\nMA 184,92,148\nEX 24\nEX 25\nEX 26\nEX 27\n"
"v_smfmac_f32_32x32x32_bf16 v[144:159], v[176:179], v[16:23], v108\nEX 28\nEX 29\nEX 30\nEX 31\n"
"v_mfma_f32_32x32x16_bf16 v[0:15], v[100:103], v[132:135], 0\nds_read_b128 v[96:99], v250 offset:17920\nds_read_b64 v[104:105], v221 offset:17920\n"
"ds_read_b128 v[168:171], v222 offset:17920\nds_read_b128 v[172:175], v111 offset:17920\nEX 32\nEX 33\nEX 34\nEX 35\n"
"v_mfma_f32_32x32x8bf16_1k v[0:15], v[106:107], v[138:139], v[0:15]\nEX 36\nEX 37\nEX 38\nEX 39\n"
"v_smfmac_f32_32x32x32_bf16 v[144:159], v[180:183], v[24:31], v108\ns_waitcnt vmcnt(20)\nMA 112,88,96\nEX 40\nEX 41\nEX 42\nEX 43\n"
"v_smfmac_f32_32x32x32_bf16 v[48:63], v[160:163], v[32:39], v108\nEX 44\nEX 45\nEX 46\nEX 47\ns_waitcnt lgkmcnt(0)\n"
"v_mfma_f32_32x32x16_bf16 v[16:31], v[96:99], v[128:131], 0\nEX 0\nEX 1\nEX 2\nEX 3\n"
"v_mfma_f32_32x32x8bf16_1k v[16:31], v[104:105], v[136:137], v[16:31]\nMA 116,90,96\nMA 120,88,100\nEX 4\nEX 5\nEX 6\nEX 7\n"
"v_smfmac_f32_32x32x32_bf16 v[48:63], v[164:167], v[40:47], v108\nMA 124,90,100\nEX 8\nEX 9\nEX 10\nEX 11\n"
"v_smfmac_f32_32x32x32_bf16 v[144:159], v[160:163], v[0:7], v108\nEX 12\nEX 13\nEX 14\nEX 15\n"
"v_mfma_f32_32x32x16_bf16 v[32:47], v[96:99], v[132:135], 0\nds_read_b128 v[100:103], v250 offset:15360\nds_read_b64 v[106:107], v221 offset:15360\n"
"ds_read_b128 v[176:179], v222 offset:15360\nds_read_b128 v[180:183], v111 offset:15360\nEX 16\nEX 17\nEX 18\nEX 19\n"
"v_mfma_f32_32x32x8bf16_1k v[32:47], v[104:105], v[138:139], v[32:47]\nEX 20\nEX 21\nEX 22\nEX 23\n"
"v_smfmac_f32_32x32x32_bf16 v[144:159], v[164:167], v[8:15], v108\ns_waitcnt vmcnt(13)\nMA 112,88,104\nEX 24\nEX 25\nEX 26\nEX 27\n"
"v_smfmac_f32_32x32x32_bf16 v[48:63], v[168:171], v[16:23], v108\nEX 28\nEX 29\nEX 30\nEX 31\ns_waitcnt lgkmcnt(0)\n"
"v_mfma_f32_32x32x16_bf16 v[0:15], v[100:103], v[128:131], 0\nEX 32\nEX 33\nEX 34\nEX 35\n"
"v_mfma_f32_32x32x8bf16_1k v[0:15], v[106:107], v[136:137], v[0:15]\nMA 116,90,104\nMA 120,88,108\nEX 36\nEX 37\nEX 38\nEX 39\n"
"v_smfmac_f32_32x32x32_bf16 v[48:63], v[172:175], v[24:31], v108\nMA 124,90,108\nMA 184,92,152\nEX 40\nEX 41\nEX 42\nEX 43\n"
"v_smfmac_f32_32x32x32_bf16 v[144:159], v[168:171], v[32:39], v108\nEX 44\nEX 45\nEX 46\nEX 47\n"
"v_mfma_f32_32x32x16_bf16 v[16:31], v[100:103], v[132:135], 0\nds_read_b128 v[96:99], v250 offset:12800\nds_read_b64 v[104:105], v221 offset:12800\n"
"ds_read_b128 v[160:163], v222 offset:12800\nds_read_b128 v[164:167], v111 offset:12800\nEX 0\nEX 1\nEX 2\nEX 3\n"
"v_mfma_f32_32x32x8bf16_1k v[16:31], v[106:107], v[138:139], v[16:31]\nEX 4\nEX 5\nEX 6\nEX 7\n"
"v_smfmac_f32_32x32x32_bf16 v[144:159], v[172:175], v[40:47], v108\nEX 8\nEX 9\nEX 10\nEX 11\n"
"v_smfmac_f32_32x32x32_bf16 v[48:63], v[176:179], v[0:7], v108\nEX 12\nEX 13\nEX 14\nEX 15\ns_waitcnt lgkmcnt(0)\n"
"v_mfma_f32_32x32x16_bf16 v[32:47], v[96:99], v[128:131], 0\nEX 16\nEX 17\nEX 18\nEX 19\n"
"v_mfma_f32_32x32x8bf16_1k v[32:47], v[104:105], v[136:137], v[32:47]\nEX 20\nEX 21\nEX 22\nEX 23\n"
"v_smfmac_f32_32x32x32_bf16 v[48:63], v[180:183], v[8:15], v108\nEX 24\nEX 25\nEX 26\nEX 27\n"
"v_smfmac_f32_32x32x32_bf16 v[144:159], v[176:179], v[16:23], v108\nEX 28\nEX 29\nEX 30\nEX 31\ns_waitcnt vmcnt(3)\ns_barrier\n"
"v_mfma_f32_32x32x16_bf16 v[0:15], v[96:99], v[132:135], 0\nds_read_b128 v[100:103], v250 offset:10240\nds_read_b64 v[106:107], v221 offset:10240\n"
"ds_read_b128 v[168:171], v222 offset:10240\nds_read_b128 v[172:175], v111 offset:10240\nEX 32\nEX 33\nEX 34\nEX 35\n"
"v_mfma_f32_32x32x8bf16_1k v[0:15], v[104:105], v[138:139], v[0:15]\nEX 36\nEX 37\nEX 38\nEX 39\n"
"v_smfmac_f32_32x32x32_bf16 v[144:159], v[180:183], v[24:31], v108\nMA 112,88,112\nEX 40\nEX 41\nEX 42\nEX 43\n"
"v_smfmac_f32_32x32x32_bf16 v[48:63], v[160:163], v[32:39], v108\nEX 44\nEX 45\nEX 46\nEX 47\ns_waitcnt lgkmcnt(0)\n"
"v_mfma_f32_32x32x16_bf16 v[16:31], v[100:103], v[128:131], 0\nEX 0\nEX 1\nEX 2\nEX 3\n"
"v_mfma_f32_32x32x8bf16_1k v[16:31], v[106:107], v[136:137], v[16:31]\nMA 116,90,112\nMA 120,88,116\nEX 4\nEX 5\nEX 6\nEX 7\n"
"v_smfmac_f32_32x32x32_bf16 v[48:63], v[164:167], v[40:47], v108\nMA 124,90,116\nEX 8\nEX 9\nEX 10\nEX 11\n"
"v_smfmac_f32_32x32x32_bf16 v[144:159], v[160:163], v[0:7], v108\nEX 12\nEX 13\nEX 14\nEX 15\n"
"v_mfma_f32_32x32x16_bf16 v[32:47], v[100:103], v[132:135], 0\nds_read_b128 v[96:99], v250 offset:7680\nds_read_b64 v[104:105], v221 offset:7680\n"
"ds_read_b128 v[176:179], v222 offset:7680\nds_read_b128 v[180:183], v111 offset:7680\nEX 16\nEX 17\nEX 18\nEX 19\n"
"v_mfma_f32_32x32x8bf16_1k v[32:47], v[106:107], v[138:139], v[32:47]\nEX 20\nEX 21\nEX 22\nEX 23\n"
"v_smfmac_f32_32x32x32_bf16 v[144:159], v[164:167], v[8:15], v108\nEX 24\nEX 25\nEX 26\nEX 27\n"
"v_smfmac_f32_32x32x32_bf16 v[48:63], v[168:171], v[16:23], v108\nEX 28\nEX 29\nEX 30\nEX 31\ns_waitcnt lgkmcnt(0)\n"
"v_mfma_f32_32x32x16_bf16 v[0:15], v[96:99], v[128:131], 0\nEX 32\nEX 33\nEX 34\nEX 35\n"
"v_mfma_f32_32x32x8bf16_1k v[0:15], v[104:105], v[136:137], v[0:15]\nEX 36\nEX 37\nEX 38\nEX 39\n"
"v_smfmac_f32_32x32x32_bf16 v[48:63], v[172:175], v[24:31], v108\nEX 40\nEX 41\nEX 42\nEX 43\n"
"v_smfmac_f32_32x32x32_bf16 v[144:159], v[168:171], v[32:39], v108\nEX 44\nEX 45\nEX 46\nEX 47\n"
"v_mfma_f32_32x32x16_bf16 v[16:31], v[96:99], v[132:135], 0\nds_read_b128 v[100:103], v250 offset:5120\nds_read_b64 v[106:107], v221 offset:5120\n"
"ds_read_b128 v[160:163], v222 offset:5120\nds_read_b128 v[164:167], v111 offset:5120\nEX 0\nEX 1\nEX 2\nEX 3\n"
"v_mfma_f32_32x32x8bf16_1k v[16:31], v[104:105], v[138:139], v[16:31]\nEX 4\nEX 5\nEX 6\nEX 7\n"
"v_smfmac_f32_32x32x32_bf16 v[144:159], v[172:175], v[40:47], v108\ns_waitcnt vmcnt(0)\nMA 112,88,120\nEX 8\nEX 9\nEX 10\nEX 11\n"
"v_smfmac_f32_32x32x32_bf16 v[48:63], v[176:179], v[0:7], v108\nEX 12\nEX 13\nEX 14\nEX 15\ns_waitcnt lgkmcnt(0)\n"
"v_mfma_f32_32x32x16_bf16 v[32:47], v[100:103], v[128:131], 0\nEX 16\nEX 17\nEX 18\nEX 19\n"
"v_mfma_f32_32x32x8bf16_1k v[32:47], v[106:107], v[136:137], v[32:47]\nMA 116,90,120\nMA 120,88,124\nEX 20\nEX 21\nEX 22\nEX 23\n"
"v_smfmac_f32_32x32x32_bf16 v[48:63], v[180:183], v[8:15], v108\nMA 124,90,124\nMA 184,92,156\nEX 24\nEX 25\nEX 26\nEX 27\n"
"v_smfmac_f32_32x32x32_bf16 v[144:159], v[176:179], v[16:23], v108\nEX 28\nEX 29\nEX 30\nEX 31\n"
"v_mfma_f32_32x32x16_bf16 v[0:15], v[100:103], v[132:135], 0\nds_read_b128 v[96:99], v250 offset:2560\nds_read_b64 v[104:105], v221 offset:2560\n"
"ds_read_b128 v[168:171], v222 offset:2560\nds_read_b128 v[172:175], v111 offset:2560\nEX 32\nEX 33\nEX 34\nEX 35\n"
"v_mfma_f32_32x32x8bf16_1k v[0:15], v[106:107], v[138:139], v[0:15]\nEX 36\nEX 37\nEX 38\nEX 39\n"
"v_smfmac_f32_32x32x32_bf16 v[144:159], v[180:183], v[24:31], v108\nEX 40\nEX 41\nEX 42\nEX 43\n"
"v_smfmac_f32_32x32x32_bf16 v[48:63], v[160:163], v[32:39], v108\nEX 44\nEX 45\nEX 46\nEX 47\ns_waitcnt lgkmcnt(0)\n"
"v_mfma_f32_32x32x16_bf16 v[16:31], v[96:99], v[128:131], 0\nEX 0\nEX 1\nEX 2\nEX 3\n"
"v_mfma_f32_32x32x8bf16_1k v[16:31], v[104:105], v[136:137], v[16:31]\nEX 4\nEX 5\nEX 6\nEX 7\n"
"v_smfmac_f32_32x32x32_bf16 v[48:63], v[164:167], v[40:47], v108\nEX 8\nEX 9\nEX 10\nEX 11\n"
"v_smfmac_f32_32x32x32_bf16 v[144:159], v[160:163], v[0:7], v108\nEX 12\nEX 13\nEX 14\nEX 15\n"
"v_mfma_f32_32x32x16_bf16 v[32:47], v[96:99], v[132:135], 0\nds_read_b128 v[100:103], v250\nds_read_b64 v[106:107], v221\n"
"ds_read_b128 v[176:179], v222\nds_read_b128 v[180:183], v111\nEX 16\nEX 17\nEX 18\nEX 19\n"
"v_mfma_f32_32x32x8bf16_1k v[32:47], v[104:105], v[138:139], v[32:47]\nEX 20\nEX 21\nEX 22\nEX 23\n"
"v_smfmac_f32_32x32x32_bf16 v[144:159], v[164:167], v[8:15], v108\nEX 24\nEX 25\nEX 26\nEX 27\n"
"v_smfmac_f32_32x32x32_bf16 v[48:63], v[168:171], v[16:23], v108\nEX 28\nEX 29\nEX 30\nEX 31\ns_waitcnt lgkmcnt(0)\n"
"v_mfma_f32_32x32x16_bf16 v[0:15], v[100:103], v[128:131], 0\nEX 32\nEX 33\nEX 34\nEX 35\n"
"v_mfma_f32_32x32x8bf16_1k v[0:15], v[106:107], v[136:137], v[0:15]\nEX 36\nEX 37\nEX 38\nEX 39\n"
"v_smfmac_f32_32x32x32_bf16 v[48:63], v[172:175], v[24:31], v108\nEX 40\nEX 41\nEX 42\nEX 43\n"
"v_smfmac_f32_32x32x32_bf16 v[144:159], v[168:171], v[32:39], v108\nEX 44\nEX 45\nEX 46\nEX 47\n"
"v_mfma_f32_32x32x16_bf16 v[16:31], v[100:103], v[132:135], 0\nEX 0\nEX 1\nEX 2\nEX 3\n"
"v_mfma_f32_32x32x8bf16_1k v[16:31], v[106:107], v[138:139], v[16:31]\nEX 4\nEX 5\nEX 6\nEX 7\n"
"v_smfmac_f32_32x32x32_bf16 v[144:159], v[172:175], v[40:47], v108\nEX 8\nEX 9\nEX 10\nEX 11\n"
"v_smfmac_f32_32x32x32_bf16 v[48:63], v[176:179], v[0:7], v108\nEX 12\nEX 13\nEX 14\nEX 15\nEX 16\nEX 17\nEX 18\nEX 19\nEX 20\nEX 21\nEX 22\nEX 23\n"
"v_smfmac_f32_32x32x32_bf16 v[48:63], v[180:183], v[8:15], v108\nEX 24\nEX 25\nEX 26\nEX 27\n"
"v_smfmac_f32_32x32x32_bf16 v[144:159], v[176:179], v[16:23], v108\nEX 28\nEX 29\nEX 30\nEX 31\ns_nop 1\n"
"v_smfmac_f32_32x32x32_bf16 v[144:159], v[180:183], v[24:31], v108\nv_mov_b32 v64, v58\nv_mov_b32 v65, v58\ns_nop 1\nv_permlane32_swap_b32 v64, v65\n"
"v_rcp_f32 v64, v64\ns_nop 0\nMU 48,48,64\nMU 49,49,64\nMU 50,50,64\nMU 51,51,64\nMU 52,52,64\nMU 53,53,64\nMU 54,54,64\nMU 55,55,64\nMU 56,56,64\n"
"MU 57,57,64\nv_cvt_pk_bf16_f32 v172, v48, v49\nv_cvt_pk_bf16_f32 v173, v50, v51\nv_cvt_pk_bf16_f32 v174, v52, v53\nv_cvt_pk_bf16_f32 v175, v54, v55\n"
"v_cvt_pk_bf16_f32 v180, v56, v57\nv_min_u32 v172, 0xfffffffe, v172\nv_min_u32 v173, 0xfffffffe, v173\nv_min_u32 v174, 0xfffffffe, v174\n"
"v_min_u32 v175, 0xfffffffe, v175\ns_mov_b32 s21, 0\nbuffer_store_dwordx4 v[172:175], v223, s[48:51], s21 offen sc1\nv_mov_b32 v66, v154\n"
"v_mov_b32 v67, v154\ns_nop 1\nv_permlane32_swap_b32 v66, v67\nv_rcp_f32 v66, v66\ns_nop 0\nMU 144,144,66\nMU 145,145,66\nMU 146,146,66\n"
"MU 147,147,66\nMU 148,148,66\nMU 149,149,66\nMU 150,150,66\nMU 151,151,66\nMU 152,152,66\nMU 153,153,66\nv_cvt_pk_bf16_f32 v176, v144, v145\n"
"v_cvt_pk_bf16_f32 v177, v146, v147\nv_cvt_pk_bf16_f32 v178, v148, v149\nv_cvt_pk_bf16_f32 v179, v150, v151\nv_cvt_pk_bf16_f32 v181, v152, v153\n"
"v_min_u32 v176, 0xfffffffe, v176\nv_min_u32 v177, 0xfffffffe, v177\nv_min_u32 v178, 0xfffffffe, v178\nv_min_u32 v179, 0xfffffffe, v179\n"
"s_mov_b32 s21, 0x8000\nbuffer_store_dwordx4 v[176:179], v223, s[48:51], s21 offen sc1\nv_permlane32_swap_b32 v180, v181\n"
"v_min_u32 v180, 0xfffffffe, v180\nbuffer_store_dword v180, v224, s[48:51], 0 offen sc1\nds_read_b64_tr_b16 a[0:1], v232\n"
"ds_read_b64_tr_b16 a[2:3], v232 offset:2048\nds_read_b64_tr_b16 a[4:5], v233\nds_read_b64_tr_b16 a[6:7], v233 offset:2048\n"
"ds_read_b64_tr_b16 a[8:9], v234\nds_read_b64_tr_b16 a[10:11], v234 offset:2048\nds_read_b64_tr_b16 a[12:13], v235\n"
"ds_read_b64_tr_b16 a[14:15], v235 offset:2048\nds_read_b64_tr_b16 a[16:17], v232 offset:4096\nds_read_b64_tr_b16 a[18:19], v232 offset:6144\n"
"ds_read_b64_tr_b16 a[20:21], v233 offset:4096\nds_read_b64_tr_b16 a[22:23], v233 offset:6144\nds_read_b64_tr_b16 a[24:25], v234 offset:4096\n"
"ds_read_b64_tr_b16 a[26:27], v234 offset:6144\ns_waitcnt lgkmcnt(8)\nds_read_b64_tr_b16 a[28:29], v235 offset:4096\n"
"ds_read_b64_tr_b16 a[30:31], v235 offset:6144\nds_read_b64_tr_b16 a[32:33], v232 offset:8192\nds_read_b64_tr_b16 a[34:35], v232 offset:10240\n"
"ds_read_b64_tr_b16 a[36:37], v233 offset:8192\nds_read_b64_tr_b16 a[38:39], v233 offset:10240\ns_waitcnt lgkmcnt(8)\n"
"ds_read_b64_tr_b16 a[40:41], v234 offset:8192\nds_read_b64_tr_b16 a[42:43], v234 offset:10240\nds_read_b64_tr_b16 a[44:45], v235 offset:8192\n"
"ds_read_b64_tr_b16 a[46:47], v235 offset:10240\nds_read_b64_tr_b16 a[48:49], v232 offset:12288\nds_read_b64_tr_b16 a[50:51], v232 offset:14336\n"
"s_waitcnt lgkmcnt(8)\nds_read_b64_tr_b16 a[52:53], v233 offset:12288\nds_read_b64_tr_b16 a[54:55], v233 offset:14336\n"
"ds_read_b64_tr_b16 a[56:57], v234 offset:12288\nds_read_b64_tr_b16 a[58:59], v234 offset:14336\nds_read_b64_tr_b16 a[60:61], v235 offset:12288\n"
"ds_read_b64_tr_b16 a[62:63], v235 offset:14336\ns_waitcnt lgkmcnt(8)\nds_read_b64_tr_b16 a[64:65], v232 offset:16384\n"
"ds_read_b64_tr_b16 a[66:67], v232 offset:18432\nds_read_b64_tr_b16 a[68:69], v233 offset:16384\nds_read_b64_tr_b16 a[70:71], v233 offset:18432\n"
"ds_read_b64_tr_b16 a[72:73], v234 offset:16384\nds_read_b64_tr_b16 a[74:75], v234 offset:18432\ns_waitcnt lgkmcnt(8)\n"
"ds_read_b64_tr_b16 a[76:77], v235 offset:16384\nds_read_b64_tr_b16 a[78:79], v235 offset:18432\nds_read_b64_tr_b16 a[80:81], v232 offset:20480\n"
"ds_read_b64_tr_b16 a[82:83], v232 offset:22528\nds_read_b64_tr_b16 a[84:85], v233 offset:20480\nds_read_b64_tr_b16 a[86:87], v233 offset:22528\n"
"s_waitcnt lgkmcnt(8)\nds_read_b64_tr_b16 a[88:89], v234 offset:20480\nds_read_b64_tr_b16 a[90:91], v234 offset:22528\n"
"ds_read_b64_tr_b16 a[92:93], v235 offset:20480\nds_read_b64_tr_b16 a[94:95], v235 offset:22528\nds_read_b64_tr_b16 a[96:97], v232 offset:24576\n"
"ds_read_b64_tr_b16 a[98:99], v232 offset:26624\ns_waitcnt lgkmcnt(8)\nds_read_b64_tr_b16 a[100:101], v233 offset:24576\n"
"ds_read_b64_tr_b16 a[102:103], v233 offset:26624\nds_read_b64_tr_b16 a[104:105], v234 offset:24576\nds_read_b64_tr_b16 a[106:107], v234 offset:26624\n"
"ds_read_b64_tr_b16 a[108:109], v235 offset:24576\nds_read_b64_tr_b16 a[110:111], v235 offset:26624\ns_waitcnt lgkmcnt(8)\n"
"ds_read_b64_tr_b16 a[112:113], v232 offset:28672\nds_read_b64_tr_b16 a[114:115], v232 offset:30720\nds_read_b64_tr_b16 a[116:117], v233 offset:28672\n"
"ds_read_b64_tr_b16 a[118:119], v233 offset:30720\nds_read_b64_tr_b16 a[120:121], v234 offset:28672\nds_read_b64_tr_b16 a[122:123], v234 offset:30720\n"
"s_waitcnt lgkmcnt(8)\nds_read_b64_tr_b16 a[124:125], v235 offset:28672\nds_read_b64_tr_b16 a[126:127], v235 offset:30720\n"
"ds_read_b64_tr_b16 a[128:129], v232 offset:32768\nds_read_b64_tr_b16 a[130:131], v232 offset:34816\nds_read_b64_tr_b16 a[132:133], v233 offset:32768\n"
"ds_read_b64_tr_b16 a[134:135], v233 offset:34816\ns_waitcnt lgkmcnt(8)\nds_read_b64_tr_b16 a[136:137], v234 offset:32768\n"
"ds_read_b64_tr_b16 a[138:139], v234 offset:34816\nds_read_b64_tr_b16 a[140:141], v235 offset:32768\nds_read_b64_tr_b16 a[142:143], v235 offset:34816\n"
"ds_read_b64_tr_b16 a[144:145], v232 offset:36864\nds_read_b64_tr_b16 a[146:147], v232 offset:38912\ns_waitcnt lgkmcnt(8)\n"
"ds_read_b64_tr_b16 a[148:149], v233 offset:36864\nds_read_b64_tr_b16 a[150:151], v233 offset:38912\nds_read_b64_tr_b16 a[152:153], v234 offset:36864\n"
"ds_read_b64_tr_b16 a[154:155], v234 offset:38912\nds_read_b64_tr_b16 a[156:157], v235 offset:36864\nds_read_b64_tr_b16 a[158:159], v235 offset:38912\n"
"s_waitcnt lgkmcnt(8)\nds_read_b64_tr_b16 a[160:161], v232 offset:40960\nds_read_b64_tr_b16 a[162:163], v232 offset:43008\n"
"ds_read_b64_tr_b16 a[164:165], v233 offset:40960\nds_read_b64_tr_b16 a[166:167], v233 offset:43008\nds_read_b64_tr_b16 a[168:169], v234 offset:40960\n"
"ds_read_b64_tr_b16 a[170:171], v234 offset:43008\ns_waitcnt lgkmcnt(8)\nds_read_b64_tr_b16 a[172:173], v235 offset:40960\n"
"ds_read_b64_tr_b16 a[174:175], v235 offset:43008\nds_read_b64_tr_b16 a[176:177], v232 offset:45056\nds_read_b64_tr_b16 a[178:179], v232 offset:47104\n"
"ds_read_b64_tr_b16 a[180:181], v233 offset:45056\nds_read_b64_tr_b16 a[182:183], v233 offset:47104\ns_waitcnt lgkmcnt(8)\n"
"ds_read_b64_tr_b16 a[184:185], v234 offset:45056\nds_read_b64_tr_b16 a[186:187], v234 offset:47104\nds_read_b64_tr_b16 a[188:189], v235 offset:45056\n"
"ds_read_b64_tr_b16 a[190:191], v235 offset:47104\nds_read_b64_tr_b16 a[192:193], v232 offset:49152\nds_read_b64_tr_b16 a[194:195], v232 offset:51200\n"
"s_waitcnt lgkmcnt(8)\nds_read_b64_tr_b16 a[196:197], v233 offset:49152\nds_read_b64_tr_b16 a[198:199], v233 offset:51200\n"
"ds_read_b64_tr_b16 a[200:201], v234 offset:49152\nds_read_b64_tr_b16 a[202:203], v234 offset:51200\nds_read_b64_tr_b16 a[204:205], v235 offset:49152\n"
"ds_read_b64_tr_b16 a[206:207], v235 offset:51200\ns_waitcnt lgkmcnt(8)\nds_read_b64_tr_b16 a[208:209], v232 offset:53248\n"
"ds_read_b64_tr_b16 a[210:211], v232 offset:55296\nds_read_b64_tr_b16 a[212:213], v233 offset:53248\nds_read_b64_tr_b16 a[214:215], v233 offset:55296\n"
"ds_read_b64_tr_b16 a[216:217], v234 offset:53248\nds_read_b64_tr_b16 a[218:219], v234 offset:55296\ns_waitcnt lgkmcnt(8)\n"
"ds_read_b64_tr_b16 a[220:221], v235 offset:53248\nds_read_b64_tr_b16 a[222:223], v235 offset:55296\nds_read_b64_tr_b16 a[224:225], v232 offset:57344\n"
"ds_read_b64_tr_b16 a[226:227], v232 offset:59392\nds_read_b64_tr_b16 a[228:229], v233 offset:57344\nds_read_b64_tr_b16 a[230:231], v233 offset:59392\n"
"s_waitcnt lgkmcnt(8)\nds_read_b64_tr_b16 a[232:233], v234 offset:57344\nds_read_b64_tr_b16 a[234:235], v234 offset:59392\n"
"ds_read_b64_tr_b16 a[236:237], v235 offset:57344\nds_read_b64_tr_b16 a[238:239], v235 offset:59392\nds_read_b64_tr_b16 a[240:241], v232 offset:61440\n"
"ds_read_b64_tr_b16 a[242:243], v232 offset:63488\ns_waitcnt lgkmcnt(8)\nds_read_b64_tr_b16 a[244:245], v233 offset:61440\n"
"ds_read_b64_tr_b16 a[246:247], v233 offset:63488\nds_read_b64_tr_b16 a[248:249], v234 offset:61440\nds_read_b64_tr_b16 a[250:251], v234 offset:63488\n"
"ds_read_b64_tr_b16 a[252:253], v235 offset:61440\nds_read_b64_tr_b16 a[254:255], v235 offset:63488\ns_waitcnt lgkmcnt(8)\n"
"v_add_u32 v232, 0x10000, v232\nv_add_u32 v233, 0x10000, v233\nv_add_u32 v234, 0x10000, v234\nv_add_u32 v235, 0x10000, v235\n"
"ds_read_b64_tr_b16 v[96:97], v232\nTR 98,232,2048\nds_read_b64_tr_b16 v[100:101], v233\nTR 102,233,2048\nds_read_b64_tr_b16 v[104:105], v234\n"
"TR 106,234,2048\ns_waitcnt lgkmcnt(8)\nds_read_b64_tr_b16 v[128:129], v235\nTR 130,235,2048\nTR 132,232,4096\nTR 134,232,6144\nTR 136,233,4096\n"
"TR 138,233,6144\ns_waitcnt lgkmcnt(8)\nTR 144,234,4096\nTR 146,234,6144\nTR 148,235,4096\nTR 150,235,6144\nds_read_b64_tr_b16 v[160:161], v236\n"
"TR 162,236,512\nTR 164,236,1024\nTR 166,236,1536\ns_mov_b32 s24, 0\ns_mov_b32 s25, 0x1000\ns_mov_b32 s26, 0x2000\ns_mov_b32 s27, 0x3000\n"
"s_mov_b32 s28, 0x4000\ns_mov_b32 s87, 0x4000\nL_opoll:\nbuffer_load_dwordx4 v[0:3], v225, s[48:51], s24 offen sc1\n"
"buffer_load_dwordx4 v[4:7], v225, s[48:51], s24 offen offset:1024 sc1\nbuffer_load_dwordx4 v[8:11], v225, s[48:51], s24 offen offset:2048 sc1\n"
"buffer_load_dwordx4 v[12:15], v225, s[48:51], s24 offen offset:3072 sc1\nbuffer_load_dwordx4 v[16:19], v225, s[48:51], s25 offen sc1\n"
"buffer_load_dwordx4 v[20:23], v225, s[48:51], s25 offen offset:1024 sc1\nbuffer_load_dwordx4 v[24:27], v225, s[48:51], s25 offen offset:2048 sc1\n"
"buffer_load_dwordx4 v[28:31], v225, s[48:51], s25 offen offset:3072 sc1\nbuffer_load_dwordx4 v[32:35], v225, s[48:51], s26 offen sc1\n"
"buffer_load_dwordx4 v[36:39], v225, s[48:51], s26 offen offset:1024 sc1\nbuffer_load_dwordx4 v[40:43], v225, s[48:51], s26 offen offset:2048 sc1\n"
"buffer_load_dwordx4 v[44:47], v225, s[48:51], s26 offen offset:3072 sc1\nbuffer_load_dwordx4 v[48:51], v225, s[48:51], s27 offen sc1\n"
"buffer_load_dwordx4 v[52:55], v225, s[48:51], s27 offen offset:1024 sc1\nbuffer_load_dwordx4 v[56:59], v225, s[48:51], s27 offen offset:2048 sc1\n"
"buffer_load_dwordx4 v[60:63], v225, s[48:51], s27 offen offset:3072 sc1\nbuffer_load_dword v64, v110, s[48:51], s24 offen sc1\n"
"buffer_load_dword v65, v110, s[48:51], s24 offen offset:512 sc1\nbuffer_load_dword v66, v110, s[48:51], s24 offen offset:1024 sc1\n"
"buffer_load_dword v67, v110, s[48:51], s24 offen offset:1536 sc1\nbuffer_load_dword v68, v110, s[48:51], s26 offen sc1\n"
"buffer_load_dword v69, v110, s[48:51], s26 offen offset:512 sc1\nbuffer_load_dword v70, v110, s[48:51], s26 offen offset:1024 sc1\n"
"buffer_load_dword v71, v110, s[48:51], s26 offen offset:1536 sc1\ns_waitcnt vmcnt(18)\nv_max_u32 v92, v0, v1\nv_max3_u32 v92, v92, v2, v3\n"
"v_max3_u32 v92, v92, v4, v5\nv_max3_u32 v92, v92, v6, v7\nv_max3_u32 v92, v92, v8, v9\nv_max3_u32 v92, v92, v10, v11\nv_max3_u32 v92, v92, v12, v13\n"
"v_max3_u32 v92, v92, v14, v15\nv_max3_u32 v92, v92, v16, v17\nv_max3_u32 v92, v92, v18, v19\nv_max3_u32 v92, v92, v20, v21\n"
"v_max3_u32 v92, v92, v22, v23\ns_waitcnt vmcnt(12)\nv_max3_u32 v92, v92, v24, v25\nv_max3_u32 v92, v92, v26, v27\nv_max3_u32 v92, v92, v28, v29\n"
"v_max3_u32 v92, v92, v30, v31\nv_max3_u32 v92, v92, v32, v33\nv_max3_u32 v92, v92, v34, v35\nv_max3_u32 v92, v92, v36, v37\n"
"v_max3_u32 v92, v92, v38, v39\nv_max3_u32 v92, v92, v40, v41\nv_max3_u32 v92, v92, v42, v43\nv_max3_u32 v92, v92, v44, v45\n"
"v_max3_u32 v92, v92, v46, v47\ns_waitcnt vmcnt(0)\nv_max3_u32 v92, v92, v48, v49\nv_max3_u32 v92, v92, v50, v51\nv_max3_u32 v92, v92, v52, v53\n"
"v_max3_u32 v92, v92, v54, v55\nv_max3_u32 v92, v92, v56, v57\nv_max3_u32 v92, v92, v58, v59\nv_max3_u32 v92, v92, v60, v61\n"
"v_max3_u32 v92, v92, v62, v63\nv_max3_u32 v92, v92, v64, v65\nv_max3_u32 v92, v92, v66, v67\nv_max3_u32 v92, v92, v68, v69\n"
"v_max3_u32 v92, v92, v70, v71\nv_cmp_eq_u32 vcc, -1, v92\ns_cbranch_vccz L_ook\ns_sub_u32 s87, s87, 1\ns_cmp_lg_u32 s87, 0\ns_cbranch_scc1 L_opoll\n"
"L_ook:\ns_waitcnt lgkmcnt(0)\nv_mfma_f32_16x16x32_bf16 v[72:75], a[0:3], v[0:3], 0\nTR 168,236,2048\nTR 170,236,2560\n"
"v_mfma_f32_16x16x32_bf16 v[76:79], a[4:7], v[0:3], 0\nv_mfma_f32_16x16x32_bf16 v[80:83], a[8:11], v[0:3], 0\n"
"v_mfma_f32_16x16x32_bf16 v[84:87], a[12:15], v[0:3], 0\nv_mfma_f32_16x16x32_bf16 v[88:91], v[160:163], v[0:3], 0\n"
"v_mfma_f32_16x16x32_bf16 v[72:75], a[16:19], v[4:7], v[72:75]\nTR 160,236,3072\nTR 162,236,3584\n"
"v_mfma_f32_16x16x32_bf16 v[76:79], a[20:23], v[4:7], v[76:79]\nv_mfma_f32_16x16x32_bf16 v[80:83], a[24:27], v[4:7], v[80:83]\n"
"v_mfma_f32_16x16x32_bf16 v[84:87], a[28:31], v[4:7], v[84:87]\nv_mfma_f32_16x16x32_bf16 v[88:91], v[164:167], v[4:7], v[88:91]\n"
"v_mfma_f32_16x16x32_bf16 v[72:75], a[32:35], v[8:11], v[72:75]\nTR 164,236,4096\nTR 166,236,4608\n"
"v_mfma_f32_16x16x32_bf16 v[76:79], a[36:39], v[8:11], v[76:79]\nv_mfma_f32_16x16x32_bf16 v[80:83], a[40:43], v[8:11], v[80:83]\n"
"v_mfma_f32_16x16x32_bf16 v[84:87], a[44:47], v[8:11], v[84:87]\ns_waitcnt lgkmcnt(4)\n"
"v_mfma_f32_16x16x32_bf16 v[88:91], v[168:171], v[8:11], v[88:91]\nv_mfma_f32_16x16x32_bf16 v[72:75], a[48:51], v[12:15], v[72:75]\nTR 168,236,5120\n"
"TR 170,236,5632\nv_mfma_f32_16x16x32_bf16 v[76:79], a[52:55], v[12:15], v[76:79]\nv_mfma_f32_16x16x32_bf16 v[80:83], a[56:59], v[12:15], v[80:83]\n"
"v_mfma_f32_16x16x32_bf16 v[84:87], a[60:63], v[12:15], v[84:87]\ns_waitcnt lgkmcnt(4)\n"
"v_mfma_f32_16x16x32_bf16 v[88:91], v[160:163], v[12:15], v[88:91]\nv_mfma_f32_16x16x32_bf16 v[72:75], a[64:67], v[16:19], v[72:75]\nTR 160,236,6144\n"
"TR 162,236,6656\nv_mfma_f32_16x16x32_bf16 v[76:79], a[68:71], v[16:19], v[76:79]\nv_mfma_f32_16x16x32_bf16 v[80:83], a[72:75], v[16:19], v[80:83]\n"
"v_mfma_f32_16x16x32_bf16 v[84:87], a[76:79], v[16:19], v[84:87]\ns_waitcnt lgkmcnt(4)\n"
"v_mfma_f32_16x16x32_bf16 v[88:91], v[164:167], v[16:19], v[88:91]\nv_mfma_f32_16x16x32_bf16 v[72:75], a[80:83], v[20:23], v[72:75]\nTR 164,236,7168\n"
"TR 166,236,7680\nv_mfma_f32_16x16x32_bf16 v[76:79], a[84:87], v[20:23], v[76:79]\nv_mfma_f32_16x16x32_bf16 v[80:83], a[88:91], v[20:23], v[80:83]\n"
"v_mfma_f32_16x16x32_bf16 v[84:87], a[92:95], v[20:23], v[84:87]\ns_waitcnt lgkmcnt(4)\n"
"v_mfma_f32_16x16x32_bf16 v[88:91], v[168:171], v[20:23], v[88:91]\nv_mfma_f32_16x16x32_bf16 v[72:75], a[96:99], v[24:27], v[72:75]\nTR 168,236,8192\n"
"TR 170,236,8704\nv_mfma_f32_16x16x32_bf16 v[76:79], a[100:103], v[24:27], v[76:79]\n"
"v_mfma_f32_16x16x32_bf16 v[80:83], a[104:107], v[24:27], v[80:83]\nv_mfma_f32_16x16x32_bf16 v[84:87], a[108:111], v[24:27], v[84:87]\n"
"s_waitcnt lgkmcnt(4)\nv_mfma_f32_16x16x32_bf16 v[88:91], v[160:163], v[24:27], v[88:91]\n"
"v_mfma_f32_16x16x32_bf16 v[72:75], a[112:115], v[28:31], v[72:75]\nTR 160,236,9216\nTR 162,236,9728\n"
"v_mfma_f32_16x16x32_bf16 v[76:79], a[116:119], v[28:31], v[76:79]\nv_mfma_f32_16x16x32_bf16 v[80:83], a[120:123], v[28:31], v[80:83]\n"
"v_mfma_f32_16x16x32_bf16 v[84:87], a[124:127], v[28:31], v[84:87]\ns_waitcnt lgkmcnt(4)\n"
"v_mfma_f32_16x16x32_bf16 v[88:91], v[164:167], v[28:31], v[88:91]\nv_mfma_f32_16x16x32_bf16 v[72:75], a[128:131], v[32:35], v[72:75]\n"
"TR 164,236,10240\nTR 166,236,10752\nv_mfma_f32_16x16x32_bf16 v[76:79], a[132:135], v[32:35], v[76:79]\n"
"v_mfma_f32_16x16x32_bf16 v[80:83], a[136:139], v[32:35], v[80:83]\nv_mfma_f32_16x16x32_bf16 v[84:87], a[140:143], v[32:35], v[84:87]\n"
"s_waitcnt lgkmcnt(4)\nv_mfma_f32_16x16x32_bf16 v[88:91], v[168:171], v[32:35], v[88:91]\n"
"v_mfma_f32_16x16x32_bf16 v[72:75], a[144:147], v[36:39], v[72:75]\nTR 168,236,11264\nTR 170,236,11776\n"
"v_mfma_f32_16x16x32_bf16 v[76:79], a[148:151], v[36:39], v[76:79]\nv_mfma_f32_16x16x32_bf16 v[80:83], a[152:155], v[36:39], v[80:83]\n"
"v_mfma_f32_16x16x32_bf16 v[84:87], a[156:159], v[36:39], v[84:87]\ns_waitcnt lgkmcnt(4)\n"
"v_mfma_f32_16x16x32_bf16 v[88:91], v[160:163], v[36:39], v[88:91]\nv_mfma_f32_16x16x32_bf16 v[72:75], a[160:163], v[40:43], v[72:75]\n"
"TR 160,236,12288\nTR 162,236,12800\nv_mfma_f32_16x16x32_bf16 v[76:79], a[164:167], v[40:43], v[76:79]\n"
"v_mfma_f32_16x16x32_bf16 v[80:83], a[168:171], v[40:43], v[80:83]\nv_mfma_f32_16x16x32_bf16 v[84:87], a[172:175], v[40:43], v[84:87]\n"
"s_waitcnt lgkmcnt(4)\nv_mfma_f32_16x16x32_bf16 v[88:91], v[164:167], v[40:43], v[88:91]\n"
"v_mfma_f32_16x16x32_bf16 v[72:75], a[176:179], v[44:47], v[72:75]\nTR 164,236,13312\nTR 166,236,13824\n"
"v_mfma_f32_16x16x32_bf16 v[76:79], a[180:183], v[44:47], v[76:79]\nv_mfma_f32_16x16x32_bf16 v[80:83], a[184:187], v[44:47], v[80:83]\n"
"v_mfma_f32_16x16x32_bf16 v[84:87], a[188:191], v[44:47], v[84:87]\ns_waitcnt lgkmcnt(4)\n"
"v_mfma_f32_16x16x32_bf16 v[88:91], v[168:171], v[44:47], v[88:91]\nv_mfma_f32_16x16x32_bf16 v[72:75], a[192:195], v[48:51], v[72:75]\n"
"TR 168,236,14336\nTR 170,236,14848\nv_mfma_f32_16x16x32_bf16 v[76:79], a[196:199], v[48:51], v[76:79]\n"
"v_mfma_f32_16x16x32_bf16 v[80:83], a[200:203], v[48:51], v[80:83]\nv_mfma_f32_16x16x32_bf16 v[84:87], a[204:207], v[48:51], v[84:87]\n"
"s_waitcnt lgkmcnt(4)\nv_mfma_f32_16x16x32_bf16 v[88:91], v[160:163], v[48:51], v[88:91]\n"
"v_mfma_f32_16x16x32_bf16 v[72:75], a[208:211], v[52:55], v[72:75]\nTR 160,236,15360\nTR 162,236,15872\n"
"v_mfma_f32_16x16x32_bf16 v[76:79], a[212:215], v[52:55], v[76:79]\nv_mfma_f32_16x16x32_bf16 v[80:83], a[216:219], v[52:55], v[80:83]\n"
"v_mfma_f32_16x16x32_bf16 v[84:87], a[220:223], v[52:55], v[84:87]\ns_waitcnt lgkmcnt(4)\n"
"v_mfma_f32_16x16x32_bf16 v[88:91], v[164:167], v[52:55], v[88:91]\nv_mfma_f32_16x16x32_bf16 v[72:75], a[224:227], v[56:59], v[72:75]\n"
"TR 164,236,16384\nTR 166,236,16896\nv_mfma_f32_16x16x32_bf16 v[76:79], a[228:231], v[56:59], v[76:79]\n"
"v_mfma_f32_16x16x32_bf16 v[80:83], a[232:235], v[56:59], v[80:83]\nv_mfma_f32_16x16x32_bf16 v[84:87], a[236:239], v[56:59], v[84:87]\n"
"s_waitcnt lgkmcnt(4)\nv_mfma_f32_16x16x32_bf16 v[88:91], v[168:171], v[56:59], v[88:91]\n"
"v_mfma_f32_16x16x32_bf16 v[72:75], a[240:243], v[60:63], v[72:75]\nTR 168,236,17408\nTR 170,236,17920\n"
"v_mfma_f32_16x16x32_bf16 v[76:79], a[244:247], v[60:63], v[76:79]\nv_mfma_f32_16x16x32_bf16 v[80:83], a[248:251], v[60:63], v[80:83]\n"
"v_mfma_f32_16x16x32_bf16 v[84:87], a[252:255], v[60:63], v[84:87]\ns_waitcnt lgkmcnt(4)\n"
"v_mfma_f32_16x16x32_bf16 v[88:91], v[160:163], v[60:63], v[88:91]\nv_mfma_f32_16x16x32_bf16 v[72:75], v[96:99], v[64:67], v[72:75]\n"
"v_mfma_f32_16x16x32_bf16 v[76:79], v[100:103], v[64:67], v[76:79]\nv_mfma_f32_16x16x32_bf16 v[80:83], v[104:107], v[64:67], v[80:83]\n"
"v_mfma_f32_16x16x32_bf16 v[84:87], v[128:131], v[64:67], v[84:87]\ns_waitcnt lgkmcnt(2)\n"
"v_mfma_f32_16x16x32_bf16 v[88:91], v[164:167], v[64:67], v[88:91]\nv_mfma_f32_16x16x32_bf16 v[72:75], v[132:135], v[68:71], v[72:75]\n"
"v_mfma_f32_16x16x32_bf16 v[76:79], v[136:139], v[68:71], v[76:79]\nv_mfma_f32_16x16x32_bf16 v[80:83], v[144:147], v[68:71], v[80:83]\n"
"v_mfma_f32_16x16x32_bf16 v[84:87], v[148:151], v[68:71], v[84:87]\ns_waitcnt lgkmcnt(0)\n"
"v_mfma_f32_16x16x32_bf16 v[88:91], v[168:171], v[68:71], v[88:91]\ns_nop 2\nv_sub_f32 v72, v72, v112\nv_sub_f32 v73, v73, v113\n"
"v_sub_f32 v74, v74, v114\nv_sub_f32 v75, v75, v115\nv_fma_f32 v72, v142, v72, v112\nv_fma_f32 v73, v142, v73, v113\nv_fma_f32 v74, v142, v74, v114\n"
"v_fma_f32 v75, v142, v75, v115\nv_cvt_pk_bf16_f32 v72, v72, v73\nv_cvt_pk_bf16_f32 v73, v74, v75\n"
"buffer_store_dwordx2 v[72:73], v237, s[56:59], 0 offen\nv_sub_f32 v76, v76, v116\nv_sub_f32 v77, v77, v117\nv_sub_f32 v78, v78, v118\n"
"v_sub_f32 v79, v79, v119\nv_fma_f32 v76, v142, v76, v116\nv_fma_f32 v77, v142, v77, v117\nv_fma_f32 v78, v142, v78, v118\n"
"v_fma_f32 v79, v142, v79, v119\nv_cvt_pk_bf16_f32 v76, v76, v77\nv_cvt_pk_bf16_f32 v77, v78, v79\n"
"buffer_store_dwordx2 v[76:77], v237, s[56:59], 0 offen offset:32\nv_sub_f32 v80, v80, v120\nv_sub_f32 v81, v81, v121\nv_sub_f32 v82, v82, v122\n"
"v_sub_f32 v83, v83, v123\nv_fma_f32 v80, v142, v80, v120\nv_fma_f32 v81, v142, v81, v121\nv_fma_f32 v82, v142, v82, v122\n"
"v_fma_f32 v83, v142, v83, v123\nv_cvt_pk_bf16_f32 v80, v80, v81\nv_cvt_pk_bf16_f32 v81, v82, v83\n"
"buffer_store_dwordx2 v[80:81], v237, s[56:59], 0 offen offset:64\nv_sub_f32 v84, v84, v124\nv_sub_f32 v85, v85, v125\nv_sub_f32 v86, v86, v126\n"
"v_sub_f32 v87, v87, v127\nv_fma_f32 v84, v142, v84, v124\nv_fma_f32 v85, v142, v85, v125\nv_fma_f32 v86, v142, v86, v126\n"
"v_fma_f32 v87, v142, v87, v127\nv_cvt_pk_bf16_f32 v84, v84, v85\nv_cvt_pk_bf16_f32 v85, v86, v87\n"
"buffer_store_dwordx2 v[84:85], v237, s[56:59], 0 offen offset:96\nv_sub_f32 v88, v88, v184\nv_sub_f32 v89, v89, v185\nv_sub_f32 v90, v90, v186\n"
"v_sub_f32 v91, v91, v187\nv_fma_f32 v88, v142, v88, v184\nv_fma_f32 v89, v142, v89, v185\nv_fma_f32 v90, v142, v90, v186\n"
"v_fma_f32 v91, v142, v91, v187\nv_cvt_pk_bf16_f32 v88, v88, v89\nv_cvt_pk_bf16_f32 v89, v90, v91\n"
"buffer_store_dwordx2 v[88:89], v238, s[56:59], 0 offen\ns_branch L_end\nL_kv_spin:\ns_mov_b32 s87, 0x4000\nL_kv_loop:\ns_sleep 4\n"
"buffer_load_dwordx4 v[0:3], v243, s[48:51], 0 offen sc1\ns_waitcnt vmcnt(0)\nv_xor_b32 v0, s84, v0\nv_xor_b32 v1, s85, v1\nv_xor_b32 v2, s84, v2\n"
"v_xor_b32 v3, s85, v3\nv_or_b32 v0, v0, v1\nv_or_b32 v0, v0, v2\nv_or_b32 v0, v0, v3\nv_cmp_ne_u32 vcc, 0, v0\ns_cbranch_vccz L_kv_ok\n"
"s_sub_u32 s87, s87, 1\ns_cmp_lg_u32 s87, 0\ns_cbranch_scc1 L_kv_loop\ns_branch L_kv_ok\nL_truemax:\ns_nop 15\nv_mov_b32 v137, 0\n"
"v_mov_b32 v140, 0xff800000\nv_mov_b32 v139, 0\nv_mov_b32 v141, 0xff800000\ns_mov_b32 s24, 0x4000\nL_truemax_loop:\n"
"buffer_load_dwordx4 v[48:51], v220, s[48:51], s24 offen sc1\nbuffer_load_dwordx2 v[52:53], v221, s[48:51], s24 offen sc1\ns_waitcnt vmcnt(0)\n"
"v_mfma_f32_32x32x16_bf16 v[0:15], v[48:51], v[128:131], 0\nv_mfma_f32_32x32x8bf16_1k v[0:15], v[52:53], v[136:137], v[0:15]\n"
"v_mfma_f32_32x32x16_bf16 v[16:31], v[48:51], v[132:135], 0\nv_mfma_f32_32x32x8bf16_1k v[16:31], v[52:53], v[138:139], v[16:31]\ns_nop 15\n"
"v_max3_f32 v140, v140, v0, v1\nv_max3_f32 v140, v140, v2, v3\nv_max3_f32 v140, v140, v4, v5\nv_max3_f32 v140, v140, v6, v7\n"
"v_max3_f32 v140, v140, v8, v9\nv_max3_f32 v140, v140, v10, v11\nv_max3_f32 v140, v140, v12, v13\nv_max3_f32 v140, v140, v14, v15\n"
"v_max3_f32 v141, v141, v16, v17\nv_max3_f32 v141, v141, v18, v19\nv_max3_f32 v141, v141, v20, v21\nv_max3_f32 v141, v141, v22, v23\n"
"v_max3_f32 v141, v141, v24, v25\nv_max3_f32 v141, v141, v26, v27\nv_max3_f32 v141, v141, v28, v29\nv_max3_f32 v141, v141, v30, v31\n"
"s_add_u32 s24, s24, 0xa00\ns_cmp_lt_u32 s24, 0x2c000\ns_cbranch_scc1 L_truemax_loop\nv_mov_b32 v0, v140\ns_nop 1\nv_permlane32_swap_b32 v140, v0\n"
"v_max_f32 v140, v140, v0\nv_mov_b32 v1, v141\ns_nop 1\nv_permlane32_swap_b32 v141, v1\nv_max_f32 v141, v141, v1\ns_nop 3\ns_branch L_truemax_ret\n"
"L_pfl_spin:\ns_mov_b32 s87, 0x4000\nL_pfl_loop:\ns_sleep 4\nbuffer_load_dwordx4 v[64:67], v244, s[48:51], 0 offen sc1\ns_waitcnt vmcnt(0)\n"
"v_xor_b32 v64, s84, v64\nv_xor_b32 v65, s85, v65\nv_xor_b32 v66, s84, v66\nv_xor_b32 v67, s85, v67\nv_or_b32 v64, v64, v65\nv_or_b32 v64, v64, v66\n"
"v_or_b32 v64, v64, v67\nv_cmp_ne_u32 vcc, 0, v64\ns_cbranch_vccz L_pfl_ok\ns_sub_u32 s87, s87, 1\ns_cmp_lg_u32 s87, 0\ns_cbranch_scc1 L_pfl_loop\n"
"s_branch L_pfl_ok\nL_arrv_spin:\ns_mov_b32 s87, 0x4000\nL_arrv_loop:\ns_sleep 4\nbuffer_load_dwordx4 v[68:71], v251, s[48:51], 0 offen sc1\n"
"buffer_load_dwordx4 v[72:75], v251, s[48:51], 0 offen offset:16 sc1\ns_waitcnt vmcnt(0)\nv_xor_b32 v68, s84, v68\nv_xor_b32 v69, s85, v69\n"
"v_xor_b32 v70, s84, v70\nv_xor_b32 v71, s85, v71\nv_xor_b32 v72, s84, v72\nv_xor_b32 v73, s85, v73\nv_xor_b32 v74, s84, v74\nv_xor_b32 v75, s85, v75\n"
"v_or_b32 v68, v68, v69\nv_or_b32 v68, v68, v70\nv_or_b32 v68, v68, v71\nv_or_b32 v68, v68, v72\nv_or_b32 v68, v68, v73\nv_or_b32 v68, v68, v74\n"
"v_or_b32 v68, v68, v75\nv_cmp_ne_u32 vcc, 0, v68\ns_cbranch_vccz L_arrv_ok\ns_sub_u32 s87, s87, 1\ns_cmp_lg_u32 s87, 0\ns_cbranch_scc1 L_arrv_loop\n"
"s_branch L_arrv_ok\nL_end:\n"
    : "+{s[68:83]}"(s2)
    : "{s[36:51]}"(s0), "{s[52:67]}"(s1)
    : "v0","v1","v2","v3","v4","v5","v6","v7","v8","v9","v10","v11","v12","v13","v14","v15","v16","v17","v18","v19","v20","v21","v22","v23","v24","v25","v26","v27","v28","v29","v30","v31","v32","v33","v34","v35","v36","v37","v38","v39","v40","v41","v42","v43","v44","v45","v46","v47","v48","v49","v50","v51","v52","v53","v54","v55","v56","v57","v58","v59","v60","v61","v62","v63","v64","v65","v66","v67","v68","v69","v70","v71","v72","v73","v74","v75","v76","v77","v78","v79","v80","v81","v82","v83","v84","v85","v86","v87","v88","v89","v90","v91","v92","v93","v94","v95","v96","v97","v98","v99","v100","v101","v102","v103","v104","v105","v106","v107","v108","v109","v110","v111","v112","v113","v114","v115","v116","v117","v118","v119","v120","v121","v122","v123","v124","v125","v126","v127","v128","v129","v130","v131","v132","v133","v134","v135","v136","v137","v138","v139","v140","v141","v142","v143","v144","v145","v146","v147","v148","v149","v150","v151","v152","v153","v154","v155","v156","v157","v158","v159","v160","v161","v162","v163","v164","v165","v166","v167","v168","v169","v170","v171","v172","v173","v174","v175","v176","v177","v178","v179","v180","v181","v182","v183","v184","v185","v186","v187","v188","v189","v190","v191","v192","v193","v194","v195","v196","v197","v198","v199","v200","v201","v202","v203","v204","v205","v206","v207","v208","v209","v210","v211","v212","v213","v214","v215","v216","v217","v218","v219","v220","v221","v222","v223","v224","v225","v226","v227","v228","v229","v230","v231","v232","v233","v234","v235","v236","v237","v238","v239","v240","v241","v242","v243","v244","v245","v246","v247","v248","v249","v250","v251","v252","v253","v254","v255","a0","a1","a2","a3","a4","a5","a6","a7","a8","a9","a10","a11","a12","a13","a14","a15","a16","a17","a18","a19","a20","a21","a22","a23","a24","a25","a26","a27","a28","a29","a30","a31","a32","a33","a34","a35","a36","a37","a38","a39","a40","a41","a42","a43","a44","a45","a46","a47","a48","a49","a50","a51","a52","a53","a54","a55","a56","a57","a58","a59","a60","a61","a62","a63","a64","a65","a66","a67","a68","a69","a70","a71","a72","a73","a74","a75","a76","a77","a78","a79","a80","a81","a82","a83","a84","a85","a86","a87","a88","a89","a90","a91","a92","a93","a94","a95","a96","a97","a98","a99","a100","a101","a102","a103","a104","a105","a106","a107","a108","a109","a110","a111","a112","a113","a114","a115","a116","a117","a118","a119","a120","a121","a122","a123","a124","a125","a126","a127","a128","a129","a130","a131","a132","a133","a134","a135","a136","a137","a138","a139","a140","a141","a142","a143","a144","a145","a146","a147","a148","a149","a150","a151","a152","a153","a154","a155","a156","a157","a158","a159","a160","a161","a162","a163","a164","a165","a166","a167","a168","a169","a170","a171","a172","a173","a174","a175","a176","a177","a178","a179","a180","a181","a182","a183","a184","a185","a186","a187","a188","a189","a190","a191","a192","a193","a194","a195","a196","a197","a198","a199","a200","a201","a202","a203","a204","a205","a206","a207","a208","a209","a210","a211","a212","a213","a214","a215","a216","a217","a218","a219","a220","a221","a222","a223","a224","a225","a226","a227","a228","a229","a230","a231","a232","a233","a234","a235","a236","a237","a238","a239","a240","a241","a242","a243","a244","a245","a246","a247","a248","a249","a250","a251","a252","a253","a254","a255","s0","s1","s2","s3","s4","s5","s6","s7","s8","s9","s10","s11","s12","s13","s14","s15","s16","s17","s18","s19","s20","s21","s22","s23","s24","s25","s26","s27","s28","s29","s30","s31","s34","s35","s84","s85","s86","s87","s88","s89","s90","s91","s92","s93","s94","s95","s96","s97","s98","s99", "vcc", "scc", "memory");
}

extern "C" void kernel_launch(void* const* d_in, const int* in_sizes, int n_in, void* d_out, int out_size, void* d_ws, size_t ws_size,
                              hipStream_t stream) {
  fused_mix<<<256, 256, 0, stream>>>((const char*)d_in[0], (const char*)d_in[1], (const char*)d_in[2], (const char*)d_in[3],
                                     (const char*)d_in[4], (const char*)d_in[6], (const char*)d_in[7], (const char*)d_in[8],
                                     (const char*)d_in[9], (char*)d_out, (char*)d_ws);
}
```
